```python
import torch

def _softmax(x, axis=-1):
    return torch.softmax(x, dim=axis)

def _gelu(x):
    return torch.nn.functional.gelu(x, approximate='tanh')

def _silu(x):
    return torch.nn.functional.silu(x)

def _sigmoid(x):
    return torch.sigmoid(x)

def _layernorm(x, axis=-1, eps=1e-05):
    m = x.mean(dim=axis, keepdim=True)
    v = x.var(dim=axis, keepdim=True, unbiased=False)
    return (x - m) / torch.sqrt(v + eps)

def _scatter_add(tgt, idx, src, axis=0):
    return tgt.clone().index_add_(axis, idx, src)

def _segment_sum(src, seg, n):
    out = torch.zeros((n,) + src.shape[1:], dtype=src.dtype)
    return out.index_add_(0, seg, src)

def _pad(x, pw):
    flat = tuple((v for pair in reversed(pw) for v in pair))
    return torch.nn.functional.pad(x, flat)
kernel_name = 'parallel_sum__linear_attn_block__mixer_gmlp__prompt'

def setup_inputs():

    _w = lambda a: (lambda t: t.to(torch.bfloat16) if t.is_floating_point() else t)(torch.as_tensor(a))

    def _A_setup():
        B, S, D = (1, 2048, 576)
        return {'x': _w((lambda *s: torch.randn(*s, dtype=torch.bfloat16) * 0.05)(B, S, D)), 'wqkv': _w((lambda *s: torch.randn(*s, dtype=torch.bfloat16) * 1.6)(D, 3 * D)), 'wo': _w((lambda *s: torch.randn(*s, dtype=torch.bfloat16) * 0.2)(D, D))}
    _A_in = _A_setup()

    def _B_setup():
        B, S, D, F = (1, 2048, 576, 4608)
        Fh = F // 2
        return {'x': _w((lambda *s: torch.randn(*s, dtype=torch.bfloat16) * 0.05)(B, S, D)), 'w_in': _w((lambda *s: torch.randn(*s, dtype=torch.bfloat16) * 0.4)(D, 2 * Fh)), 'w_sp': _w(0.01 * (lambda *s: torch.randn(*s, dtype=torch.bfloat16) * 0.4)(S, S)), 'b_sp': _w(1.0 + 0.01 * (lambda *s: torch.randn(*s, dtype=torch.bfloat16) * 0.05)(S)), 'w_out': _w((lambda *s: torch.randn(*s, dtype=torch.bfloat16) * 0.05)(Fh, D))}
    _B_in = _B_setup()
    return {'A_x': _A_in['x'], 'A_wqkv': _A_in['wqkv'], 'A_wo': _A_in['wo'], 'B_x': _B_in['x'], 'B_w_in': _B_in['w_in'], 'B_w_sp': _B_in['w_sp'], 'B_b_sp': _B_in['b_sp'], 'B_w_out': _B_in['w_out']}

def reference(A_x, A_wqkv, A_wo, B_x, B_w_in, B_w_sp, B_b_sp, B_w_out):

    def _A_ref(x, wqkv, wo):
        B, S, D = x.shape
        H = 32
        Dh = D // H
        qkv = (lambda x, *d: x.permute(*d))((x @ wqkv).reshape(B, S, 3, H, Dh), 2, 0, 3, 1, 4)
        q, k, v = (qkv[0], qkv[1], qkv[2])
        phi = lambda t: torch.where(t > 0, t + 1, torch.exp(t)) + 1e-06
        pq, pk = (phi(q), phi(k))
        kv = torch.einsum('bhsd,bhse->bhde', pk, v)
        z = torch.einsum('bhsd->bhd', pk)
        num = torch.einsum('bhqd,bhde->bhqe', pq, kv)
        den = torch.einsum('bhqd,bhd->bhq', pq, z)[..., None] + 1e-06
        o = num / den
        return (lambda x, *d: x.permute(*d))(o, 0, 2, 1, 3).reshape(B, S, D) @ wo

    def _B_ref(x, w_in, w_sp, b_sp, w_out):
        B, S, D = x.shape
        F = w_in.shape[-1]
        Fh = F // 2
        z = _gelu(torch.einsum('bsd,df->bsf', x, w_in))
        z1 = z[..., :Fh].clone()
        z2 = _layernorm(z[..., Fh:], axis=-1)
        g = torch.einsum('ts,bsf->btf', w_sp, z2) + b_sp[None, :, None]
        return torch.einsum('bsf,fd->bsd', z1 * g, w_out)
    x = A_x
    xn = _layernorm(x, axis=-1)
    return x + _A_ref(xn, A_wqkv, A_wo) + _B_ref(xn, B_w_in, B_w_sp, B_b_sp, B_w_out)
```

```cpp
#include <hip/hip_runtime.h>
#include <hip/hip_bf16.h>
typedef unsigned short u16;
__device__ __forceinline__ float bf2f(u16 h) { return __uint_as_float((unsigned)h << 16); }
__device__ __forceinline__ u16 f2bf(float f) { __bf16 b = (__bf16)f; return __builtin_bit_cast(u16, b); }
__device__ __forceinline__ float rbf(float f) { return bf2f(f2bf(f)); }

__global__ void k_ln(const u16* in, int ld, int c0, int n, u16* out) {
  int r = blockIdx.x; __shared__ float red[256];
  float s = 0; for (int i = threadIdx.x; i < n; i += 256) s += bf2f(in[(size_t)r * ld + c0 + i]);
  red[threadIdx.x] = s; __syncthreads();
  for (int o = 128; o; o >>= 1) { if (threadIdx.x < o) red[threadIdx.x] += red[threadIdx.x + o]; __syncthreads(); }
  float m = red[0] / n; __syncthreads();
  s = 0; for (int i = threadIdx.x; i < n; i += 256) { float d = bf2f(in[(size_t)r * ld + c0 + i]) - m; s += d * d; }
  red[threadIdx.x] = s; __syncthreads();
  for (int o = 128; o; o >>= 1) { if (threadIdx.x < o) red[threadIdx.x] += red[threadIdx.x + o]; __syncthreads(); }
  float rs = rsqrtf(red[0] / n + 1e-5f);
  for (int i = threadIdx.x; i < n; i += 256) out[(size_t)r * n + i] = f2bf((bf2f(in[(size_t)r * ld + c0 + i]) - m) * rs);
}
__global__ void k_gemm(const u16* A, int lda, const u16* B, int ldb, float* C, int ldc, int K) {
  __shared__ float As[16][65], Bs[16][65];
  int tx = threadIdx.x & 15, ty = threadIdx.x >> 4, m0 = blockIdx.y * 64, n0 = blockIdx.x * 64;
  float acc[4][4] = {};
  for (int k0 = 0; k0 < K; k0 += 16) {
    for (int i = threadIdx.x; i < 1024; i += 256) { int m = i >> 4, k = i & 15; As[k][m] = bf2f(A[(size_t)(m0 + m) * lda + k0 + k]); }
    for (int i = threadIdx.x; i < 1024; i += 256) { int k = i >> 6, n = i & 63; Bs[k][n] = bf2f(B[(size_t)(k0 + k) * ldb + n0 + n]); }
    __syncthreads();
    for (int k = 0; k < 16; ++k) for (int i = 0; i < 4; ++i) for (int j = 0; j < 4; ++j) acc[i][j] += As[k][ty * 4 + i] * Bs[k][tx * 4 + j];
    __syncthreads();
  }
  for (int i = 0; i < 4; ++i) for (int j = 0; j < 4; ++j) C[(size_t)(m0 + ty * 4 + i) * ldc + n0 + tx * 4 + j] = acc[i][j];
}
__global__ void k_gelu(const float* in, u16* out, size_t n) { size_t i = blockIdx.x * 256ull + threadIdx.x; if (i < n) { float x = rbf(in[i]); float u = 0.7978845608f * (x + 0.044715f * x * x * x); out[i] = f2bf(0.5f * x * (1.f + tanhf(u))); } }
__global__ void k_phi(const float* in, u16* out, size_t n) { size_t i = blockIdx.x * 256ull + threadIdx.x; if (i < n) { int c = i % 1728; float x = rbf(in[i]); if (c < 1152) x = (x > 0 ? x + 1.f : __expf(x)) + 1e-6f; out[i] = f2bf(x); } }
__global__ void k_kv(const u16* qkv, float* kv) {
  int h = blockIdx.x, d = threadIdx.x / 19, e = threadIdx.x % 19; if (d >= 18) return;
  float s = 0; for (int t = 0; t < 2048; ++t) s += bf2f(qkv[(size_t)t * 1728 + 576 + h * 18 + d]) * (e < 18 ? bf2f(qkv[(size_t)t * 1728 + 1152 + h * 18 + e]) : 1.f);
  kv[(h * 18 + d) * 19 + e] = rbf(s);
}
__global__ void k_o(const u16* qkv, const float* kv, u16* hx) {
  size_t i = blockIdx.x * 256ull + threadIdx.x; int t = i / 576, c = i % 576, h = c / 18, e = c % 18;
  float num = 0, den = 0; for (int d = 0; d < 18; ++d) { float q = bf2f(qkv[(size_t)t * 1728 + h * 18 + d]); num += q * kv[(h * 18 + d) * 19 + e]; den += q * kv[(h * 18 + d) * 19 + 18]; }
  hx[(size_t)t * 2880 + c] = f2bf(num / (den + 1e-6f));
}
__global__ void k_h(const float* g, const u16* z, const u16* b, u16* hx) { size_t i = blockIdx.x * 256ull + threadIdx.x; int t = i / 2304, f = i % 2304;
  hx[(size_t)t * 2880 + 576 + f] = f2bf(bf2f(z[(size_t)t * 4608 + f]) * (g[i] + bf2f(b[t]))); }
__global__ void k_out(const float* c1, const float* c2, const u16* x, u16* out) { size_t i = blockIdx.x * 256ull + threadIdx.x; out[i] = f2bf(bf2f(x[i]) + c1[i] + c2[i]); }

extern "C" void kernel_launch(void* const* d_in, const int* in_sizes, int n_in, void* d_out, int out_size, void* d_ws, size_t ws_size, hipStream_t stream) {
  const u16 *x = (const u16*)d_in[0], *wqkv = (const u16*)d_in[1], *wo = (const u16*)d_in[2], *win = (const u16*)d_in[4], *wsp = (const u16*)d_in[5], *bsp = (const u16*)d_in[6], *wout = (const u16*)d_in[7];
  char* ws = (char*)d_ws;
  u16* xn = (u16*)ws; float* tmp = (float*)(ws + (4 << 20)); u16* qkv = (u16*)(ws + (48 << 20)); u16* z = (u16*)(ws + (56 << 20)); u16* z2n = (u16*)(ws + (80 << 20));
  float* kv = (float*)(ws + (92 << 20)); u16* hx = (u16*)(ws + (96 << 20)); float* c1 = (float*)(ws + (112 << 20)); float* c2 = (float*)(ws + (120 << 20));
  k_ln<<<2048, 256, 0, stream>>>(x, 576, 0, 576, xn);
  k_gemm<<<dim3(27, 32), 256, 0, stream>>>(xn, 576, wqkv, 1728, tmp, 1728, 576);
  k_phi<<<2048 * 1728 / 256, 256, 0, stream>>>(tmp, qkv, 2048 * 1728);
  k_gemm<<<dim3(72, 32), 256, 0, stream>>>(xn, 576, win, 4608, tmp, 4608, 576);
  k_gelu<<<2048 * 4608 / 256, 256, 0, stream>>>(tmp, z, 2048 * 4608);
  k_ln<<<2048, 256, 0, stream>>>(z, 4608, 2304, 2304, z2n);
  k_kv<<<32, 384, 0, stream>>>(qkv, kv);
  k_o<<<2048 * 576 / 256, 256, 0, stream>>>(qkv, kv, hx);
  k_gemm<<<dim3(36, 32), 256, 0, stream>>>(wsp, 2048, z2n, 2304, tmp, 2304, 2048);
  k_h<<<2048 * 2304 / 256, 256, 0, stream>>>(tmp, z, bsp, hx);
  k_gemm<<<dim3(9, 32), 256, 0, stream>>>(hx, 2880, wo, 576, c1, 576, 576);
  k_gemm<<<dim3(9, 32), 256, 0, stream>>>(hx + 576, 2880, wout, 576, c2, 576, 2304);
  k_out<<<2048 * 576 / 256, 256, 0, stream>>>(c1, c2, x, (u16*)d_out);
}
```

```cpp
#include <hip/hip_runtime.h>
#include <hip/hip_bf16.h>

#define LAS __attribute__((address_space(3)))
#define GAS __attribute__((address_space(1)))
typedef unsigned short u16;
typedef unsigned long long u64;
typedef short bf16x8 __attribute__((ext_vector_type(8)));
typedef short s16x4 __attribute__((ext_vector_type(4)));
typedef float f32x4 __attribute__((ext_vector_type(4)));
typedef float f32x2 __attribute__((ext_vector_type(2)));
typedef unsigned u32x4 __attribute__((ext_vector_type(4)));
typedef unsigned u32x2 __attribute__((ext_vector_type(2)));
typedef __bf16 bf16x2_t __attribute__((ext_vector_type(2)));

#define WAIT_V(n) asm volatile("s_waitcnt vmcnt(" #n ")" ::: "memory")
#define WAIT_L0() asm volatile("s_waitcnt lgkmcnt(0)" ::: "memory")
#define BAR() do { asm volatile("" ::: "memory"); __builtin_amdgcn_s_barrier(); asm volatile("" ::: "memory"); } while (0)
#define SB0() __builtin_amdgcn_sched_barrier(0)
#define SGB(mask, n) __builtin_amdgcn_sched_group_barrier(mask, n, 0)
#define MFMA(a, b, c) __builtin_amdgcn_mfma_f32_16x16x32_bf16(a, b, c, 0, 0, 0)
typedef u32x4 rsrc_t;
__device__ __forceinline__ rsrc_t MKRS(const void* p, unsigned bytes) {
  const u64 a = (u64)p;
  return (rsrc_t){(unsigned)a, (unsigned)(a >> 32) & 0xffffu, bytes, 0x00020000u};
}
__device__ __forceinline__ void BLDS(rsrc_t rs, unsigned voff, unsigned soff, LAS char* l) {
  asm volatile("s_mov_b32 m0, %3\n\ts_nop 0\n\tbuffer_load_dwordx4 %0, %1, %2 offen lds"
               :: "v"(voff), "s"(rs), "s"(soff), "s"((unsigned)(size_t)l) : "memory");
}

template <int V> struct IC { static constexpr int value = V; };

constexpr size_t WS_EPOCH = 0;
constexpr size_t WS_STAT = 4096;
constexpr size_t WS_KVP = WS_STAT + 16 * 12 * 256 * 8;
constexpr size_t WS_PQ = WS_KVP + 16 * 16 * 2816;
constexpr size_t WS_WX = WS_PQ + 128 * 16 * 1152;
constexpr size_t WS_Z2N = WS_WX + 8 * 90 * 4608;
constexpr size_t WS_HX = WS_Z2N + 16 * 64 * 9 * 1024;

struct Params {
  const u16* x; const u16* wqkv; const u16* wo; const u16* win; const u16* wsp; const u16* bsp; const u16* wout;
  u16* out; char* ws;
};

__device__ __forceinline__ unsigned cvt_pk(float lo, float hi) {
  f32x2 v = {lo, hi}; bf16x2_t b = __builtin_convertvector(v, bf16x2_t); return __builtin_bit_cast(unsigned, b);
}
__device__ __forceinline__ float bf_lo(unsigned v) { return __uint_as_float(v << 16); }
__device__ __forceinline__ float bf_hi(unsigned v) { return __uint_as_float(v & 0xffff0000u); }
__device__ __forceinline__ float gelu_t(float x) {
  const float k0 = -2.302208198f;
  const float k1 = -0.1029432397f;
  const float w = __builtin_fmaf(x * x, k1, k0);
  const float e = __builtin_amdgcn_exp2f(x * w);
  return x * __builtin_amdgcn_rcpf(1.0f + e);
}
__device__ __forceinline__ float phi_f(float t) {
  const float e = __builtin_amdgcn_exp2f(t * 1.4426950409f);
  return (t > 0.f ? t + 1.0f : e) + 1e-6f;
}
__device__ __forceinline__ bf16x8 tr_frag(LAS char* a0, LAS char* a1) {
  const s16x4 lo = __builtin_amdgcn_ds_read_tr16_b64_v4i16((LAS s16x4*)a0);
  const s16x4 hi = __builtin_amdgcn_ds_read_tr16_b64_v4i16((LAS s16x4*)a1);
  return __builtin_shufflevector(lo, hi, 0, 1, 2, 3, 4, 5, 6, 7);
}
template <int CTRL> __device__ __forceinline__ float dpp_add(float v) {
  return v + __builtin_bit_cast(float, __builtin_amdgcn_update_dpp(0, __builtin_bit_cast(int, v), CTRL, 0xf, 0xf, true));
}
__device__ __forceinline__ float row16_sum(float v) {
  v = dpp_add<0x128>(v); v = dpp_add<0x124>(v); v = dpp_add<0x122>(v); v = dpp_add<0x121>(v); return v;
}
__device__ __forceinline__ int slot_row(int rs) { return (rs & ~31) + 8 * ((rs & 15) >> 2) + 4 * ((rs >> 4) & 1) + (rs & 3); }

constexpr int A_LDS = 160 * 1024;
constexpr int AZ_SLOT = 24 * 1024, AZ_PARK = 96 * 1024, AZ_ST = 144 * 1024, AZ_ST2 = 146 * 1024;
constexpr int AQ_SLOT = 18 * 1024, AQ_PK = 72 * 1024, AQ_V = 108 * 1024;

template <int G>
__device__ __forceinline__ void a_zblock(const Params& P, LAS char* smem, bf16x8 (&xf)[2][18], const unsigned (&voff)[3],
                                         int p, int j, int wid, int lane, int tid, unsigned tag) {
  const int rw = wid & 3, kq = lane >> 4;
  const int fr = ((lane >> 3) & 1) | ((kq & 1) << 1);
  const int rb = (8 * kq + ((lane >> 2) & 3)) * 128 + (lane & 3) * 8;
  const int rd0 = rb + (((2 * G) ^ fr) * 32), rd1 = rb + (((2 * G + 1) ^ fr) * 32);
  const rsrc_t rsw = MKRS(P.win, 576 * 9216);
  f32x4 acc[2][2], prev[2][2];
  float ssum[8], ssq[8];
#pragma unroll
  for (int i = 0; i < 8; ++i) { ssum[i] = 0.f; ssq[i] = 0.f; }
#pragma unroll
  for (int t = 0; t < 2; ++t) { prev[t][0] = (f32x4){0, 0, 0, 0}; prev[t][1] = (f32x4){0, 0, 0, 0}; }
  char* hx = P.ws + WS_HX + (size_t)(p * 8 + rw * 2) * (90 * 1024) + lane * 16;

  auto epi_z2 = [&](int t, int rr) {
    float y[8];
#pragma unroll
    for (int u = 0; u < 2; ++u)
#pragma unroll
      for (int e = 0; e < 4; ++e) { const float v = gelu_t(prev[t][u][e]); y[u * 4 + e] = v; ssum[u * 4 + e] += v; ssq[u * 4 + e] = __builtin_fmaf(v, v, ssq[u * 4 + e]); }
    u32x4 w; w.x = cvt_pk(y[0], y[1]); w.y = cvt_pk(y[2], y[3]); w.z = cvt_pk(y[4], y[5]); w.w = cvt_pk(y[6], y[7]);
    *(LAS u32x4*)(smem + AZ_PARK + ((rr * 2 + t) * 8 + wid) * 1024 + lane * 16) = w;
  };
  auto epi_z1 = [&](int u, int rr) {
    float y[8];
#pragma unroll
    for (int t = 0; t < 2; ++t)
#pragma unroll
      for (int e = 0; e < 4; ++e) y[t * 4 + e] = gelu_t(prev[t][u][e]);
    u32x4 w; w.x = cvt_pk(y[0], y[1]); w.y = cvt_pk(y[2], y[3]); w.z = cvt_pk(y[4], y[5]); w.w = cvt_pk(y[6], y[7]);
    *(u32x4*)(hx + u * (90 * 1024) + ((3 * j + rr) * 2 + G) * 1024) = w;
  };

  auto round = [&](auto ORI_, int r) {
    constexpr int ORI = decltype(ORI_)::value;
#pragma unroll
    for (int part = 0; part < 3; ++part) {
      const int s = r * 3 + part;
      if (ORI == 0 || part == 0 || r < 5) { WAIT_V(6); } else if (part == 1) { WAIT_V(5); } else { WAIT_V(2); }
      WAIT_L0(); BAR();
      if (ORI == 0 || r < 5) {
        const int r1 = r + 1;
        const int col = r1 < 3 ? 2304 + (3 * j + r1) * 64 : (3 * j + r1 - 3) * 64;
        LAS char* dst = smem + ((s + 3) & 3) * AZ_SLOT + wid * 3072;
#pragma unroll
        for (int i = 0; i < 3; ++i) BLDS(rsw, voff[i], part * 192 * 9216 + col * 2, dst + i * 1024);
      }
      auto mma = [&]() {
        LAS char* b0 = smem + (s & 3) * AZ_SLOT + rd0;
        LAS char* b1 = smem + (s & 3) * AZ_SLOT + rd1;
#pragma unroll
        for (int k6 = 0; k6 < 6; ++k6) {
          const int kb = part * 6 + k6;
          const bf16x8 f0 = tr_frag(b0 + k6 * 4096, b0 + k6 * 4096 + 512), f1 = tr_frag(b1 + k6 * 4096, b1 + k6 * 4096 + 512);
          const f32x4 z = {0, 0, 0, 0};
          if (ORI == 0) {
            acc[0][0] = MFMA(xf[0][kb], f0, kb ? acc[0][0] : z); acc[0][1] = MFMA(xf[1][kb], f0, kb ? acc[0][1] : z);
            acc[1][1] = MFMA(xf[1][kb], f1, kb ? acc[1][1] : z); acc[1][0] = MFMA(xf[0][kb], f1, kb ? acc[1][0] : z);
          } else {
            acc[0][0] = MFMA(f0, xf[0][kb], kb ? acc[0][0] : z); acc[0][1] = MFMA(f0, xf[1][kb], kb ? acc[0][1] : z);
            acc[1][1] = MFMA(f1, xf[1][kb], kb ? acc[1][1] : z); acc[1][0] = MFMA(f1, xf[0][kb], kb ? acc[1][0] : z);
          }
        }
        SGB(0x100, 8);
#pragma unroll
        for (int i = 0; i < 16; ++i) { SGB(0x8, 1); SGB(0x100, 1); }
        SGB(0x8, 8);
      };
      auto epi = [&]() {
        if (part < 2) {
          if (ORI == 0) { if (r >= 1) epi_z2(part, r - 1); }
          else if (r == 3) epi_z2(part, 2);
          else epi_z1(part, r - 4);
          if (ORI == 1 && part == 0 && G == 0 && r == 4) {
            const float v = *(LAS float*)(smem + AZ_ST + tid * 4) + *(LAS float*)(smem + AZ_ST + 1024 + tid * 4);
            GAS u64* g = (GAS u64*)(P.ws + WS_STAT) + (size_t)(p * 12 + j) * 256 + tid;
            __hip_atomic_store(g, ((u64)tag << 32) | __float_as_uint(v), __ATOMIC_RELAXED, __HIP_MEMORY_SCOPE_AGENT);
          }
        } else if (ORI == 1 && r == 3) {
#pragma unroll
          for (int i = 0; i < 8; ++i) { ssum[i] = row16_sum(ssum[i]); ssq[i] = row16_sum(ssq[i]); }
          if ((lane & 15) == 0) {
            LAS f32x4* d = (LAS f32x4*)(smem + AZ_ST + G * 1024 + (rw * 32 + kq * 8) * 8);
#pragma unroll
            for (int i = 0; i < 4; ++i) d[i] = (f32x4){ssum[2 * i], ssq[2 * i], ssum[2 * i + 1], ssq[2 * i + 1]};
          }
        }
      };
      if (G == 0) { mma(); SB0(); epi(); } else { epi(); SB0(); mma(); }
      SB0();
    }
#pragma unroll
    for (int t = 0; t < 2; ++t) { prev[t][0] = acc[t][0]; prev[t][1] = acc[t][1]; }
  };
#pragma unroll 1
  for (int r = 0; r < 3; ++r) round(IC<0>{}, r);
#pragma unroll 1
  for (int r = 3; r < 6; ++r) round(IC<1>{}, r);
  GAS u64* g = (GAS u64*)(P.ws + WS_STAT) + (size_t)(p * 12 + 6 * G) * 256 + (tid & 255);
  u64 gv[6];
#pragma unroll
  for (int i = 0; i < 6; ++i) gv[i] = __hip_atomic_load(g + i * 256, __ATOMIC_RELAXED, __HIP_MEMORY_SCOPE_AGENT);
  epi_z1(0, 2); epi_z1(1, 2);
  for (unsigned spins = 0;; ++spins) {
    bool ok = true;
#pragma unroll
    for (int i = 0; i < 6; ++i) ok &= (unsigned)(gv[i] >> 32) == tag;
    if (__all(ok) || spins > 400000u) break;
    __builtin_amdgcn_s_sleep(1);
#pragma unroll
    for (int i = 0; i < 6; ++i) gv[i] = __hip_atomic_load(g + i * 256, __ATOMIC_RELAXED, __HIP_MEMORY_SCOPE_AGENT);
  }
  {
    float sum = 0.f;
#pragma unroll
    for (int i = 0; i < 6; ++i) sum += __uint_as_float((unsigned)gv[i]);
    *(LAS float*)(smem + AZ_ST2 + tid * 4) = sum;
  }
  WAIT_L0(); BAR();
  if (j == 0 && tid == 0)
    __hip_atomic_store((GAS unsigned*)(P.ws + WS_EPOCH) + p * 32, tag, __ATOMIC_RELAXED, __HIP_MEMORY_SCOPE_AGENT);
  float sc[8], sh[8];
  {
    LAS f32x4* a = (LAS f32x4*)(smem + AZ_ST2 + (rw * 32 + kq * 8) * 8);
#pragma unroll
    for (int i = 0; i < 4; ++i) {
      const f32x4 v = a[i] + a[i + 64];
      const float m0 = v[0] * (1.0f / 2304.0f), m1 = v[2] * (1.0f / 2304.0f);
      const float v0 = fmaxf(v[1] * (1.0f / 2304.0f) - m0 * m0, 0.f), v1 = fmaxf(v[3] * (1.0f / 2304.0f) - m1 * m1, 0.f);
      sc[2 * i] = __builtin_amdgcn_rsqf(v0 + 1e-5f); sc[2 * i + 1] = __builtin_amdgcn_rsqf(v1 + 1e-5f);
      sh[2 * i] = -m0 * sc[2 * i]; sh[2 * i + 1] = -m1 * sc[2 * i + 1];
    }
  }
#pragma unroll
  for (int q = 0; q < 6; ++q) {
    const u32x4 v = *(LAS u32x4*)(smem + AZ_PARK + (q * 8 + wid) * 1024 + lane * 16);
    u32x4 w;
#pragma unroll
    for (int i = 0; i < 4; ++i)
      w[i] = cvt_pk(__builtin_fmaf(bf_lo(v[i]), sc[2 * i], sh[2 * i]), __builtin_fmaf(bf_hi(v[i]), sc[2 * i + 1], sh[2 * i + 1]));
    const int tg = (3 * j + (q >> 1)) * 4 + 2 * G + (q & 1);
    const int c = tg < 128 ? tg >> 3 : tg - 128, ft = tg < 128 ? tg & 7 : 8;
    *(u32x4*)(P.ws + WS_Z2N + (size_t)((c * 64 + p * 4 + rw) * 9 + ft) * 1024 + lane * 16) = w;
  }
}

template <int G>
__device__ __forceinline__ void a_qkvblock(const Params& P, LAS char* smem, bf16x8 (&xf)[2][18], const unsigned (&voff)[3],
                                           int p, int quad, int wid, int lane) {
  constexpr int NT = G ? 4 : 5, T0 = G ? 5 : 0;
  const int rw = wid & 3, kq = lane >> 4, i16 = lane & 15;
  const int rq = (lane >> 2) & 3;
  const int rdA = (8 * kq + 4 * (kq & 1) + rq) * 288 + (lane & 3) * 8 + T0 * 32;
  const int rdB = (8 * kq + 4 * ((kq & 1) ^ 1) + rq) * 288 + (lane & 3) * 8 + T0 * 32;
  f32x4 acc[NT][2];
  u32x4 prev[NT];
#pragma unroll
  for (int t = 0; t < NT; ++t) prev[t] = (u32x4){0, 0, 0, 0};
  const rsrc_t rsw = MKRS(P.wqkv, 576 * 3456);
  auto round = [&](auto ORI_, int r) {
    constexpr int ORI = decltype(ORI_)::value;
#pragma unroll
    for (int st = 0; st < 9; ++st) {
      const int s = r * 9 + st;
      if (ORI == 1 || st < 7 || r < 2) { if (wid < 2) { WAIT_V(6); } else { WAIT_V(4); } }
      else if (st == 7) { if (wid < 2) { WAIT_V(3); } else { WAIT_V(2); } }
      else { WAIT_V(0); }
      WAIT_L0(); BAR();
      if (ORI == 1 || st < 6 || r < 2) {
        const int r1 = st < 6 ? r : r + 1, st1 = st < 6 ? st + 3 : st - 6;
        const int so = quad * 288 + st1 * 64 * 3456 + r1 * 1152;
        LAS char* dst = smem + ((s + 3) & 3) * AQ_SLOT;
        BLDS(rsw, voff[0], so, dst + wid * 1024);
        BLDS(rsw, voff[1], so, dst + (wid + 8) * 1024);
        if (G == 0 && wid < 2) BLDS(rsw, voff[2], so, dst + (wid + 16) * 1024);
      }
      auto mma = [&]() {
        LAS char* bA = smem + (s & 3) * AQ_SLOT + rdA;
        LAS char* bB = smem + (s & 3) * AQ_SLOT + rdB;
#pragma unroll
        for (int k2 = 0; k2 < 2; ++k2) {
          const int kb = st * 2 + k2;
#pragma unroll
          for (int t = 0; t < NT; ++t) {
            const bf16x8 f = tr_frag(bA + k2 * 9216 + t * 32, bB + k2 * 9216 + t * 32);
            const f32x4 z = {0, 0, 0, 0};
            if (ORI == 1) { acc[t][0] = MFMA(f, xf[0][kb], kb ? acc[t][0] : z); acc[t][1] = MFMA(f, xf[1][kb], kb ? acc[t][1] : z); }
            else { acc[t][0] = MFMA(xf[0][kb], f, kb ? acc[t][0] : z); acc[t][1] = MFMA(xf[1][kb], f, kb ? acc[t][1] : z); }
          }
        }
        SGB(0x100, 6);
#pragma unroll
        for (int i = 0; i < 4 * NT - 6; ++i) { SGB(0x8, 1); SGB(0x100, 1); }
        SGB(0x8, 6);
      };
      auto epi = [&]() {
        if (ORI == 0 && st < NT) {
          u32x4 w;
#pragma unroll
          for (int i = 0; i < 4; ++i) w[i] = cvt_pk(phi_f(bf_lo(prev[st < NT ? st : 0][i])), phi_f(bf_hi(prev[st < NT ? st : 0][i])));
          int ln = lane; asm volatile("" : "+v"(ln));
          if (r == 1) {
            const int gr = (T0 + st) * 4 + (ln >> 4), cc = (gr * 57) >> 9, gl = gr - cc * 9;
            const int off = gl < 8 ? (gl >> 1) * 256 + (ln & 15) * 16 + (gl & 1) * 8 : 1024 + (ln & 15) * 8;
            char* d = P.ws + WS_PQ + (size_t)((p * 8 + rw * 2) * 16 + quad * 4) * 1152 + (unsigned)(cc * 1152 + off);
            *(u32x2*)d = (u32x2){w.x, w.y};
            *(u32x2*)(d + 16 * 1152) = (u32x2){w.z, w.w};
          } else {
            *(LAS u32x4*)(smem + AQ_PK + ((T0 + st) * 4 + rw) * 1024 + ln * 16) = w;
          }
        }
      };
      if (G == 0) { mma(); SB0(); epi(); } else { epi(); SB0(); mma(); }
      SB0();
    }
#pragma unroll
    for (int t = 0; t < NT; ++t) {
      prev[t].x = cvt_pk(acc[t][0][0], acc[t][0][1]); prev[t].y = cvt_pk(acc[t][0][2], acc[t][0][3]);
      prev[t].z = cvt_pk(acc[t][1][0], acc[t][1][1]); prev[t].w = cvt_pk(acc[t][1][2], acc[t][1][3]);
    }
  };
  round(IC<1>{}, 0);
#pragma unroll 1
  for (int r = 1; r < 3; ++r) round(IC<0>{}, r);
#pragma unroll
  for (int t = 0; t < NT; ++t) *(LAS u32x4*)(smem + AQ_V + ((T0 + t) * 4 + rw) * 1024 + lane * 16) = prev[t];
  WAIT_L0(); BAR();
  float* kvp = (float*)(P.ws + WS_KVP);
  const bf16x8 ones = {0x3f80, 0x3f80, 0x3f80, 0x3f80, 0x3f80, 0x3f80, 0x3f80, 0x3f80};
#pragma unroll 1
  for (int i = wid; i < 9; i += 8) {
    bf16x8 pk[4];
#pragma unroll
    for (int k = 0; k < 4; ++k) pk[k] = *(LAS bf16x8*)(smem + AQ_PK + (i * 4 + k) * 1024 + lane * 16);
    int ob[4], hk[4];
#pragma unroll
    for (int e = 0; e < 4; ++e) {
      const int kcol = 16 * i + 4 * kq + e; hk[e] = kcol / 18;
      ob[e] = ((quad * 4 + (hk[e] >> 1)) * 16 + p) * 704 + (hk[e] & 1) * 342 + (kcol - 18 * hk[e]) * 19;
    }
    {
      f32x4 d = {0, 0, 0, 0};
#pragma unroll
      for (int k = 0; k < 4; ++k) d = MFMA(pk[k], ones, d);
      if (i16 == 0) {
#pragma unroll
        for (int e = 0; e < 4; ++e) kvp[ob[e] + 18] = d[e];
      }
    }
#pragma unroll
    for (int dj = -1; dj <= 1; ++dj) {
      const int jv = i + dj;
      if (jv >= 0 && jv < 9) {
        f32x4 d = {0, 0, 0, 0};
#pragma unroll
        for (int k = 0; k < 4; ++k) d = MFMA(pk[k], *(LAS bf16x8*)(smem + AQ_V + (jv * 4 + k) * 1024 + lane * 16), d);
        const int vcol = 16 * jv + i16, hv = vcol / 18, ee = vcol - 18 * hv;
#pragma unroll
        for (int e = 0; e < 4; ++e) if (hk[e] == hv) kvp[ob[e] + ee] = d[e];
      }
    }
  }
}

__global__ __launch_bounds__(512, 2) void kA(const Params P) {
  extern __shared__ __attribute__((aligned(16))) char smem_[];
  LAS char* smem = (LAS char*)smem_;
  const int tid = threadIdx.x, wid = __builtin_amdgcn_readfirstlane(tid >> 6), lane = tid & 63;
  const int bid = blockIdx.x, xcd = bid & 7, idx = bid >> 3;
  const int p = (xcd >> 2) * 8 + (idx >> 2), quad = xcd & 3, jj = idx & 3;
  {
    const rsrc_t rsx = MKRS(P.x, 2048 * 1152);
#pragma unroll
    for (int i = 0; i < 18; ++i) {
      const int lg = i * 8 + (lane >> 3), rl = lg / 9, ln = lg - rl * 9;
      const unsigned off = (unsigned)(slot_row(wid * 16 + rl) * 1152 + ln * 128 + (((lane & 7) ^ (rl >> 1)) * 16));
      BLDS(rsx, off, p * 128 * 1152, smem + wid * 18432 + i * 1024);
    }
  }
  const unsigned ep = __hip_atomic_load((GAS unsigned*)(P.ws + WS_EPOCH) + p * 32, __ATOMIC_RELAXED, __HIP_MEMORY_SCOPE_AGENT);
  {
    const int id = bid * 203 + tid;
    if (tid < 203 && id < 51840) {
      const int kb = id / 576, rem = id - kb * 576, q = rem / 144, dg = rem - q * 144;
      const char* s0; int du;
      if (kb < 72) { s0 = (const char*)P.wout + (size_t)(32 * kb + 4 * q) * 1152; du = 16; }
      else if (kb < 88) { s0 = (const char*)P.wo + (size_t)(36 * (kb - 72) + 4 * q) * 1152; du = 16; }
      else { s0 = (const char*)P.wo + (size_t)(36 * (8 * (kb - 88) + 2 * q) + 32) * 1152; du = 36; }
      s0 += dg * 8;
      u32x2 v[8];
#pragma unroll
      for (int e = 0; e < 4; ++e) { v[e] = *(const u32x2*)(s0 + e * 1152); v[4 + e] = *(const u32x2*)(s0 + (size_t)(du + e) * 1152); }
      const int cg = dg / 18, dl = (dg - cg * 18) * 4;
      const int off = dl < 64 ? (2 * (dl >> 5) + ((dl >> 2) & 1)) * 1024 + q * 256 + ((dl & 31) >> 3) * 64 : 4096 + q * 128 + (dl - 64) * 16;
      u32x4* d = (u32x4*)(P.ws + WS_WX + (size_t)(cg * 90 + kb) * 4608 + off);
#pragma unroll
      for (int cI = 0; cI < 4; ++cI) {
        u32x4 w;
#pragma unroll
        for (int e = 0; e < 4; ++e)
          w[e] = __builtin_amdgcn_perm(v[2 * e + 1][cI >> 1], v[2 * e][cI >> 1], (cI & 1) ? 0x07060302u : 0x05040100u);
        d[cI] = w;
      }
    }
  }
  unsigned voff[3];
  if (jj < 3) {
#pragma unroll
    for (int i = 0; i < 3; ++i) {
      const int row = (wid * 3 + i) * 8 + (lane >> 3);
      const int f = ((row >> 1) & 1) | (((row >> 3) & 1) << 1);
      voff[i] = (unsigned)(row * 9216 + (((lane & 7) ^ (f << 1)) * 16));
    }
  } else {
#pragma unroll
    for (int i = 0; i < 3; ++i) {
      const int pc = (wid + 8 * i) * 64 + lane, sr = pc / 18, ch = pc - sr * 18;
      const int row = sr ^ (((sr >> 3) & 1) << 2);
      voff[i] = (unsigned)(row * 3456 + ch * 16);
    }
  }
  WAIT_V(0);
  const unsigned tag = __builtin_amdgcn_readfirstlane(ep) + 1u;
  BAR();
  bf16x8 xf[2][18];
  {
    const int r16 = lane & 15, kq = lane >> 4, rw = wid & 3;
    LAS char* b0 = smem + (rw * 32 + r16) * 1152 + ((kq ^ (r16 >> 1)) * 16);
    LAS char* b1 = smem + (rw * 32 + r16) * 1152 + (((kq + 4) ^ (r16 >> 1)) * 16);
#pragma unroll
    for (int u = 0; u < 2; ++u)
#pragma unroll
      for (int kb = 0; kb < 18; ++kb) xf[u][kb] = *(LAS bf16x8*)(((kb & 1) ? b1 : b0) + u * 18432 + (kb >> 1) * 128);
  }
  WAIT_L0(); BAR();
  if (jj < 3) {
    const int j = quad * 3 + jj;
#pragma unroll
    for (int s = 0; s < 3; ++s) {
      const rsrc_t rsw = MKRS(P.win, 576 * 9216);
#pragma unroll
      for (int i = 0; i < 3; ++i) BLDS(rsw, voff[i], s * 192 * 9216 + (2304 + 3 * j * 64) * 2, smem + s * AZ_SLOT + wid * 3072 + i * 1024);
    }
  } else {
#pragma unroll
    for (int s = 0; s < 3; ++s) {
      const rsrc_t rsw = MKRS(P.wqkv, 576 * 3456);
      const int so = quad * 288 + s * 64 * 3456;
      LAS char* dst = smem + s * AQ_SLOT;
      BLDS(rsw, voff[0], so, dst + wid * 1024);
      BLDS(rsw, voff[1], so, dst + (wid + 8) * 1024);
      if (wid < 2) BLDS(rsw, voff[2], so, dst + (wid + 16) * 1024);
    }
  }
#pragma unroll
  for (int u = 0; u < 2; ++u) {
    const bf16x2_t one = {(__bf16)1.0f, (__bf16)1.0f};
    float s0 = 0.f, s1 = 0.f, q0 = 0.f, q1 = 0.f;
#pragma unroll
    for (int kb = 0; kb < 18; ++kb) {
      const u32x4 v = __builtin_bit_cast(u32x4, xf[u][kb]);
#pragma unroll
      for (int i = 0; i < 4; i += 2) {
        const unsigned ua = v[i], ub = v[i + 1];
        const bf16x2_t a = __builtin_bit_cast(bf16x2_t, ua), b = __builtin_bit_cast(bf16x2_t, ub);
        s0 = __builtin_amdgcn_fdot2_f32_bf16(a, one, s0, false); q0 = __builtin_amdgcn_fdot2_f32_bf16(a, a, q0, false);
        s1 = __builtin_amdgcn_fdot2_f32_bf16(b, one, s1, false); q1 = __builtin_amdgcn_fdot2_f32_bf16(b, b, q1, false);
      }
    }
    float s = s0 + s1, q = q0 + q1;
    s += __shfl_xor(s, 16); q += __shfl_xor(q, 16);
    s += __shfl_xor(s, 32); q += __shfl_xor(q, 32);
    const float m = s * (1.0f / 576.0f);
    const float var = fmaxf(q * (1.0f / 576.0f) - m * m, 0.f);
    const float rs = __builtin_amdgcn_rsqf(var + 1e-5f), sh = -m * rs;
#pragma unroll
    for (int kb = 0; kb < 18; ++kb) {
      u32x4 v = __builtin_bit_cast(u32x4, xf[u][kb]);
#pragma unroll
      for (int i = 0; i < 4; ++i) v[i] = cvt_pk(__builtin_fmaf(bf_lo(v[i]), rs, sh), __builtin_fmaf(bf_hi(v[i]), rs, sh));
      xf[u][kb] = __builtin_bit_cast(bf16x8, v);
    }
  }
  SB0();
  if (jj < 3) {
    if (wid < 4) a_zblock<0>(P, smem, xf, voff, p, quad * 3 + jj, wid, lane, tid, tag);
    else a_zblock<1>(P, smem, xf, voff, p, quad * 3 + jj, wid, lane, tid, tag);
  } else {
    if (wid < 4) a_qkvblock<0>(P, smem, xf, voff, p, quad, wid, lane);
    else a_qkvblock<1>(P, smem, xf, voff, p, quad, wid, lane);
  }
}

constexpr int STG = 34 * 1024;
constexpr int B_KV = 4 * STG, B_CNT = B_KV + 12 * 1024, B_LDS = B_CNT + 64;

__global__ __launch_bounds__(512, 2) void kB(const Params P) {
  extern __shared__ __attribute__((aligned(16))) char smem_[];
  LAS char* smem = (LAS char*)smem_;
  const int tid = threadIdx.x, wid = __builtin_amdgcn_readfirstlane(tid >> 6), lane = tid & 63;
  const int bid = blockIdx.x, xcd = bid & 7, idx = bid >> 3;
  const int p = (xcd >> 1) * 4 + (idx >> 3), c = (xcd & 1) * 8 + (idx & 7);
  const int r16 = lane & 15, kq = lane >> 4;
  if (wid >= 4) {
    const int lw = wid - 4, lt = tid - 256;
    f32x4 part[16]; u32x4 pq0[2]; u32x2 pq1[2];
    auto side_loads = [&]() {
      const char* kp = P.ws + WS_KVP + (size_t)c * 16 * 2816 + (lt < 170 ? lt : 170) * 16;
#pragma unroll
      for (int i = 0; i < 16; ++i) part[i] = *(const f32x4*)(kp + i * 2816);
#pragma unroll
      for (int tt = 0; tt < 2; ++tt) {
        const char* q = P.ws + WS_PQ + (size_t)((p * 8 + lw * 2 + tt) * 16 + c) * 1152;
        pq0[tt] = *(const u32x4*)(q + lane * 16);
        pq1[tt] = *(const u32x2*)(q + 1024 + r16 * 8);
      }
    };
    auto zero_img = [&]() {
#pragma unroll
      for (int i = 0; i < 3; ++i) *(LAS u32x4*)(smem + B_KV + i * 4096 + lt * 16) = (u32x4){0, 0, 0, 0};
      if (lt == 0) *(LAS unsigned*)(smem + B_CNT) = 0u;
      WAIT_L0();
    };
    if (lw < 2) {
      const rsrc_t rs = MKRS(P.ws + WS_Z2N + (size_t)c * (64 * 9 * 1024), 64 * 9 * 1024);
      auto issue = [&](int n) {
        LAS char* d = smem + (n & 3) * STG + lw * 9216;
#pragma unroll
        for (int i = 0; i < 9; ++i) BLDS(rs, lane * 16, n * 18432 + lw * 9216 + i * 1024, d + i * 1024);
      };
      issue(0); issue(1); issue(2);
      zero_img();
#pragma unroll 1
      for (int n = 0; n < 29; ++n) { WAIT_V(18); BAR(); issue(n + 3); }
      side_loads();
      WAIT_V(38); BAR(); WAIT_V(29); BAR(); WAIT_V(20); BAR();
    } else {
      const int hf = lw - 2;
      unsigned voff[8];
#pragma unroll
      for (int i = 0; i < 8; ++i) {
        const int rs = hf * 64 + i * 8 + (lane >> 3);
        voff[i] = (unsigned)(slot_row(rs) * 4096 + (((lane & 7) ^ ((rs & 15) >> 1)) * 16));
      }
      const rsrc_t rs = MKRS((const char*)P.wsp + (size_t)p * 128 * 4096, 128 * 4096);
      auto issue = [&](int n) {
        LAS char* d = smem + (n & 3) * STG + 18432 + hf * 8192;
#pragma unroll
        for (int i = 0; i < 8; ++i) BLDS(rs, voff[i], n * 128, d + i * 1024);
      };
      issue(0); issue(1); issue(2);
      zero_img();
#pragma unroll 1
      for (int n = 0; n < 29; ++n) { WAIT_V(16); BAR(); issue(n + 3); }
      side_loads();
      WAIT_V(36); BAR(); WAIT_V(28); BAR(); WAIT_V(20); BAR();
    }
    f32x4 sum = part[0];
#pragma unroll
    for (int i = 1; i < 16; ++i) sum += part[i];
    if (lt < 171) {
#pragma unroll
      for (int e4 = 0; e4 < 4; ++e4) {
        const int id = lt * 4 + e4, hh = id >= 342, rem = id - hh * 342, d = rem / 19, e = rem - d * 19;
        const int k = 18 * hh + d;
        const int kof = (k >> 5) * 1024 + ((k >> 3) & 3) * 256 + (k & 7) * 2;
        const u16 val = (u16)(cvt_pk(sum[e4], 0.f) & 0xffffu);
        if (e < 18) { const int n = 18 * hh + e; *(LAS u16*)(smem + B_KV + (n >> 4) * 2048 + (n & 15) * 16 + kof) = val; }
        else for (int n = 18 * hh; n < 18 * hh + 18; ++n) *(LAS u16*)(smem + B_KV + 6144 + (n >> 4) * 2048 + (n & 15) * 16 + kof) = val;
      }
    }
    WAIT_L0();
    if (lane == 0) __hip_atomic_fetch_add((LAS unsigned*)(smem + B_CNT), 1u, __ATOMIC_RELAXED, __HIP_MEMORY_SCOPE_WORKGROUP);
    while (__hip_atomic_load((LAS unsigned*)(smem + B_CNT), __ATOMIC_RELAXED, __HIP_MEMORY_SCOPE_WORKGROUP) < 4u) __builtin_amdgcn_s_sleep(1);
    asm volatile("" ::: "memory");
    bf16x8 kv[2][3][2];
#pragma unroll
    for (int m = 0; m < 2; ++m)
#pragma unroll
      for (int nt = 0; nt < 3; ++nt)
#pragma unroll
        for (int kb = 0; kb < 2; ++kb) kv[m][nt][kb] = *(LAS bf16x8*)(smem + B_KV + m * 6144 + nt * 2048 + kb * 1024 + lane * 16);
#pragma unroll
    for (int tt = 0; tt < 2; ++tt) {
      const bf16x8 q0 = __builtin_bit_cast(bf16x8, pq0[tt]);
      const u32x4 q1v = kq == 0 ? (u32x4){pq1[tt].x, pq1[tt].y, 0u, 0u} : (u32x4){0u, 0u, 0u, 0u};
      const bf16x8 q1 = __builtin_bit_cast(bf16x8, q1v);
      u32x2 o[3];
#pragma unroll
      for (int nt = 0; nt < 3; ++nt) {
        const f32x4 z = {0, 0, 0, 0};
        const f32x4 num = MFMA(kv[0][nt][1], q1, MFMA(kv[0][nt][0], q0, z));
        const f32x4 den = MFMA(kv[1][nt][1], q1, MFMA(kv[1][nt][0], q0, z));
        float r[4];
#pragma unroll
        for (int e = 0; e < 4; ++e) r[e] = num[e] * __builtin_amdgcn_rcpf(den[e] + 1e-6f);
        o[nt] = (u32x2){cvt_pk(r[0], r[1]), cvt_pk(r[2], r[3])};
      }
      char* hb = P.ws + WS_HX + (size_t)(p * 8 + lw * 2 + tt) * (90 * 1024);
      *(u32x4*)(hb + (72 + c) * 1024 + lane * 16) = (u32x4){o[0].x, o[0].y, o[1].x, o[1].y};
      if (kq == 0) *(u32x2*)(hb + (88 + (c >> 3)) * 1024 + ((c & 7) >> 1) * 256 + r16 * 16 + (c & 1) * 8) = o[2];
    }
    return;
  }
  char* hb = P.ws + WS_HX + (size_t)(p * 8 + wid * 2) * (90 * 1024) + lane * 16;
  u32x4 z1[2][4]; u32x2 z1o[2]; float bias[2];
#pragma unroll
  for (int tt = 0; tt < 2; ++tt) {
#pragma unroll
    for (int m = 0; m < 4; ++m) z1[tt][m] = *(const u32x4*)(hb + tt * (90 * 1024) + (c * 4 + m) * 1024);
    z1o[tt] = *(const u32x2*)(hb + tt * (90 * 1024) + (64 + (c >> 1)) * 1024 + (c & 1) * 8);
    bias[tt] = bf_lo(P.bsp[p * 128 + slot_row(wid * 32 + tt * 16 + r16)]);
  }
  f32x4 acc[9][2];
#pragma unroll
  for (int a = 0; a < 9; ++a) { acc[a][0] = (f32x4){0, 0, 0, 0}; acc[a][1] = (f32x4){0, 0, 0, 0}; }
  const int zoff = lane * 16;
  const int woff0 = 18432 + (wid * 32 + r16) * 128 + ((kq ^ (r16 >> 1)) * 16);
  const int woff1 = 18432 + (wid * 32 + r16) * 128 + (((kq + 4) ^ (r16 >> 1)) * 16);
  bf16x8 za[9], wa[2], zb[9], wb[2];
#define B_RD(Z, W, slot, kb) do { LAS char* b_ = smem + (slot) * STG; \
    W[0] = *(LAS bf16x8*)(b_ + ((kb) ? woff1 : woff0)); W[1] = *(LAS bf16x8*)(b_ + ((kb) ? woff1 : woff0) + 2048); \
    _Pragma("unroll") for (int i = 0; i < 9; ++i) Z[i] = *(LAS bf16x8*)(b_ + zoff + (kb) * 9216 + i * 1024); } while (0)
#define B_MM(Z, W) do { _Pragma("unroll") for (int i = 0; i < 9; ++i) { \
    acc[i][i & 1] = MFMA(Z[i], W[i & 1], acc[i][i & 1]); acc[i][(i & 1) ^ 1] = MFMA(Z[i], W[(i & 1) ^ 1], acc[i][(i & 1) ^ 1]); } } while (0)
#define B_ILV() do { _Pragma("unroll") for (int i_ = 0; i_ < 11; ++i_) { SGB(0x100, 1); SGB(0x8, 1); } SGB(0x8, 7); } while (0)
  BAR();
  B_RD(za, wa, 0, 0);
  B_RD(zb, wb, 0, 1);
  B_MM(za, wa);
#pragma unroll 1
  for (int n = 1; n < 32; ++n) {
    WAIT_L0(); BAR();
    const int slot = n & 3;
    B_RD(za, wa, slot, 0); B_MM(zb, wb); B_ILV(); SB0();
    B_RD(zb, wb, slot, 1); B_MM(za, wa); B_ILV(); SB0();
  }
  B_MM(zb, wb);
#pragma unroll
  for (int tt = 0; tt < 2; ++tt) {
    const float b = bias[tt];
#pragma unroll
    for (int m = 0; m < 4; ++m) {
      const u32x4 z = z1[tt][m]; const f32x4 g0 = acc[2 * m][tt] + b, g1 = acc[2 * m + 1][tt] + b;
      u32x4 v;
      v.x = cvt_pk(bf_lo(z.x) * g0[0], bf_hi(z.x) * g0[1]); v.y = cvt_pk(bf_lo(z.y) * g0[2], bf_hi(z.y) * g0[3]);
      v.z = cvt_pk(bf_lo(z.z) * g1[0], bf_hi(z.z) * g1[1]); v.w = cvt_pk(bf_lo(z.w) * g1[2], bf_hi(z.w) * g1[3]);
      *(u32x4*)(hb + tt * (90 * 1024) + (c * 4 + m) * 1024) = v;
    }
    const u32x2 z = z1o[tt]; const f32x4 g0 = acc[8][tt] + b;
    u32x2 v; v.x = cvt_pk(bf_lo(z.x) * g0[0], bf_hi(z.x) * g0[1]); v.y = cvt_pk(bf_lo(z.y) * g0[2], bf_hi(z.y) * g0[3]);
    *(u32x2*)(hb + tt * (90 * 1024) + (64 + (c >> 1)) * 1024 + (c & 1) * 8) = v;
  }
}

constexpr int C_LDS = 4 * STG;

template <int DH>
__device__ __forceinline__ void c_compute(const Params& P, LAS char* smem, int rg, int cg, int th, int lane) {
  constexpr int ND = DH ? 2 : 3;
  const int r16 = lane & 15, kq = lane >> 4;
  u32x4 xr[2]; u32x2 xo[2]; size_t orow[2];
#pragma unroll
  for (int tt = 0; tt < 2; ++tt) {
    orow[tt] = (size_t)(rg * 64 + slot_row(th * 32 + tt * 16 + r16)) * 1152 + cg * 144;
    xr[tt] = *(const u32x4*)((const char*)P.x + orow[tt] + DH * 64 + kq * 16);
    if (DH == 0) xo[tt] = *(const u32x2*)((const char*)P.x + orow[tt] + 128 + (kq & 1) * 8);
  }
  f32x4 acc[ND][2];
#pragma unroll
  for (int a = 0; a < ND; ++a) { acc[a][0] = (f32x4){0, 0, 0, 0}; acc[a][1] = (f32x4){0, 0, 0, 0}; }
  const int woff = DH * 2048 + lane * 16, woff4 = 4096 + kq * 128 + (r16 & 7) * 16;
  const int hoff = 18432 + th * 8192 + lane * 16;
  bf16x8 wa[2][ND], ha[2][2], wb[2][ND], hb[2][2];
#define C_RD(W, H, slot, kp) do { LAS char* b_ = smem + (slot) * STG; \
    _Pragma("unroll") for (int k = 0; k < 2; ++k) { \
      H[k][0] = *(LAS bf16x8*)(b_ + hoff + ((kp) * 2 + k) * 1024); H[k][1] = *(LAS bf16x8*)(b_ + hoff + 4096 + ((kp) * 2 + k) * 1024); \
      W[k][0] = *(LAS bf16x8*)(b_ + woff + ((kp) * 2 + k) * 4608); W[k][1] = *(LAS bf16x8*)(b_ + woff + 1024 + ((kp) * 2 + k) * 4608); \
      if (ND == 3) W[k][ND - 1] = *(LAS bf16x8*)(b_ + woff4 + ((kp) * 2 + k) * 4608); } } while (0)
#define C_MM(W, H) do { _Pragma("unroll") for (int k = 0; k < 2; ++k) _Pragma("unroll") for (int i = 0; i < ND; ++i) { \
    acc[i][0] = MFMA(W[k][i], H[k][0], acc[i][0]); acc[i][1] = MFMA(W[k][i], H[k][1], acc[i][1]); } } while (0)
#define C_ILV() do { _Pragma("unroll") for (int i_ = 0; i_ < ND + 2; ++i_) { SGB(0x100, 2); SGB(0x8, 1); } SGB(0x8, 3 * ND - 2); } while (0)
  BAR();
  C_RD(wa, ha, 0, 0);
  C_RD(wb, hb, 0, 1);
  C_MM(wa, ha);
#pragma unroll 1
  for (int n = 1; n < 22; ++n) {
    WAIT_L0(); BAR();
    const int slot = n & 3;
    C_RD(wa, ha, slot, 0); C_MM(wb, hb); C_ILV(); SB0();
    C_RD(wb, hb, slot, 1); C_MM(wa, ha); C_ILV(); SB0();
  }
  WAIT_L0(); BAR();
  C_RD(wa, ha, 2, 0); C_MM(wb, hb);
  C_MM(wa, ha);
#pragma unroll
  for (int tt = 0; tt < 2; ++tt) {
    const u32x4 x = xr[tt]; const f32x4 a0 = acc[0][tt], a1 = acc[1][tt];
    u32x4 v;
    v.x = cvt_pk(bf_lo(x.x) + a0[0], bf_hi(x.x) + a0[1]); v.y = cvt_pk(bf_lo(x.y) + a0[2], bf_hi(x.y) + a0[3]);
    v.z = cvt_pk(bf_lo(x.z) + a1[0], bf_hi(x.z) + a1[1]); v.w = cvt_pk(bf_lo(x.w) + a1[2], bf_hi(x.w) + a1[3]);
    *(u32x4*)((char*)P.out + orow[tt] + DH * 64 + kq * 16) = v;
    if (DH == 0) {
      const f32x4 a2 = acc[ND - 1][tt];
      u32x2 w; w.x = cvt_pk(bf_lo(xo[tt].x) + a2[0], bf_hi(xo[tt].x) + a2[1]); w.y = cvt_pk(bf_lo(xo[tt].y) + a2[2], bf_hi(xo[tt].y) + a2[3]);
      if (kq < 2) *(u32x2*)((char*)P.out + orow[tt] + 128 + kq * 8) = w;
    }
  }
}

__global__ __launch_bounds__(512, 2) void kC(const Params P) {
  extern __shared__ __attribute__((aligned(16))) char smem_[];
  LAS char* smem = (LAS char*)smem_;
  const int tid = threadIdx.x, wid = __builtin_amdgcn_readfirstlane(tid >> 6), lane = tid & 63;
  const int bid = blockIdx.x, xcd = bid & 7, idx = bid >> 3;
  const int rg = (xcd >> 1) * 8 + (idx >> 2), cg = (xcd & 1) * 4 + (idx & 3);
  if (wid >= 4) {
    const int lw = wid - 4, kp = lw & 1;
    if (lw < 2) {
      const rsrc_t rs = MKRS(P.ws + WS_WX + (size_t)cg * (90 * 4608), 90 * 4608);
      auto issue = [&](int n) {
        LAS char* d = smem + (n & 3) * STG + kp * 9216;
#pragma unroll
        for (int i = 0; i < 9; ++i) BLDS(rs, lane * 16, n * 18432 + kp * 9216 + i * 1024, d + i * 1024);
      };
      issue(0); issue(1); issue(2);
#pragma unroll 1
      for (int n = 0; n < 19; ++n) { WAIT_V(18); BAR(); issue(n + 3); }
      if (kp == 0) { WAIT_V(18); BAR(); issue(22); WAIT_V(18); BAR(); WAIT_V(9); BAR(); WAIT_V(0); BAR(); }
      else { WAIT_V(18); BAR(); WAIT_V(9); BAR(); WAIT_V(0); BAR(); BAR(); }
    } else {
      const rsrc_t rs = MKRS(P.ws + WS_HX + (size_t)rg * 4 * (90 * 1024), 4 * 90 * 1024);
      auto issue = [&](int n) {
        LAS char* d = smem + (n & 3) * STG + 18432 + kp * 2048;
#pragma unroll
        for (int i = 0; i < 8; ++i)
          BLDS(rs, lane * 16, n * 4096 + kp * 2048 + (i >> 1) * (90 * 1024) + (i & 1) * 1024, d + (i >> 1) * 4096 + (i & 1) * 1024);
      };
      issue(0); issue(1); issue(2);
#pragma unroll 1
      for (int n = 0; n < 19; ++n) { WAIT_V(16); BAR(); issue(n + 3); }
      if (kp == 0) { WAIT_V(16); BAR(); issue(22); WAIT_V(16); BAR(); WAIT_V(8); BAR(); WAIT_V(0); BAR(); }
      else { WAIT_V(16); BAR(); WAIT_V(8); BAR(); WAIT_V(0); BAR(); BAR(); }
    }
    return;
  }
  if (wid < 2) c_compute<0>(P, smem, rg, cg, wid & 1, lane);
  else c_compute<1>(P, smem, rg, cg, wid & 1, lane);
}

extern "C" void kernel_launch(void* const* d_in, const int* in_sizes, int n_in, void* d_out, int out_size, void* d_ws,
                              size_t ws_size, hipStream_t stream) {
  Params P;
  P.x = (const u16*)d_in[0]; P.wqkv = (const u16*)d_in[1]; P.wo = (const u16*)d_in[2]; P.win = (const u16*)d_in[4];
  P.wsp = (const u16*)d_in[5]; P.bsp = (const u16*)d_in[6]; P.wout = (const u16*)d_in[7];
  P.out = (u16*)d_out; P.ws = (char*)d_ws;
  (void)hipFuncSetAttribute((const void*)kA, hipFuncAttributeMaxDynamicSharedMemorySize, A_LDS);
  (void)hipFuncSetAttribute((const void*)kB, hipFuncAttributeMaxDynamicSharedMemorySize, B_LDS);
  (void)hipFuncSetAttribute((const void*)kC, hipFuncAttributeMaxDynamicSharedMemorySize, C_LDS);
  kA<<<256, 512, A_LDS, stream>>>(P);
  kB<<<256, 512, B_LDS, stream>>>(P);
  kC<<<256, 512, C_LDS, stream>>>(P);
}
```

```cpp
#include <hip/hip_runtime.h>
#include <hip/hip_bf16.h>

#if defined(__HIP_DEVICE_COMPILE__)
#define NOPK __attribute__((target("no-packed-fp32-ops")))
#else
#define NOPK
#endif
#define LAS __attribute__((address_space(3)))
#define GAS __attribute__((address_space(1)))
typedef unsigned short u16;
typedef unsigned long long u64;
typedef short bf16x8 __attribute__((ext_vector_type(8)));
typedef short s16x4 __attribute__((ext_vector_type(4)));
typedef float f32x4 __attribute__((ext_vector_type(4)));
typedef float f32x2 __attribute__((ext_vector_type(2)));
typedef float f32x16 __attribute__((ext_vector_type(16)));
typedef unsigned u32x4 __attribute__((ext_vector_type(4)));
typedef unsigned u32x2 __attribute__((ext_vector_type(2)));
typedef __bf16 bf16x2_t __attribute__((ext_vector_type(2)));
typedef __amdgpu_buffer_rsrc_t brs_t;
typedef u32x4 rsrc_t;

#define WAIT_L0() asm volatile("s_waitcnt lgkmcnt(0)" ::: "memory")
#define BAR() do { asm volatile("" ::: "memory"); __builtin_amdgcn_s_barrier(); asm volatile("" ::: "memory"); } while (0)
#define SB0() __builtin_amdgcn_sched_barrier(0)
#define SGB(mask, n) __builtin_amdgcn_sched_group_barrier(mask, n, 0)
#define PIN(x) asm volatile("" : "+v"(x))
#define MFMA(a, b, c) __builtin_amdgcn_mfma_f32_16x16x32_bf16(a, b, c, 0, 0, 0)
#define MFMA32(a, b, c) __builtin_amdgcn_mfma_f32_32x32x16_bf16(a, b, c, 0, 0, 0)
#define DI __device__ __forceinline__
template <int N> DI void wait_v() { asm volatile("s_waitcnt vmcnt(%0)" :: "i"(N) : "memory"); }
template <int V> struct IC { static constexpr int value = V; };
template <int I, int N, class F> DI void sfor(F&& f) {
  if constexpr (I < N) { f(IC<I>{}); sfor<I + 1, N>(f); }
}

DI rsrc_t MKRS(const void* p, unsigned bytes) {
  const u64 a = (u64)p;
  return (rsrc_t){(unsigned)a, (unsigned)(a >> 32) & 0xffffu, bytes, 0x00020000u};
}
DI void BLDS(rsrc_t rs, unsigned voff, unsigned soff, LAS char* l) {
  asm volatile("s_mov_b32 m0, %3\n\ts_nop 0\n\tbuffer_load_dwordx4 %0, %1, %2 offen lds"
               :: "v"(voff), "s"(rs), "s"(soff), "s"((unsigned)(size_t)l) : "memory");
}
DI void BLDS1(rsrc_t rs, unsigned voff, unsigned soff, LAS char* l) {
  asm volatile("s_mov_b32 m0, %3\n\ts_nop 0\n\tbuffer_load_dwordx4 %0, %1, %2 offen sc1 lds"
               :: "v"(voff), "s"(rs), "s"(soff), "s"((unsigned)(size_t)l) : "memory");
}
template <int N> DI void BLDSx(rsrc_t rs, unsigned voff, unsigned soff, LAS char* l) {
  if (N == 4)
    asm volatile("s_mov_b32 m0, %3\n\ts_nop 0\n\tbuffer_load_dwordx4 %0, %1, %2 offen sc1 lds\n\tbuffer_load_dwordx4 %0, %1, %2 offen offset:1024 sc1 lds\n\t"
                 "buffer_load_dwordx4 %0, %1, %2 offen offset:2048 sc1 lds\n\tbuffer_load_dwordx4 %0, %1, %2 offen offset:3072 sc1 lds"
                 :: "v"(voff), "s"(rs), "s"(soff), "s"((unsigned)(size_t)l) : "memory");
  else if (N == 2)
    asm volatile("s_mov_b32 m0, %3\n\ts_nop 0\n\tbuffer_load_dwordx4 %0, %1, %2 offen sc1 lds\n\tbuffer_load_dwordx4 %0, %1, %2 offen offset:1024 sc1 lds"
                 :: "v"(voff), "s"(rs), "s"(soff), "s"((unsigned)(size_t)l) : "memory");
  else
    asm volatile("s_mov_b32 m0, %3\n\ts_nop 0\n\tbuffer_load_dwordx4 %0, %1, %2 offen sc1 lds"
                 :: "v"(voff), "s"(rs), "s"(soff), "s"((unsigned)(size_t)l) : "memory");
}
DI void BLDS4v(rsrc_t rs, const unsigned (&v)[4], unsigned soff, LAS char* l) {
  asm volatile("s_mov_b32 m0, %6\n\ts_nop 0\n\tbuffer_load_dwordx4 %0, %4, %5 offen lds\n\tbuffer_load_dwordx4 %1, %4, %5 offen offset:1024 lds\n\t"
               "buffer_load_dwordx4 %2, %4, %5 offen offset:2048 lds\n\tbuffer_load_dwordx4 %3, %4, %5 offen offset:3072 lds"
               :: "v"(v[0]), "v"(v[1]), "v"(v[2]), "v"(v[3]), "s"(rs), "s"(soff), "s"((unsigned)(size_t)l) : "memory");
}
DI void BLDS16(rsrc_t rs, unsigned voff, unsigned soff, LAS char* l) {
  asm volatile("s_mov_b32 m0, %3\n\ts_mov_b64 exec, 0xffff\n\tbuffer_load_dwordx4 %0, %1, %2 offen lds\n\ts_mov_b64 exec, -1"
               :: "v"(voff), "s"(rs), "s"(soff), "s"((unsigned)(size_t)l) : "memory");
}
DI void TOUCH(rsrc_t rs, unsigned voff, unsigned soff, LAS char* l) {
  asm volatile("s_mov_b32 m0, %3\n\ts_nop 0\n\tbuffer_load_dword %0, %1, %2 offen lds"
               :: "v"(voff), "s"(rs), "s"(soff), "s"((unsigned)(size_t)l) : "memory");
}
DI void ald16(u32x4& d, rsrc_t rs, unsigned voff, unsigned soff) {
  asm volatile("buffer_load_dwordx4 %0, %1, %2, %3 offen sc1" : "=v"(d) : "v"(voff), "s"(rs), "s"(soff) : "memory");
}
DI void ald8(u32x2& d, rsrc_t rs, unsigned voff, unsigned soff) {
  asm volatile("buffer_load_dwordx2 %0, %1, %2, %3 offen sc1" : "=v"(d) : "v"(voff), "s"(rs), "s"(soff) : "memory");
}
DI void ald4(unsigned& d, rsrc_t rs, unsigned voff, unsigned soff) {
  asm volatile("buffer_load_dword %0, %1, %2, %3 offen sc1" : "=v"(d) : "v"(voff), "s"(rs), "s"(soff) : "memory");
}
DI void gld8(u32x2& d, const void* p) { asm volatile("global_load_dwordx2 %0, %1, off" : "=v"(d) : "v"(p) : "memory"); }
#define ST16(rs, voff, soff, v) __builtin_amdgcn_raw_buffer_store_b128(v, rs, (int)((voff) + (soff)), 0, 16)
#define ST8(rs, voff, soff, v) __builtin_amdgcn_raw_buffer_store_b64(v, rs, (int)(voff), (int)(soff), 16)
#define ST4(rs, voff, soff, v) __builtin_amdgcn_raw_buffer_store_b32(v, rs, (int)(voff), (int)(soff), 16)
#define LD16(rs, voff, soff) __builtin_amdgcn_raw_buffer_load_b128(rs, (int)(voff), (int)(soff), 16)
#define LD8(rs, voff, soff) __builtin_amdgcn_raw_buffer_load_b64(rs, (int)(voff), (int)(soff), 16)

constexpr unsigned WS_EPOCH = 0;
constexpr unsigned WS_F1 = 1024;
constexpr unsigned WS_F2Q = WS_F1 + 4096;
constexpr unsigned WS_F2Z = WS_F2Q + 512;
constexpr unsigned WS_F3O = WS_F2Z + 2048;
constexpr unsigned WS_F3W = WS_F3O + 2048;
constexpr unsigned WS_F4 = WS_F3W + 1024;
constexpr unsigned WS_STAT = 16384;
constexpr unsigned WS_KVP = WS_STAT + 16 * 12 * 256 * 8;
constexpr unsigned WS_PQ = WS_KVP + 16 * 16 * 2816;
constexpr unsigned WS_WX = WS_PQ + 128 * 16 * 1152;
constexpr unsigned WS_Z2N = WS_WX + 8 * 90 * 4608;
constexpr unsigned WS_Z1 = WS_Z2N + 16 * 64 * 9 * 1024;
constexpr unsigned WS_HX = WS_Z1 + 128 * 72 * 1024;
constexpr unsigned WS_END = WS_HX + 128 * 90 * 1024;
constexpr unsigned SPIN_MAX = 1u << 18;

#define STAMP(K, i) do {} while (0)

struct Params {
  const u16* x; const u16* wqkv; const u16* wo; const u16* win; const u16* wsp; const u16* bsp; const u16* wout;
  u16* out; char* ws;
};

DI int lane_id() {
  int l; asm volatile("v_mbcnt_lo_u32_b32 %0, -1, 0\n\tv_mbcnt_hi_u32_b32 %0, -1, %0" : "=v"(l)); return l;
}
DI unsigned cvt_pk(float lo, float hi) {
  f32x2 v = {lo, hi}; bf16x2_t b = __builtin_convertvector(v, bf16x2_t); return __builtin_bit_cast(unsigned, b);
}
DI float bf_lo(unsigned v) { return __uint_as_float(v << 16); }
DI float bf_hi(unsigned v) { return __uint_as_float(v & 0xffff0000u); }
constexpr float GK0 = -2.302208198f, GK1 = -0.1029432397f;
DI float gelu_t(float x) {
  const float e = __builtin_amdgcn_exp2f(x * __builtin_fmaf(x * x, GK1, GK0));
  return x * __builtin_amdgcn_rcpf(1.0f + e);
}
DI float phi_f(float t) {
  return __builtin_fmaxf(t, 1e-6f) + __builtin_amdgcn_fmed3f(__builtin_amdgcn_exp2f(t * 1.4426950409f), 0.f, 1.f);
}
DI bf16x8 tr_frag(LAS char* a0, LAS char* a1) {
  const s16x4 lo = __builtin_amdgcn_ds_read_tr16_b64_v4i16((LAS s16x4*)a0);
  const s16x4 hi = __builtin_amdgcn_ds_read_tr16_b64_v4i16((LAS s16x4*)a1);
  return __builtin_shufflevector(lo, hi, 0, 1, 2, 3, 4, 5, 6, 7);
}
template <int CTRL> DI float dpp_get(float v) {
  return __builtin_bit_cast(float, __builtin_amdgcn_update_dpp(0, __builtin_bit_cast(int, v), CTRL, 0xf, 0xf, true));
}
DI float oct_sum(float v) {
  v += dpp_get<0xB1>(v); v += dpp_get<0x4E>(v); v += dpp_get<0x141>(v); return v;
}
DI int slot_row(int rs) { return (rs & ~31) + 16 * ((rs >> 2) & 1) + 4 * ((rs >> 3) & 3) + (rs & 3); }
DI unsigned flag_ld(const char* ws, unsigned off) {
  return __hip_atomic_load((GAS unsigned*)(ws + off), __ATOMIC_RELAXED, __HIP_MEMORY_SCOPE_AGENT);
}
DI void flag_st(char* ws, unsigned off, unsigned v) {
  __hip_atomic_store((GAS unsigned*)(ws + off), v, __ATOMIC_RELAXED, __HIP_MEMORY_SCOPE_AGENT);
}
DI bool all_eq(unsigned v, unsigned tag) { return __builtin_amdgcn_ballot_w64(v != tag) == 0; }
DI void spin_flag(const char* ws, unsigned off, unsigned tag) {
  for (unsigned i = 0; i < SPIN_MAX; ++i) {
    if (all_eq(flag_ld(ws, off), tag)) break;
    __builtin_amdgcn_s_sleep(1);
  }
  asm volatile("" ::: "memory");
}

constexpr int LDS_BYTES = 160 * 1024;
constexpr int L_SCR = 159 * 1024;
constexpr int A_XN = 15 * 1024;
constexpr int AZ_SLOT = 24 * 1024, AZ_PARK = 96 * 1024, AZ_ST = 144 * 1024, AZ_ST2 = 147 * 1024, AZ_TAB = 149 * 1024;
constexpr int AQ_SLOT = 18 * 1024, AQ_PK = 123 * 1024, AQ_V = 72 * 1024, AQ_KV = 108 * 1024;
constexpr int STG = 34 * 1024, B_KV = 4 * STG;
#define ROT(n, r) (((n) & ~3) | (((n) + (r)) & 3))

struct Ctx {
  int pa, quad, jj, p, c, wid, bid;
  unsigned tag;
};

template <int ROLE> struct Loader {
  rsrc_t rsws, rsw; unsigned voff[4]; unsigned l16; int p, c; LAS char* smem;
  DI void init(const Params& P, const Ctx& X, LAS char* sm) {
    smem = sm; p = X.p; c = X.c;
    const int ln = lane_id();
    l16 = ln * 16;
    rsws = MKRS(P.ws, WS_END);
    if (ROLE == 1) {
      rsw = MKRS((const char*)P.wsp + (size_t)p * 128 * 4096, 128 * 4096);
#pragma unroll
      for (int i = 0; i < 4; ++i) {
        const int rs = i * 8 + (ln >> 3);
        voff[i] = (unsigned)(slot_row(rs) * 4096 + (((ln & 7) ^ ((rs & 15) >> 1)) * 16) - i * 1024);
      }
    }
  }
  DI void issue_b(int N) {
    LAS char* d = smem + (N & 3) * STG;
    const int n = ROT(N, p + c);
    if (ROLE == 0) {
      const unsigned so = WS_Z2N + c * (64 * 9 * 1024) + n * 18432;
#pragma unroll
      for (int i = 0; i < 4; ++i) BLDSx<4>(rsws, l16, so + i * 4096, d + i * 4096);
      BLDSx<2>(rsws, l16, so + 16384, d + 16384);
    } else {
#pragma unroll
      for (int i = 0; i < 4; ++i) BLDS4v(rsw, voff, n * 128 + i * (32 * 4096), d + 18432 + i * 4096);
    }
  }
  template <int NK> DI void issue_c(int N) {
    LAS char* d = smem + (N & 3) * STG;
    const int M = N - 32, rg = 2 * p + (c >> 3), cg = c & 7;
    const int kb = M == 0 ? 88 : M < 5 ? 72 + 4 * (M - 1) : 4 * (M < 21 ? ROT(M - 5, p + c) : M - 5);
    if (ROLE == 0) {
      const unsigned so = WS_WX + (cg * 90 + kb) * 4608;
#pragma unroll
      for (int i = 0; i < NK; ++i) BLDSx<4>(rsws, l16, so + i * 4096, d + i * 4096);
      BLDSx<NK / 2>(rsws, l16, so + NK * 4096, d + NK * 4096);
    } else {
#pragma unroll
      for (int i = 0; i < 4; ++i) BLDSx<NK>(rsws, l16, WS_HX + ((rg * 4 + i) * 90 + kb) * 1024, d + 18432 + i * 4096);
    }
  }
};
DI unsigned f1_off(const Ctx& X) { const int l = lane_id(); return WS_F1 + X.c * 256 + (l < 48 ? l : 47) * 4; }
DI int f1_src(int c, int w) { return (w == 0 ? 8 * c : w == 1 ? 8 * c + 7 : 128 + c) / 12; }
DI void warm_wsp(const Params& P, const Ctx& X, LAS char* smem) {
  const rsrc_t rs = MKRS(P.wsp, 2048 * 4096);
  const int l = lane_id();
  const unsigned vo = (l >> 5) * 4096 + (l & 31) * 128;
#pragma unroll
  for (int i = 0; i < 2; ++i) TOUCH(rs, vo, (X.p * 128 + (X.c & 7) * 16 + X.wid * 4 + i * 2) * 4096, smem + L_SCR);
}

DI void a_zblock(const Params& P, const Ctx& X, LAS char* smem) {
  const int wid = X.wid, p = X.pa, j = X.quad * 3 + X.jj;
  int lane = lane_id();
  const int rw = wid & 3, G = wid >> 2;
  int h = lane >> 5;
  const rsrc_t rsw = MKRS(P.win, 576 * 9216);
  const brs_t ws = __builtin_amdgcn_make_buffer_rsrc(P.ws, 0, WS_END, 0x00020000);
  unsigned voff;
  {
    const int row = wid * 8 + (lane >> 3);
    voff = (unsigned)(row * 9216 + (((lane & 7) ^ (((row >> 1) & 1) << 2)) * 16));
  }
  const int cz2 = (2304 + 3 * j * 64) * 2, cz1 = 3 * j * 64 * 2;
  auto dma = [&](auto S_, auto I_) {
    constexpr int S = decltype(S_)::value, I = decltype(I_)::value, R = S / 3;
    BLDS(rsw, voff, (R < 3 ? cz2 + R * 128 : cz1 + (R - 3) * 128) + ((S % 3) * 192 + I * 64) * 9216,
         smem + (S & 3) * AZ_SLOT + I * 8192 + wid * 1024);
  };
  bf16x8 xf[36];
  {
    LAS char* xb = smem + A_XN + (rw * 32 + (lane & 31)) * 128;
    const int sw = (lane >> 1) & 7;
    LAS char* b[4];
#pragma unroll
    for (int i = 0; i < 4; ++i) b[i] = xb + (((2 * i + h) ^ sw) * 16);
#pragma unroll
    for (int ks = 0; ks < 12; ++ks) xf[ks] = *(LAS bf16x8*)(b[ks & 3] + (ks >> 2) * 16384);
    WAIT_L0(); BAR();
    sfor<0, 2>([&](auto S_) { sfor<0, 3>([&](auto I_) { dma(S_, I_); }); });
#pragma unroll
    for (int ks = 12; ks < 36; ++ks) xf[ks] = *(LAS bf16x8*)(b[ks & 3] + (ks >> 2) * 16384);
  }
  STAMP(0, 3);
  const int rq = (lane >> 2) & 3;
  LAS char* rd = smem + (8 * h + rq) * 128 + ((G ^ (rq >> 1)) * 64) + ((lane >> 4) & 1) * 32 + (lane & 3) * 8;
  LAS char* park = smem + AZ_PARK + wid * 2048 + h * 1024 + (lane & 31) * 16;
  const bf16x8 ones = {0x3f80, 0x3f80, 0x3f80, 0x3f80, 0x3f80, 0x3f80, 0x3f80, 0x3f80};
  f32x16 acc[2];
  bf16x8 wf[5];
  f32x4 ssum[2], ssq[2];
  unsigned pk[4], z1r[2][8];
  float gt[4] = {0.f, 0.f, 0.f, 0.f}, gb = 0.f;
  u32x2 gv[6];
  f32x4 tab[4];
  u32x4 nv;
  auto rdw = [&](auto E_) {
    constexpr int E = decltype(E_)::value, S = E / 12, o = (S & 3) * AZ_SLOT + (E % 12) * 2048;
    wf[E % 5] = tr_frag(rd + o, rd + o + 512);
  };
  auto stats = [&](auto RR_) {
    constexpr int RR = decltype(RR_)::value;
    const int ln = lane_id();
    LAS char* a = smem + AZ_PARK + RR * 16384 + wid * 2048 + ((ln >> 1) & 1) * 512 + (8 * (ln >> 4) + ((ln >> 2) & 3)) * 16 + (ln & 1) * 8;
#pragma unroll
    for (int u = 0; u < 2; ++u) {
      const bf16x8 z = tr_frag(a + u * 1024, a + u * 1024 + 64);
      const f32x4 zz = {0, 0, 0, 0};
      ssum[u] = MFMA(ones, z, RR ? ssum[u] : zz);
      ssq[u] = MFMA(z, z, RR ? ssq[u] : zz);
      PIN(ssum[u]); PIN(ssq[u]);
    }
  };
  auto gelu_st = [&](auto RR_, auto EL_, auto ST_) {
    constexpr int RR = decltype(RR_)::value, EL = decltype(EL_)::value, ST = decltype(ST_)::value;
    if constexpr (EL >= 0 && EL < 16) {
      const float x = acc[RR & 1][EL];
      float& g = gt[EL & 3];
      if (ST == 0) { g = __builtin_fmaf(x * x, GK1, GK0); PIN(g); }
      if (ST == 1) { g = __builtin_amdgcn_exp2f(x * g); PIN(g); }
      if (ST == 2) { g = __builtin_amdgcn_rcpf(1.0f + g); PIN(g); }
      if (ST == 3) {
        const float y = x * g;
        if (EL & 1) { pk[(EL >> 1) & 3] = cvt_pk(gb, y); PIN(pk[(EL >> 1) & 3]); } else { gb = y; PIN(gb); }
        if ((EL & 7) == 7) *(LAS u32x4*)(park + RR * 16384 + (EL >> 3) * 512) = (u32x4){pk[0], pk[1], pk[2], pk[3]};
      }
    }
  };
  wait_v<3>(); WAIT_L0(); BAR();
  sfor<0, 4>(rdw);
  sfor<0, 216>([&](auto E_) {
    constexpr int E = decltype(E_)::value, R = E / 36, ks = E % 36, EN = E + 4, SN = EN / 12;
    if constexpr (EN < 216) {
      if constexpr (EN % 12 == 0) {
        wait_v<(SN >= 16 ? 6 : SN == 14 ? 9 : SN == 13 ? 4 : 3)>();
        if (SN == 12 || SN == 15) WAIT_L0();
        BAR();
      }
      rdw(IC<EN>{});
      if constexpr (SN + 2 < 18 && EN % 12 >= 4 && EN % 12 < 7) dma(IC<SN + 2>{}, IC<EN % 12 - 4>{});
    }
    const f32x16 zz = {};
    if constexpr (R < 3) acc[R & 1] = MFMA32(xf[ks], wf[E % 5], ks ? acc[R & 1] : zz);
    else acc[R & 1] = MFMA32(wf[E % 5], xf[ks], ks ? acc[R & 1] : zz);
    PIN(acc[R & 1]);
    if constexpr ((R == 1 || R == 2) && ks >= 1) {
      gelu_st(IC<R - 1>{}, IC<(ks - 1) / 2>{}, IC<(ks - 1) & 1>{});
      gelu_st(IC<R - 1>{}, IC<(ks - 1) / 2 - 1>{}, IC<((ks - 1) & 1) + 2>{});
      if constexpr (ks == 35) stats(IC<R - 1>{});
    }
    if constexpr (R == 3) {
      sfor<0, 4>([&](auto ST_) {
        constexpr int q = ks - 1 - decltype(ST_)::value;
        if constexpr (q >= 0 && q % 3 != 2) gelu_st(IC<2>{}, IC<(q / 3) * 2 + q % 3>{}, ST_);
      });
      if constexpr (ks == 27) stats(IC<2>{});
      if constexpr (ks == 30) {
        const int ln = lane_id();
        const int dst = (ln >> 4) == ((ln >> 2) & 3) ? G * 1024 + (rw * 32 + (ln & 15)) * 8 : 2048 + (ln & 15) * 8;
#pragma unroll
        for (int u = 0; u < 2; ++u) {
          const float q = (ln & 2) ? ((ln & 1) ? ssq[u][3] : ssq[u][2]) : ((ln & 1) ? ssq[u][1] : ssq[u][0]);
          *(LAS f32x2*)(smem + AZ_ST + dst + u * 128) = (f32x2){ssum[u][0], q};
        }
      }
      if constexpr (ks == 33) {
        const int t8 = rw * 64 + lane_id();
        const float v = *(LAS float*)(smem + AZ_ST + t8 * 4) + *(LAS float*)(smem + AZ_ST + 1024 + t8 * 4);
        ST8(ws, t8 * 8, WS_STAT + (p * 12 + j) * 2048, ((u32x2){__float_as_uint(v), X.tag}));
      }
    }
    if constexpr (R >= 4 && ks >= 1 && ks < 9) {
      constexpr int e = (ks - 1) * 2, reg = (e & 3) + ((e >> 2) & 1) * 8 + (e >> 3) * 4;
      z1r[R - 4][ks - 1] = cvt_pk(acc[(R - 1) & 1][reg], acc[(R - 1) & 1][reg + 1]); PIN(z1r[R - 4][ks - 1]);
    }
    if constexpr (R == 4) {
      if constexpr (ks == 17) {
        const int t8 = rw * 64 + lane_id();
        asm volatile("" ::: "memory");
#pragma unroll
        for (int i = 0; i < 6; ++i) gv[i] = LD8(ws, t8 * 8 + i * 2048, WS_STAT + (p * 12 + 6 * G) * 2048);
        asm volatile("" ::: "memory");
      }
      if constexpr (ks == 30) {
        const int t8 = rw * 64 + lane_id();
        for (unsigned spins = 0; spins < SPIN_MAX; ++spins) {
          bool ok = true;
#pragma unroll
          for (int i = 0; i < 6; ++i) ok &= gv[i].y == X.tag;
          if (__builtin_amdgcn_ballot_w64(!ok) == 0) break;
          __builtin_amdgcn_s_sleep(1);
          asm volatile("" ::: "memory");
#pragma unroll
          for (int i = 0; i < 6; ++i) gv[i] = LD8(ws, t8 * 8 + i * 2048, WS_STAT + (p * 12 + 6 * G) * 2048);
        }
        float sum = 0.f;
#pragma unroll
        for (int i = 0; i < 6; ++i) sum += __uint_as_float(gv[i].x);
        *(LAS float*)(smem + AZ_ST2 + G * 1024 + t8 * 4) = sum;
      }
      if constexpr (ks == 33) {
        const int ln = lane_id();
        LAS float* a = (LAS float*)(smem + AZ_ST2 + rw * 256 + ln * 4);
        const float v = (a[0] + a[256]) * (1.0f / 2304.0f), o = dpp_get<0xB1>(v);
        const float m = (ln & 1) ? o : v, q = (ln & 1) ? v : o;
        const float sc = __builtin_amdgcn_rsqf(__builtin_fmaxf(q - m * m, 0.f) + 1e-5f);
        *(LAS float*)(smem + AZ_TAB + wid * 256 + ln * 4) = (ln & 1) ? -m * sc : sc;
      }
    }
    if constexpr (E >= 179 && E < 194) {
      constexpr int q = E - 179;
      sfor<0, 6>([&](auto CH_) {
        constexpr int ch = decltype(CH_)::value, cc = ch / 3, rr = ch % 3, q0 = 1 + 2 * ch + cc;
        if constexpr (q == q0 + 1 || q == q0 + 2) {
          constexpr int hf = q - q0 - 1;
#pragma unroll
          for (int i = 2 * hf; i < 2 * hf + 2; ++i) {
            unsigned t = nv[i];
            t = cvt_pk(__builtin_fmaf(bf_lo(t), tab[i][0], tab[i][1]), __builtin_fmaf(bf_hi(t), tab[i][2], tab[i][3]));
            PIN(t); pk[i] = t;
          }
          if constexpr (hf == 1) {
            const int ln = lane_id();
            const int tg = (3 * j + rr) * 4 + 2 * G + ((ln >> 4) & 1);
            const int c = tg < 128 ? tg >> 3 : tg - 128, ft = tg < 128 ? tg & 7 : 8;
            ST16(ws, (c * 64 * 9 + ft) * 1024 + (ln >> 5) * 512 + (ln & 15) * 16 + cc * 256, WS_Z2N + (p * 4 + rw) * 9216,
                 ((u32x4){pk[0], pk[1], pk[2], pk[3]}));
          }
        }
        if constexpr (rr == 0 && q == q0 - 1 + cc) {
          const int hb = (lane_id() >> 5) * 128;
#pragma unroll
          for (int i = 0; i < 4; ++i) tab[i] = *(LAS f32x4*)(smem + AZ_TAB + wid * 256 + hb + cc * 64 + i * 16);
        }
        if constexpr (q == q0) nv = *(LAS u32x4*)(park + rr * 16384 + cc * 512);
      });
    }
    SB0();
  });
  wait_v<0>(); BAR();
  lane = lane_id(); h = lane >> 5;
  if (wid == 0 && lane < 48 && f1_src(lane & 15, lane >> 4) == j) flag_st(P.ws, WS_F1 + (lane & 15) * 256 + ((lane >> 4) * 16 + p) * 4, X.tag);
  STAMP(0, 10);
  const unsigned zo = WS_Z1 + ((p * 8 + rw * 2) * 72 + 6 * j + G) * 1024;
  const unsigned zv = ((lane >> 4) & 1) * (72 * 1024) + h * 256 + (lane & 15) * 16;
  auto g1 = [&](auto RR_) {
    constexpr int RR = decltype(RR_)::value;
    u32x4 w[2];
#pragma unroll
    for (int i = 0; i < 8; ++i) {
      const int e = 2 * i, reg = (e & 3) + ((e >> 2) & 1) * 8 + (e >> 3) * 4;
      const float a = RR < 2 ? bf_lo(z1r[RR & 1][i]) : acc[1][reg], b = RR < 2 ? bf_hi(z1r[RR & 1][i]) : acc[1][reg + 1];
      w[i >> 2][i & 3] = cvt_pk(gelu_t(a), gelu_t(b));
    }
    PIN(w[0]); PIN(w[1]);
    return __builtin_shufflevector(w[0], w[1], 0, 1, 2, 3, 4, 5, 6, 7);
  };
  auto s1 = [&](int rr, auto w) {
    ST16(ws, zv, zo + rr * 2048, __builtin_shufflevector(w, w, 0, 1, 2, 3));
    ST16(ws, zv + 512, zo + rr * 2048, __builtin_shufflevector(w, w, 4, 5, 6, 7));
  };
  if (wid == 4) {
    Loader<0> L; L.init(P, X, smem);
    const unsigned fo = f1_off(X);
    const auto w0 = g1(IC<0>{});
    unsigned f = flag_ld(P.ws, fo);
    const auto w1 = g1(IC<1>{});
    const bool done = all_eq(f, X.tag);
    if (done) L.issue_b(0);
    const auto w2 = g1(IC<2>{});
    if (!done) { spin_flag(P.ws, fo, X.tag); L.issue_b(0); }
    L.issue_b(1); L.issue_b(2);
    s1(0, w0); s1(1, w1); s1(2, w2);
  } else if (wid == 5) {
    Loader<1> L; L.init(P, X, smem);
    L.issue_b(0); const auto w0 = g1(IC<0>{});
    L.issue_b(1); const auto w1 = g1(IC<1>{});
    L.issue_b(2); const auto w2 = g1(IC<2>{});
    s1(0, w0); s1(1, w1); s1(2, w2);
  } else {
    if (wid < 4) warm_wsp(P, X, smem);
    s1(0, g1(IC<0>{})); s1(1, g1(IC<1>{})); s1(2, g1(IC<2>{}));
  }
}

template <int G>
DI void a_qkvblock(const Params& P, const Ctx& X, LAS char* smem) {
  constexpr int NT = G ? 4 : 5, T0 = G ? 5 : 0, SPS = 2 * NT, NE = 54 * NT;
  const int wid = X.wid, p = X.pa, quad = X.quad;
  int lane = lane_id();
  const int rw = wid & 3;
  int kq = lane >> 4, i16 = lane & 15;
  const rsrc_t rsw = MKRS(P.wqkv, 576 * 3456);
  const brs_t ws = __builtin_amdgcn_make_buffer_rsrc(P.ws, 0, WS_END, 0x00020000);
  unsigned voff[3];
#pragma unroll
  for (int i = 0; i < 3; ++i) {
    const int pc = i < 2 ? (wid + 8 * i) * 64 + lane : 1024 + wid * 16 + (lane & 15), sr = pc / 18, ch = pc - sr * 18;
    voff[i] = (unsigned)((sr ^ (((sr >> 3) & 1) << 2)) * 3456 + ch * 16);
  }
  auto dma = [&](auto S_, auto I_) {
    constexpr int S = decltype(S_)::value, I = decltype(I_)::value;
    const unsigned so = quad * 288 + (S % 9) * 64 * 3456 + (S / 9) * 1152;
    LAS char* d = smem + (S % 6) * AQ_SLOT;
    if (I < 2) BLDS(rsw, voff[I], so, d + (wid + 8 * I) * 1024); else BLDS16(rsw, voff[I], so, d + 16384 + wid * 256);
  };
  bf16x8 xf[2][18];
  {
    LAS char* b0 = smem + A_XN + (rw * 32 + i16) * 128 + ((kq ^ (i16 >> 1)) * 16);
    LAS char* b1 = smem + A_XN + (rw * 32 + i16) * 128 + (((kq + 4) ^ (i16 >> 1)) * 16);
#pragma unroll
    for (int kb = 0; kb < 8; ++kb)
#pragma unroll
      for (int u = 0; u < 2; ++u) xf[u][kb] = *(LAS bf16x8*)(((kb & 1) ? b1 : b0) + u * 2048 + (kb >> 1) * 16384);
    WAIT_L0(); BAR();
    sfor<0, 4>([&](auto S_) { sfor<0, 3>([&](auto I_) { dma(S_, I_); }); });
#pragma unroll
    for (int kb = 8; kb < 18; ++kb)
#pragma unroll
      for (int u = 0; u < 2; ++u) xf[u][kb] = *(LAS bf16x8*)(((kb & 1) ? b1 : b0) + u * 2048 + (kb >> 1) * 16384);
  }
  STAMP(0, 3);
  const int rq = (lane >> 2) & 3;
  LAS char* rdA = smem + (8 * kq + 4 * (kq & 1) + rq) * 288 + (lane & 3) * 8 + T0 * 32;
  LAS char* rdB = smem + (8 * kq + 4 * ((kq & 1) ^ 1) + rq) * 288 + (lane & 3) * 8 + T0 * 32;
  f32x4 acc[NT][2];
  u32x4 prev[NT];
  bf16x8 wf[4];
  float pa = 0.f, pe = 0.f, pt = 0.f, pm = 0.f, pl = 0.f;
  auto rdw = [&](auto E_) {
    constexpr int E = decltype(E_)::value, S = E / SPS, o = (S % 6) * AQ_SLOT + ((E % SPS) / NT) * 9216 + (E % NT) * 32;
    wf[E % 4] = tr_frag(rdA + o, rdB + o);
  };
  wait_v<9>(); WAIT_L0(); BAR();
  sfor<0, 3>(rdw);
  sfor<0, NE>([&](auto E_) {
    constexpr int E = decltype(E_)::value, R = E / (18 * NT), er = E % (18 * NT), kb = er / NT, t = er % NT, EN = E + 3, SN = EN / SPS;
    if constexpr (EN < NE) {
      if constexpr (EN % SPS == 0) { wait_v<3 * (26 - SN < 3 ? 26 - SN : 3)>(); BAR(); }
      rdw(IC<EN>{});
      if constexpr (SN + 4 < 27 && EN % SPS >= 3 && EN % SPS < 6) dma(IC<SN + 4>{}, IC<EN % SPS - 3>{});
    }
    if constexpr (R >= 1 && kb == 0) {
      prev[t] = (u32x4){cvt_pk(acc[t][0][0], acc[t][0][1]), cvt_pk(acc[t][0][2], acc[t][0][3]),
                        cvt_pk(acc[t][1][0], acc[t][1][1]), cvt_pk(acc[t][1][2], acc[t][1][3])};
      PIN(prev[t]);
      if constexpr (R == 1) {
        const int ln = lane_id();
        const unsigned so = WS_PQ + (p * 8 + rw * 2) * 18432 + (quad * 9 + T0 + t) * 512;
        ST8(ws, ln * 8, so, ((u32x2){prev[t].x, prev[t].y}));
        ST8(ws, ln * 8, so + 18432, ((u32x2){prev[t].z, prev[t].w}));
      }
    }
    const f32x4 zz = {0, 0, 0, 0};
    if constexpr (R == 0) { acc[t][0] = MFMA(wf[E % 4], xf[0][kb], kb ? acc[t][0] : zz); acc[t][1] = MFMA(wf[E % 4], xf[1][kb], kb ? acc[t][1] : zz); }
    else { acc[t][0] = MFMA(xf[0][kb], wf[E % 4], kb ? acc[t][0] : zz); acc[t][1] = MFMA(xf[1][kb], wf[E % 4], kb ? acc[t][1] : zz); }
    PIN(acc[t][0]); PIN(acc[t][1]);
    if constexpr (R == 2 && er >= NT) {
      constexpr int q = er - NT, el = q / 2;
      if constexpr ((q & 1) == 0) {
        if constexpr (el < 8 * NT) {
          const unsigned src = prev[el / 8][(el & 7) / 2];
          pt = (el & 1) ? bf_hi(src) : bf_lo(src); pm = pt * 1.4426950409f; PIN(pt); PIN(pm);
        }
        if constexpr (el >= 1 && el - 1 < 8 * NT) {
          constexpr int e1 = el - 1, tl = e1 / 8, w = (e1 & 7) / 2;
          const float r = pa + pe;
          if constexpr ((e1 & 1) == 0) { pl = r; PIN(pl); }
          else { unsigned o = cvt_pk(pl, r); PIN(o); prev[tl][w] = o; }
          if constexpr ((e1 & 7) == 7) {
            const int ln = lane_id();
            *(LAS u32x4*)(smem + AQ_PK + ((T0 + tl) * 4 + rw) * 1024 + ln * 16) = prev[tl];
          }
        }
      } else if constexpr (el < 8 * NT) {
        pe = __builtin_amdgcn_fmed3f(__builtin_amdgcn_exp2f(pm), 0.f, 1.f); pa = __builtin_fmaxf(pt, 1e-6f); PIN(pe); PIN(pa);
      }
    }
    SB0();
  });
  STAMP(0, 9);
  lane = lane_id();
  unsigned f = 0;
  if (wid == 4) f = flag_ld(P.ws, f1_off(X));
#pragma unroll
  for (int t = 0; t < NT; ++t)
    *(LAS u32x4*)(smem + AQ_V + ((T0 + t) * 4 + rw) * 1024 + lane * 16) =
        (u32x4){cvt_pk(acc[t][0][0], acc[t][0][1]), cvt_pk(acc[t][0][2], acc[t][0][3]), cvt_pk(acc[t][1][0], acc[t][1][1]), cvt_pk(acc[t][1][2], acc[t][1][3])};
  WAIT_L0(); BAR();
  if (G == 0) warm_wsp(P, X, smem);
  else if (wid == 5) { Loader<1> L; L.init(P, X, smem); L.issue_b(0); }
  kq = lane >> 4; i16 = lane & 15;
  const bf16x8 ones = {0x3f80, 0x3f80, 0x3f80, 0x3f80, 0x3f80, 0x3f80, 0x3f80, 0x3f80};
#pragma unroll
  for (int it = 0; it < (G ? 1 : 2); ++it) {
    const int i = it ? 8 : wid;
    if (it == 0 || wid == 0) {
      bf16x8 pk[4];
#pragma unroll
      for (int k = 0; k < 4; ++k) pk[k] = *(LAS bf16x8*)(smem + AQ_PK + (i * 4 + k) * 1024 + lane * 16);
      f32x4 dz = {0, 0, 0, 0}, dv[3];
#pragma unroll
      for (int k = 0; k < 4; ++k) dz = MFMA(pk[k], ones, dz);
#pragma unroll
      for (int dj = 0; dj < 3; ++dj) {
        const int jv = i + dj - 1 < 0 ? 0 : i + dj - 1 > 8 ? 8 : i + dj - 1;
        dv[dj] = (f32x4){0, 0, 0, 0};
#pragma unroll
        for (int k = 0; k < 4; ++k) dv[dj] = MFMA(pk[k], *(LAS bf16x8*)(smem + AQ_V + (jv * 4 + k) * 1024 + lane * 16), dv[dj]);
      }
#pragma unroll
      for (int e = 0; e < 4; ++e) {
        const int kcol = 16 * i + 4 * kq + e, hk = (kcol * 57) >> 10;
        const int ob = (hk >> 1) * 2816 + ((hk & 1) * 342 + (kcol - 18 * hk) * 19) * 4;
        *(LAS float*)(smem + AQ_KV + (i16 == 0 ? ob + 72 : 11264 + lane * 4)) = dz[e];
#pragma unroll
        for (int dj = 0; dj < 3; ++dj) {
          const int ee = 16 * (i + dj - 1) + i16 - 18 * hk;
          *(LAS float*)(smem + AQ_KV + ((unsigned)ee < 18u ? ob + ee * 4 : 11264 + lane * 4)) = dv[dj][e];
        }
      }
    }
  }
  WAIT_L0(); BAR();
  if (G == 0) {
    u32x4 v[3];
#pragma unroll
    for (int i = 0; i < 3; ++i) v[i] = *(LAS u32x4*)(smem + AQ_KV + wid * 2816 + i * 1024 + lane * 16);
    const unsigned so = WS_KVP + ((quad * 4 + wid) * 16 + p) * 2816;
    ST16(ws, lane * 16, so, v[0]); ST16(ws, lane * 16 + 1024, so, v[1]);
    ST16(ws, lane < 48 ? lane * 16 + 2048 : 0x7fffff00u, so, v[2]);
  } else if (wid == 4) {
    Loader<0> L; L.init(P, X, smem);
    if (!all_eq(f, X.tag)) spin_flag(P.ws, f1_off(X), X.tag);
    L.issue_b(0); L.issue_b(1); L.issue_b(2);
  } else if (wid == 5) {
    Loader<1> L; L.init(P, X, smem); L.issue_b(1); L.issue_b(2);
  }
}

template <int ROLE>
DI void bc_loader(const Params& P, const Ctx& X, LAS char* smem) {
  constexpr int CNT = ROLE ? 16 : 18;
  __builtin_amdgcn_s_setprio(3);
  Loader<ROLE> L; L.init(P, X, smem);
  const int lane = lane_id(), p = X.p, c = X.c;
#pragma unroll 1
  for (int n = 0; n < 29; ++n) {
    if (n < 3 && X.jj < 3) wait_v<2 * CNT + 6>(); else wait_v<2 * CNT>();
    BAR();
    L.issue_b(n + 3);
  }
  wait_v<2 * CNT>(); BAR(); wait_v<CNT>(); BAR(); wait_v<0>(); BAR();
  if (ROLE == 0) spin_flag(P.ws, WS_F3W + (c & 7) * 128 + (lane & 31) * 4, X.tag);
  else spin_flag(P.ws, WS_F3O + p * 128 + (lane & 15) * 4, X.tag);
  L.template issue_c<2>(32); L.template issue_c<4>(33); L.template issue_c<4>(34);
  int n = 32;
  if (ROLE == 1) {
    wait_v<2 * CNT>(); BAR(); L.template issue_c<4>(35);
    wait_v<2 * CNT>(); BAR(); L.template issue_c<4>(36);
    wait_v<2 * CNT>(); BAR(); wait_v<CNT>(); BAR(); wait_v<0>(); BAR();
    STAMP(2, 5);
    spin_flag(P.ws, WS_F4 + (2 * p + (c >> 3)) * 128 + (lane & 31) * 4, X.tag);
    STAMP(2, 6);
    L.template issue_c<4>(37); L.template issue_c<4>(38); L.template issue_c<4>(39);
    n = 37;
  }
#pragma unroll 1
  for (; n < 52; ++n) { wait_v<2 * CNT>(); BAR(); L.template issue_c<4>(n + 3); }
  wait_v<2 * CNT>(); BAR(); wait_v<CNT>(); BAR(); wait_v<0>(); BAR();
}

DI void bc_side(const Params& P, const Ctx& X, LAS char* smem) {
  __builtin_amdgcn_s_setprio(2);
  const brs_t ws = __builtin_amdgcn_make_buffer_rsrc(P.ws, 0, WS_END, 0x00020000);
  const int lane = lane_id();
  const int sw = X.wid - 6, lt = sw * 64 + lane, p = X.p, c = X.c, kq = lane >> 4, r16 = lane & 15;
  u32x4 part[2][4]; u32x2 qv[4][3]; f32x4 sum[2]; int dst[8]; bf16x8 kvf[3][4];
  const int pk0 = (p * 2 + (c >> 3)) * 2 + sw;
  const unsigned oob = 0x7fffff00u;
  auto pack_ld = [&](int kbr, LAS char* pbuf) {
    const int kb = kbr < 90 ? kbr : 0;
    const rsrc_t rs = kb < 72 ? MKRS(P.wout, 2304 * 1152) : MKRS(P.wo, 576 * 1152);
#pragma unroll
    for (int i = 0; i < 5; ++i) {
      const int pc = i * 64 + lane, k = pc < 288 ? (pc * 57) >> 9 : 31, ch = pc < 288 ? pc - k * 9 : 8;
      const int q = k >> 3, u = (k >> 2) & 1, e = k & 3;
      const int row = kb < 64 ? 32 * kb + 16 * u + 4 * q + e : kb < 72 ? 32 * kb + k : kb < 88 ? 36 * (kb - 72) + 16 * u + 4 * q + e
                                                                                            : 36 * (8 * (kb - 88) + 2 * q + u) + 32 + e;
      BLDS(rs, row * 1152 + ch * 16, (c & 7) * 144, pbuf + i * 1024);
    }
  };
  auto pack_st = [&](int kb, LAS char* pbuf) {
    LAS char* a = pbuf + (8 * kq + ((lane >> 2) & 3)) * 144;
    const unsigned so = WS_WX + ((c & 7) * 90 + kb) * 4608;
#pragma unroll
    for (int dt = 0; dt < 4; ++dt) {
      LAS char* b = a + (dt >> 1) * 64 + (lane & 3) * 16 + (dt & 1) * 8;
      ST16(ws, lane * 16 + dt * 1024, so, __builtin_bit_cast(u32x4, tr_frag(b, b + 576)));
    }
    LAS char* b = a + 128 + (lane & 1) * 8;
    ST16(ws, r16 < 8 ? 4096 + kq * 128 + r16 * 16 : oob, so, __builtin_bit_cast(u32x4, tr_frag(b, b + 576)));
  };
  LAS char* pb = smem + B_KV + sw * 10240;
  const unsigned f2 = kq == 0 ? WS_F2Q + (c >> 2) * 128 + r16 * 4 : WS_F2Z + p * 128 + f1_src(c, kq - 1) * 4;
  unsigned fl = 0;
  sfor<0, 32>([&](auto N_) {
    constexpr int N = decltype(N_)::value;
    if constexpr (N == 3) wait_v<10>();
    if constexpr (N == 5 || N == 27) wait_v<0>();
    if constexpr (N == 10) { if (!all_eq(fl, X.tag)) spin_flag(P.ws, f2, X.tag); }
    if constexpr (N == 9 || N == 20) WAIT_L0();
    BAR();
    STAMP(1, N);
    if constexpr (N == 0) pack_ld(pk0, pb);
    if constexpr (N == 1) pack_ld(pk0 + 64, pb + 5120);
    if constexpr (N == 3) {
      if (lt == 0) flag_st(P.ws, X.jj < 3 ? WS_F2Z + X.pa * 128 + (X.quad * 3 + X.jj) * 4 : WS_F2Q + X.quad * 128 + X.pa * 4, X.tag);
    }
    if constexpr (N == 5) pack_st(pk0, pb);
    if constexpr (N == 6) { if (pk0 < 26) pack_st(pk0 + 64, pb + 5120); }
    if constexpr (N == 7) fl = flag_ld(P.ws, f2);
    if constexpr (N == 8) {
#pragma unroll
      for (int i = 0; i < 4; ++i) *(LAS u32x4*)(smem + B_KV + i * 2048 + lt * 16) = (u32x4){0, 0, 0, 0};
    }
    if constexpr (N >= 12 && N <= 19) {
#pragma unroll
      for (int i = 0; i < 4; ++i) {
        const f32x4 v = __builtin_bit_cast(f32x4, part[N & 1][i]);
        if (N == 12 && i < 2) sum[i] = v; else sum[i & 1] += v;
      }
      PIN(sum[0]); PIN(sum[1]);
    }
    if constexpr (N >= 10 && N <= 17) {
#pragma unroll
      for (int i = 0; i < 4; ++i)
        part[N & 1][i] = LD16(ws, (i & 1) ? (lt < 43 ? lt + 128 : 170) * 16 : lt * 16, WS_KVP + (c * 16 + 2 * (N - 10) + (i >> 1)) * 2816);
    }
    if constexpr (N >= 12 && N <= 15) {
      constexpr int tt = N - 12;
      const unsigned so = WS_PQ + (p * 8 + sw * 4 + tt) * 18432 + c * 1152;
      qv[tt][0] = LD8(ws, kq * 256 + r16 * 8, so); qv[tt][1] = LD8(ws, kq * 256 + 128 + r16 * 8, so); qv[tt][2] = LD8(ws, 1024 + r16 * 8, so);
    }
    if constexpr (N >= 15 && N <= 18) {
      constexpr int tt = N - 15;
#pragma unroll
      for (int i = 0; i < 3; ++i) {
        qv[tt][i] = (u32x2){cvt_pk(phi_f(bf_lo(qv[tt][i].x)), phi_f(bf_hi(qv[tt][i].x))), cvt_pk(phi_f(bf_lo(qv[tt][i].y)), phi_f(bf_hi(qv[tt][i].y)))};
        PIN(qv[tt][i]);
      }
    }
    if constexpr (N == 2 || N == 3 || N == 4 || N == 9) {
      constexpr int e0 = N == 9 ? 6 : 2 * (N - 2);
#pragma unroll
      for (int e8 = e0; e8 < e0 + 2; ++e8) {
        const int id = (lt + 128 * (e8 >> 2)) * 4 + (e8 & 3), hh = id >= 342, rem = id - hh * 342, d = (rem * 1725) >> 15, e = rem - d * 19;
        const int k = 18 * hh + d, n = 18 * hh + e;
        const int kof = (k >> 5) * 1024 + ((k >> 3) & 3) * 256 + (k & 7) * 2;
        dst[e8] = id >= 684 ? 8192 + lane * 2 : (e < 18 ? (n >> 4) * 2048 + (n & 15) * 16 : 6144 + hh * 16) + kof;
        PIN(dst[e8]);
      }
    }
    if constexpr (N == 19) {
#pragma unroll
      for (int e8 = 0; e8 < 8; ++e8) *(LAS u16*)(smem + B_KV + dst[e8]) = (u16)(cvt_pk(sum[e8 >> 2][e8 & 3], 0.f) & 0xffffu);
    }
    if constexpr (N == 20) {
#pragma unroll
      for (int nt = 0; nt < 3; ++nt) {
        LAS char* kb = smem + B_KV + nt * 2048 + lane * 16;
        LAS char* zb = smem + B_KV + 6144 + kq * 256 + (nt * 16 + r16 >= 18 ? 16 : 0);
        kvf[nt][0] = *(LAS bf16x8*)kb; kvf[nt][1] = *(LAS bf16x8*)(kb + 1024);
        kvf[nt][2] = *(LAS bf16x8*)zb; kvf[nt][3] = *(LAS bf16x8*)(zb + 1024);
      }
    }
    if constexpr (N >= 21 && N <= 24) {
      constexpr int tt = N - 21;
      const bf16x8 q0 = __builtin_bit_cast(bf16x8, ((u32x4){qv[tt][0].x, qv[tt][0].y, qv[tt][1].x, qv[tt][1].y}));
      const u32x4 q1v = kq == 0 ? (u32x4){qv[tt][2].x, qv[tt][2].y, 0u, 0u} : (u32x4){0u, 0u, 0u, 0u};
      const bf16x8 q1 = __builtin_bit_cast(bf16x8, q1v);
      u32x2 o[3];
#pragma unroll
      for (int nt = 0; nt < 3; ++nt) {
        const f32x4 z = {0, 0, 0, 0};
        const f32x4 num = MFMA(kvf[nt][1], q1, MFMA(kvf[nt][0], q0, z));
        const f32x4 den = MFMA(kvf[nt][3], q1, MFMA(kvf[nt][2], q0, z));
        float r[4];
#pragma unroll
        for (int e = 0; e < 4; ++e) r[e] = num[e] * __builtin_amdgcn_rcpf(den[e] + 1e-6f);
        o[nt] = (u32x2){cvt_pk(r[0], r[1]), cvt_pk(r[2], r[3])};
      }
      const unsigned so = WS_HX + (p * 8 + sw * 4 + tt) * (90 * 1024);
      ST16(ws, lane * 16, so + (72 + c) * 1024, ((u32x4){o[0].x, o[0].y, o[1].x, o[1].y}));
      ST8(ws, kq == 0 ? r16 * 16 : oob, so + (88 + (c >> 3)) * 1024 + ((c & 7) >> 1) * 256 + (c & 1) * 8, o[2]);
    }
    if constexpr (N == 27) { if (lt < 2) flag_st(P.ws, lt ? WS_F3W + (c & 7) * 128 + (p * 2 + (c >> 3)) * 4 : WS_F3O + p * 128 + c * 4, X.tag); }
    SB0();
  });
  if (X.bid == 0 && sw == 0) {
#pragma unroll 1
    for (int n = 32; n < 42; ++n) BAR();
    unsigned f4[4];
#pragma unroll
    for (int i = 0; i < 4; ++i) f4[i] = flag_ld(P.ws, WS_F3O + (i * 4 + kq) * 128 + r16 * 4);
    BAR(); BAR(); BAR();
    if (!(all_eq(f4[0], X.tag) && all_eq(f4[1], X.tag) && all_eq(f4[2], X.tag) && all_eq(f4[3], X.tag))) {
#pragma unroll 1
      for (int i = 0; i < 4; ++i) spin_flag(P.ws, WS_F3O + (i * 4 + kq) * 128 + r16 * 4, X.tag);
    }
    if (lane == 0) flag_st(P.ws, WS_EPOCH, X.tag);
  }
}

template <int DH>
DI void c_compute(const Params& P, const Ctx& X, LAS char* smem) {
  constexpr int ND = DH ? 2 : 3;
  int lane = lane_id();
  const int r16 = lane & 15, kq = lane >> 4, th = X.wid & 1;
  const int rg = 2 * X.p + (X.c >> 3), cg = X.c & 7;
  f32x4 acc[ND][2];
#pragma unroll
  for (int a = 0; a < ND; ++a) { acc[a][0] = (f32x4){0, 0, 0, 0}; acc[a][1] = (f32x4){0, 0, 0, 0}; }
  const int woff = DH * 2048 + lane * 16, woff4 = 4096 + kq * 128 + (r16 & 7) * 16;
  const int hoff = 18432 + th * 8192 + lane * 16;
  bf16x8 wa[2][ND], ha[2][2], wb[2][ND], hb[2][2];
#define C_RD(W, H, slot, kp) do { LAS char* b_ = smem + (slot) * STG; \
    _Pragma("unroll") for (int k = 0; k < 2; ++k) { \
      H[k][0] = *(LAS bf16x8*)(b_ + hoff + ((kp) * 2 + k) * 1024); H[k][1] = *(LAS bf16x8*)(b_ + hoff + 4096 + ((kp) * 2 + k) * 1024); \
      W[k][0] = *(LAS bf16x8*)(b_ + woff + ((kp) * 2 + k) * 4608); W[k][1] = *(LAS bf16x8*)(b_ + woff + 1024 + ((kp) * 2 + k) * 4608); \
      if (ND == 3) W[k][ND - 1] = *(LAS bf16x8*)(b_ + woff4 + ((kp) * 2 + k) * 4608); } } while (0)
#define C_MM(W, H) do { _Pragma("unroll") for (int k = 0; k < 2; ++k) _Pragma("unroll") for (int i = 0; i < ND; ++i) { \
    acc[i][0] = MFMA(W[k][i], H[k][0], acc[i][0]); acc[i][1] = MFMA(W[k][i], H[k][1], acc[i][1]); } } while (0)
#define C_ILV() do { _Pragma("unroll") for (int i_ = 0; i_ < ND + 2; ++i_) { SGB(0x100, 2); SGB(0x8, 1); } SGB(0x8, 3 * ND - 2); } while (0)
#define C_STAGE(n) do { WAIT_L0(); BAR(); const int slot = (n) & 3; \
    C_RD(wa, ha, slot, 0); C_MM(wb, hb); C_ILV(); SB0(); \
    C_RD(wb, hb, slot, 1); C_MM(wa, ha); C_ILV(); SB0(); } while (0)
  BAR();
  C_RD(wb, hb, 0, 0);
  C_STAGE(33);
  wait_v<0>();
  if (lane == 0) flag_st(P.ws, WS_F4 + (X.p * 2 + (X.wid >> 1)) * 128 + (X.c * 2 + (X.wid & 1)) * 4, X.tag);
  STAMP(2, 1);
  u32x4 xr[2]; u32x2 xo[2]; size_t orow[2];
#pragma unroll
  for (int tt = 0; tt < 2; ++tt) {
    orow[tt] = (size_t)(rg * 64 + slot_row(th * 32 + tt * 16 + r16)) * 1152 + cg * 144;
    xr[tt] = *(const u32x4*)((const char*)P.x + orow[tt] + DH * 64 + kq * 16);
    if (DH == 0) xo[tt] = *(const u32x2*)((const char*)P.x + orow[tt] + 128 + (kq & 1) * 8);
  }
#pragma unroll 1
  for (int n = 34; n < 55; ++n) C_STAGE(n);
  C_MM(wb, hb);
  SB0();
  STAMP(2, 2);
#pragma unroll
  for (int tt = 0; tt < 2; ++tt) {
    const u32x4 x = xr[tt]; const f32x4 a0 = acc[0][tt], a1 = acc[1][tt];
    u32x4 v;
    v.x = cvt_pk(bf_lo(x.x) + a0[0], bf_hi(x.x) + a0[1]); v.y = cvt_pk(bf_lo(x.y) + a0[2], bf_hi(x.y) + a0[3]);
    v.z = cvt_pk(bf_lo(x.z) + a1[0], bf_hi(x.z) + a1[1]); v.w = cvt_pk(bf_lo(x.w) + a1[2], bf_hi(x.w) + a1[3]);
    *(u32x4*)((char*)P.out + orow[tt] + DH * 64 + kq * 16) = v;
    if (DH == 0) {
      const f32x4 a2 = acc[ND - 1][tt];
      u32x2 w; w.x = cvt_pk(bf_lo(xo[tt].x) + a2[0], bf_hi(xo[tt].x) + a2[1]); w.y = cvt_pk(bf_lo(xo[tt].y) + a2[2], bf_hi(xo[tt].y) + a2[3]);
      if (kq < 2) *(u32x2*)((char*)P.out + orow[tt] + 128 + kq * 8) = w;
    }
  }
  STAMP(2, 3);
}

DI void b_compute(const Params& P, const Ctx& X, LAS char* smem) {
  int lane = lane_id();
  const int wid = X.wid, p = X.p, c = X.c, r16 = lane & 15, kq = lane >> 4;
  const brs_t ws = __builtin_amdgcn_make_buffer_rsrc(P.ws, 0, WS_END, 0x00020000);
  f32x4 acc[9][2];
#pragma unroll
  for (int a = 0; a < 9; ++a) { acc[a][0] = (f32x4){0, 0, 0, 0}; acc[a][1] = (f32x4){0, 0, 0, 0}; }
  const int zoff = lane * 16;
  const int woff0 = 18432 + (wid * 32 + r16) * 128 + ((kq ^ (r16 >> 1)) * 16);
  const int woff1 = 18432 + (wid * 32 + r16) * 128 + (((kq + 4) ^ (r16 >> 1)) * 16);
  bf16x8 za[9], wa[2], zb[9], wb[2];
#define B_RD(Z, W, slot, kb) do { LAS char* b_ = smem + (slot) * STG; \
    W[0] = *(LAS bf16x8*)(b_ + ((kb) ? woff1 : woff0)); W[1] = *(LAS bf16x8*)(b_ + ((kb) ? woff1 : woff0) + 2048); \
    _Pragma("unroll") for (int i = 0; i < 9; ++i) Z[i] = *(LAS bf16x8*)(b_ + zoff + (kb) * 9216 + i * 1024); } while (0)
#define B_MM(Z, W) do { _Pragma("unroll") for (int i = 0; i < 9; ++i) { \
    acc[i][i & 1] = MFMA(Z[i], W[i & 1], acc[i][i & 1]); acc[i][(i & 1) ^ 1] = MFMA(Z[i], W[(i & 1) ^ 1], acc[i][(i & 1) ^ 1]); } } while (0)
#define B_ILV() do { _Pragma("unroll") for (int i_ = 0; i_ < 11; ++i_) { SGB(0x100, 1); SGB(0x8, 1); } SGB(0x8, 7); } while (0)
#define B_STAGE(n) do { WAIT_L0(); BAR(); const int slot = (n) & 3; \
    B_RD(za, wa, slot, 0); B_MM(zb, wb); B_ILV(); SB0(); \
    B_RD(zb, wb, slot, 1); B_MM(za, wa); B_ILV(); SB0(); } while (0)
  BAR();
  STAMP(1, 1);
  B_RD(za, wa, 0, 0);
  B_RD(zb, wb, 0, 1);
  B_MM(za, wa);
#pragma unroll 1
  for (int n = 1; n < 18; ++n) {
    if (n == 3) wait_v<0>();
    B_STAGE(n);
  }
  u32x4 z1[2][4]; u32x2 z1o[2]; u16 bsv[2];
  sfor<18, 32>([&](auto N_) {
    constexpr int N = decltype(N_)::value, tt = (N - 18) / 6, m = (N - 18) % 6;
    if constexpr (N < 30) {
      const unsigned so = WS_Z1 + (p * 8 + wid * 2 + tt) * (72 * 1024);
      if constexpr (m < 4) z1[tt][m] = LD16(ws, zoff + m * 1024, so + c * 4096);
      else if constexpr (m == 4) z1o[tt] = LD8(ws, zoff, so + (64 + (c >> 1)) * 1024 + (c & 1) * 8);
      else bsv[tt] = P.bsp[p * 128 + slot_row(wid * 32 + tt * 16 + r16)];
      SB0();
    }
    B_STAGE(N);
  });
  B_MM(zb, wb);
  SB0();
  STAMP(1, 2);
#pragma unroll
  for (int tt = 0; tt < 2; ++tt) {
    const float b = bf_lo(bsv[tt]);
    const unsigned so = WS_HX + (p * 8 + wid * 2 + tt) * (90 * 1024);
#pragma unroll
    for (int m = 0; m < 4; ++m) {
      const u32x4 z = z1[tt][m]; const f32x4 g0 = acc[2 * m][tt] + b, g1 = acc[2 * m + 1][tt] + b;
      u32x4 v;
      v.x = cvt_pk(bf_lo(z.x) * g0[0], bf_hi(z.x) * g0[1]); v.y = cvt_pk(bf_lo(z.y) * g0[2], bf_hi(z.y) * g0[3]);
      v.z = cvt_pk(bf_lo(z.z) * g1[0], bf_hi(z.z) * g1[1]); v.w = cvt_pk(bf_lo(z.w) * g1[2], bf_hi(z.w) * g1[3]);
      ST16(ws, lane * 16 + m * 1024, so + c * 4096, v);
    }
    const u32x2 z = z1o[tt]; const f32x4 g0 = acc[8][tt] + b;
    u32x2 v; v.x = cvt_pk(bf_lo(z.x) * g0[0], bf_hi(z.x) * g0[1]); v.y = cvt_pk(bf_lo(z.y) * g0[2], bf_hi(z.y) * g0[3]);
    ST8(ws, (kq >> 1) * 256 + r16 * 16 + (kq & 1) * 8, so + (64 + (c >> 1)) * 1024 + (c & 1) * 512, v);
  }
  STAMP(1, 3);
}

__global__ NOPK __launch_bounds__(512, 2) void kF(const Params P) {
  extern __shared__ __attribute__((aligned(16))) char smem_[];
  LAS char* smem = (LAS char*)smem_;
  Ctx X;
  X.wid = __builtin_amdgcn_readfirstlane(threadIdx.x >> 6); X.bid = blockIdx.x;
  const int xcd = X.bid & 7, idx = X.bid >> 3, wid = X.wid, lane = threadIdx.x & 63;
  X.pa = (xcd >> 2) * 8 + (idx >> 2); X.quad = xcd & 3; X.jj = idx & 3;
  X.p = (xcd >> 1) * 4 + (idx >> 3); X.c = (xcd & 1) * 8 + (idx & 7);
  STAMP(0, 0);
  const int sl0 = ((wid + 2 * X.jj) & 7) * 16 + (lane >> 3);
  u32x4 xr[2][9];
#pragma unroll
  for (int ln = 0; ln < 9; ++ln)
#pragma unroll
    for (int hh = 0; hh < 2; ++hh)
      xr[hh][ln] = *(const u32x4*)((const char*)P.x + (size_t)(X.pa * 128 + slot_row(sl0 + hh * 8)) * 1152 + (lane & 7) * 16 + ln * 128);
  const unsigned ep = flag_ld(P.ws, WS_EPOCH);
  SB0();
  float rs_[2], sh_[2];
  {
    const bf16x2_t one = {(__bf16)1.0f, (__bf16)1.0f};
    float s[2] = {0.f, 0.f}, q[2] = {0.f, 0.f};
#pragma unroll
    for (int ln = 0; ln < 9; ++ln)
#pragma unroll
      for (int i = 0; i < 4; ++i)
#pragma unroll
        for (int hh = 0; hh < 2; ++hh) {
          const unsigned ua = xr[hh][ln][i];
          const bf16x2_t a = __builtin_bit_cast(bf16x2_t, ua);
          s[hh] = __builtin_amdgcn_fdot2_f32_bf16(a, one, s[hh], false); q[hh] = __builtin_amdgcn_fdot2_f32_bf16(a, a, q[hh], false);
        }
#pragma unroll
    for (int hh = 0; hh < 2; ++hh) {
      const float m = oct_sum(s[hh]) * (1.0f / 576.0f);
      const float var = __builtin_fmaxf(oct_sum(q[hh]) * (1.0f / 576.0f) - m * m, 0.f);
      rs_[hh] = __builtin_amdgcn_rsqf(var + 1e-5f); sh_[hh] = -m * rs_[hh];
      PIN(rs_[hh]); PIN(sh_[hh]);
    }
  }
  X.tag = __builtin_amdgcn_readfirstlane(ep) + 1u;
  STAMP(0, 1);
  {
    const int r0 = ((idx >> 2) & 7) * 72;
    if (X.jj < 3) {
      if (wid < 6) {
        const rsrc_t rs = MKRS(P.win, 576 * 9216);
        const int col = (wid < 3 ? 2304 + (3 * (X.quad * 3 + X.jj) + wid) * 64 : (3 * (X.quad * 3 + X.jj) + wid - 3) * 64) * 2;
        TOUCH(rs, lane * 9216, r0 * 9216 + col, smem + L_SCR);
        if (lane < 8) TOUCH(rs, lane * 9216, (r0 + 64) * 9216 + col, smem + L_SCR);
      }
    } else {
      const rsrc_t rs = MKRS(P.wqkv, 576 * 3456);
      for (int i = wid; i < 9; i += 8) {
        const int col = (i / 3) * 1152 + (X.quad * 2 + i % 3) * 128;
        TOUCH(rs, lane * 3456, r0 * 3456 + col, smem + L_SCR);
        if (lane < 8) TOUCH(rs, lane * 3456, (r0 + 64) * 3456 + col, smem + L_SCR);
      }
    }
  }
  SB0();
#pragma unroll
  for (int ln = 0; ln < 9; ++ln)
#pragma unroll
    for (int hh = 0; hh < 2; ++hh) {
      u32x4 v = xr[hh][ln];
#pragma unroll
      for (int i = 0; i < 4; ++i) v[i] = cvt_pk(__builtin_fmaf(bf_lo(v[i]), rs_[hh], sh_[hh]), __builtin_fmaf(bf_hi(v[i]), rs_[hh], sh_[hh]));
      const int sl = sl0 + hh * 8;
      *(LAS u32x4*)(smem + A_XN + ln * 16384 + sl * 128 + (((lane & 7) ^ ((sl >> 1) & 7)) * 16)) = v;
      SB0();
    }
  WAIT_L0(); BAR();
  STAMP(0, 2);
  if (X.jj < 3) a_zblock(P, X, smem);
  else if (wid < 4) a_qkvblock<0>(P, X, smem);
  else a_qkvblock<1>(P, X, smem);
  STAMP(0, 11);
  if (wid < 4) {
    b_compute(P, X, smem);
    if (wid < 2) c_compute<0>(P, X, smem); else c_compute<1>(P, X, smem);
  } else if (wid == 4) bc_loader<0>(P, X, smem);
  else if (wid == 5) bc_loader<1>(P, X, smem);
  else bc_side(P, X, smem);
}

extern "C" void kernel_launch(void* const* d_in, const int* in_sizes, int n_in, void* d_out, int out_size, void* d_ws,
                              size_t ws_size, hipStream_t stream) {
  Params P;
  P.x = (const u16*)d_in[0]; P.wqkv = (const u16*)d_in[1]; P.wo = (const u16*)d_in[2]; P.win = (const u16*)d_in[4];
  P.wsp = (const u16*)d_in[5]; P.bsp = (const u16*)d_in[6]; P.wout = (const u16*)d_in[7];
  P.out = (u16*)d_out; P.ws = (char*)d_ws;
  (void)hipFuncSetAttribute((const void*)kF, hipFuncAttributeMaxDynamicSharedMemorySize, LDS_BYTES);
  kF<<<256, 512, LDS_BYTES, stream>>>(P);
}
```

```cpp
#include <hip/hip_runtime.h>
#include <hip/hip_bf16.h>

#if defined(__HIP_DEVICE_COMPILE__)
#define NOPK __attribute__((target("no-packed-fp32-ops")))
#else
#define NOPK
#endif
#define LAS __attribute__((address_space(3)))
#define GAS __attribute__((address_space(1)))
typedef unsigned short u16;
typedef unsigned long long u64;
typedef short bf16x8 __attribute__((ext_vector_type(8)));
typedef short s16x4 __attribute__((ext_vector_type(4)));
typedef float f32x4 __attribute__((ext_vector_type(4)));
typedef float f32x2 __attribute__((ext_vector_type(2)));
typedef float f32x16 __attribute__((ext_vector_type(16)));
typedef unsigned u32x4 __attribute__((ext_vector_type(4)));
typedef unsigned u32x2 __attribute__((ext_vector_type(2)));
typedef __bf16 bf16x2_t __attribute__((ext_vector_type(2)));
typedef __amdgpu_buffer_rsrc_t brs_t;
typedef u32x4 rsrc_t;

#define WAIT_L0() asm volatile("s_waitcnt lgkmcnt(0)" ::: "memory")
#define BAR() do { asm volatile("" ::: "memory"); __builtin_amdgcn_s_barrier(); asm volatile("" ::: "memory"); } while (0)
#define SB0() __builtin_amdgcn_sched_barrier(0)
#define SGB(mask, n) __builtin_amdgcn_sched_group_barrier(mask, n, 0)
#define PIN(x) asm volatile("" : "+v"(x))
#define MFMA(a, b, c) __builtin_amdgcn_mfma_f32_16x16x32_bf16(a, b, c, 0, 0, 0)
#define MFMA32(a, b, c) __builtin_amdgcn_mfma_f32_32x32x16_bf16(a, b, c, 0, 0, 0)
#define DI __device__ __forceinline__
template <int N> DI void wait_v() { asm volatile("s_waitcnt vmcnt(%0)" :: "i"(N) : "memory"); }
template <int V> struct IC { static constexpr int value = V; };
template <int I, int N, class F> DI void sfor(F&& f) {
  if constexpr (I < N) { f(IC<I>{}); sfor<I + 1, N>(f); }
}

DI rsrc_t MKRS(const void* p, unsigned bytes) {
  const u64 a = (u64)p;
  return (rsrc_t){(unsigned)a, (unsigned)(a >> 32) & 0xffffu, bytes, 0x00020000u};
}
DI void BLDS(rsrc_t rs, unsigned voff, unsigned soff, LAS char* l) {
  asm volatile("s_mov_b32 m0, %3\n\ts_nop 0\n\tbuffer_load_dwordx4 %0, %1, %2 offen lds"
               :: "v"(voff), "s"(rs), "s"(soff), "s"((unsigned)(size_t)l) : "memory");
}
DI void BLDS1(rsrc_t rs, unsigned voff, unsigned soff, LAS char* l) {
  asm volatile("s_mov_b32 m0, %3\n\ts_nop 0\n\tbuffer_load_dwordx4 %0, %1, %2 offen sc1 lds"
               :: "v"(voff), "s"(rs), "s"(soff), "s"((unsigned)(size_t)l) : "memory");
}
template <int N> DI void BLDSx(rsrc_t rs, unsigned voff, unsigned soff, LAS char* l) {
  if (N == 4)
    asm volatile("s_mov_b32 m0, %3\n\ts_nop 0\n\tbuffer_load_dwordx4 %0, %1, %2 offen sc1 lds\n\tbuffer_load_dwordx4 %0, %1, %2 offen offset:1024 sc1 lds\n\t"
                 "buffer_load_dwordx4 %0, %1, %2 offen offset:2048 sc1 lds\n\tbuffer_load_dwordx4 %0, %1, %2 offen offset:3072 sc1 lds"
                 :: "v"(voff), "s"(rs), "s"(soff), "s"((unsigned)(size_t)l) : "memory");
  else if (N == 2)
    asm volatile("s_mov_b32 m0, %3\n\ts_nop 0\n\tbuffer_load_dwordx4 %0, %1, %2 offen sc1 lds\n\tbuffer_load_dwordx4 %0, %1, %2 offen offset:1024 sc1 lds"
                 :: "v"(voff), "s"(rs), "s"(soff), "s"((unsigned)(size_t)l) : "memory");
  else
    asm volatile("s_mov_b32 m0, %3\n\ts_nop 0\n\tbuffer_load_dwordx4 %0, %1, %2 offen sc1 lds"
                 :: "v"(voff), "s"(rs), "s"(soff), "s"((unsigned)(size_t)l) : "memory");
}
DI void BLDS4v(rsrc_t rs, const unsigned (&v)[4], unsigned soff, LAS char* l) {
  asm volatile("s_mov_b32 m0, %6\n\ts_nop 0\n\tbuffer_load_dwordx4 %0, %4, %5 offen lds\n\tbuffer_load_dwordx4 %1, %4, %5 offen offset:1024 lds\n\t"
               "buffer_load_dwordx4 %2, %4, %5 offen offset:2048 lds\n\tbuffer_load_dwordx4 %3, %4, %5 offen offset:3072 lds"
               :: "v"(v[0]), "v"(v[1]), "v"(v[2]), "v"(v[3]), "s"(rs), "s"(soff), "s"((unsigned)(size_t)l) : "memory");
}
DI void BLDS32(rsrc_t rs, unsigned voff, unsigned soff, LAS char* l) {
  asm volatile("s_mov_b32 m0, %3\n\ts_mov_b32 exec_hi, 0\n\tbuffer_load_dwordx4 %0, %1, %2 offen lds\n\ts_mov_b32 exec_hi, -1"
               :: "v"(voff), "s"(rs), "s"(soff), "s"((unsigned)(size_t)l) : "memory");
}
DI void BLDS3v(rsrc_t rs, const unsigned (&v)[3], unsigned soff, LAS char* l) {
  asm volatile("s_mov_b32 m0, %5\n\ts_nop 0\n\tbuffer_load_dwordx4 %0, %3, %4 offen lds\n\tbuffer_load_dwordx4 %1, %3, %4 offen offset:1024 lds\n\t"
               "buffer_load_dwordx4 %2, %3, %4 offen offset:2048 lds"
               :: "v"(v[0]), "v"(v[1]), "v"(v[2]), "s"(rs), "s"(soff), "s"((unsigned)(size_t)l) : "memory");
}
DI void BLDS15v(rsrc_t rs, const unsigned (&v)[3], unsigned soff, LAS char* l) {
  asm volatile("s_mov_b32 m0, %4\n\ts_nop 0\n\tbuffer_load_dwordx4 %0, %2, %3 offen lds\n\ts_mov_b32 exec_hi, 0\n\t"
               "buffer_load_dwordx4 %1, %2, %3 offen offset:1024 lds\n\ts_mov_b32 exec_hi, -1"
               :: "v"(v[0]), "v"(v[1]), "s"(rs), "s"(soff), "s"((unsigned)(size_t)l) : "memory");
}
template <int OFF> DI void AST16(rsrc_t rs, unsigned voff, unsigned soff, u32x4 v) {
  asm volatile("buffer_store_dwordx4 %0, %1, %2, %3 offen offset:%4 sc1\n\ts_nop 1" :: "v"(v), "v"(voff), "s"(rs), "s"(soff), "i"(OFF) : "memory");
}
DI void BLDS16(rsrc_t rs, unsigned voff, unsigned soff, LAS char* l) {
  asm volatile("s_mov_b32 m0, %3\n\ts_mov_b64 exec, 0xffff\n\tbuffer_load_dwordx4 %0, %1, %2 offen lds\n\ts_mov_b64 exec, -1"
               :: "v"(voff), "s"(rs), "s"(soff), "s"((unsigned)(size_t)l) : "memory");
}
DI void TOUCH(rsrc_t rs, unsigned voff, unsigned soff, LAS char* l) {
  asm volatile("s_mov_b32 m0, %3\n\ts_nop 0\n\tbuffer_load_dword %0, %1, %2 offen lds"
               :: "v"(voff), "s"(rs), "s"(soff), "s"((unsigned)(size_t)l) : "memory");
}
DI void ald16(u32x4& d, rsrc_t rs, unsigned voff, unsigned soff) {
  asm volatile("buffer_load_dwordx4 %0, %1, %2, %3 offen sc1" : "=v"(d) : "v"(voff), "s"(rs), "s"(soff) : "memory");
}
DI void ald8(u32x2& d, rsrc_t rs, unsigned voff, unsigned soff) {
  asm volatile("buffer_load_dwordx2 %0, %1, %2, %3 offen sc1" : "=v"(d) : "v"(voff), "s"(rs), "s"(soff) : "memory");
}
DI void ald4(unsigned& d, rsrc_t rs, unsigned voff, unsigned soff) {
  asm volatile("buffer_load_dword %0, %1, %2, %3 offen sc1" : "=v"(d) : "v"(voff), "s"(rs), "s"(soff) : "memory");
}
DI void gld8(u32x2& d, const void* p) { asm volatile("global_load_dwordx2 %0, %1, off" : "=v"(d) : "v"(p) : "memory"); }
#define ST16(rs, voff, soff, v) __builtin_amdgcn_raw_buffer_store_b128(v, rs, (int)((voff) + (soff)), 0, 16)
#define ST8(rs, voff, soff, v) __builtin_amdgcn_raw_buffer_store_b64(v, rs, (int)(voff), (int)(soff), 16)
#define ST4(rs, voff, soff, v) __builtin_amdgcn_raw_buffer_store_b32(v, rs, (int)(voff), (int)(soff), 16)
#define LD16(rs, voff, soff) __builtin_amdgcn_raw_buffer_load_b128(rs, (int)(voff), (int)(soff), 16)
#define LD8(rs, voff, soff) __builtin_amdgcn_raw_buffer_load_b64(rs, (int)(voff), (int)(soff), 16)

constexpr unsigned WS_EPOCH = 0;
constexpr unsigned WS_F1 = 1024;
constexpr unsigned WS_F2K = WS_F1 + 4096;
constexpr unsigned WS_F2Z = WS_F2K + 2048;
constexpr unsigned WS_F3O = WS_F2Z + 2048;
constexpr unsigned WS_F4 = WS_F3O + 2048;
constexpr unsigned WS_STAT = 20480;
constexpr unsigned WS_KVP = WS_STAT + 16 * 12 * 256 * 8;
constexpr unsigned WS_KVF = WS_KVP + 16 * 16 * 2816;
constexpr unsigned WS_PQ = WS_KVF + 16 * 5632;
constexpr unsigned WS_WX = WS_PQ + 128 * 16 * 1152;
constexpr unsigned WS_Z2N = WS_WX + 8 * 90 * 4608;
constexpr unsigned WS_Z2O = WS_Z2N + 16 * 64 * 8 * 1024;
constexpr unsigned WS_Z1 = WS_Z2O + 64 * 16 * 1024;
constexpr unsigned WS_HX = WS_Z1 + 128 * 72 * 1024;
constexpr unsigned WS_END = WS_HX + 128 * 90 * 1024;
constexpr unsigned SPIN_MAX = 1u << 18;

#define STAMP(K, i) do {} while (0)
#define FINE_DECL() do {} while (0)
#define FINE(n) do {} while (0)
#define FINE_OUT(n) do {} while (0)

struct Params {
  const u16* x; const u16* wqkv; const u16* wo; const u16* win; const u16* wsp; const u16* bsp; const u16* wout;
  u16* out; char* ws;
};

DI int lane_id() {
  int l; asm volatile("v_mbcnt_lo_u32_b32 %0, -1, 0\n\tv_mbcnt_hi_u32_b32 %0, -1, %0" : "=v"(l)); return l;
}
DI unsigned cvt_pk(float lo, float hi) {
  f32x2 v = {lo, hi}; bf16x2_t b = __builtin_convertvector(v, bf16x2_t); return __builtin_bit_cast(unsigned, b);
}
DI float bf_lo(unsigned v) { return __uint_as_float(v << 16); }
DI float bf_hi(unsigned v) { return __uint_as_float(v & 0xffff0000u); }
constexpr float GC = 0.4686686931f, GKK = 4.912229539f;
DI float gelu_s(float x) {
  const float e = __builtin_amdgcn_exp2f(-x * __builtin_fmaf(x, x, GKK));
  return x * __builtin_amdgcn_rcpf(1.0f + e);
}
DI float phi_f(float t) {
  return __builtin_fmaxf(t, 1e-6f) + __builtin_amdgcn_fmed3f(__builtin_amdgcn_exp2f(t * 1.4426950409f), 0.f, 1.f);
}
DI bf16x8 tr_frag(LAS char* a0, LAS char* a1) {
  const s16x4 lo = __builtin_amdgcn_ds_read_tr16_b64_v4i16((LAS s16x4*)a0);
  const s16x4 hi = __builtin_amdgcn_ds_read_tr16_b64_v4i16((LAS s16x4*)a1);
  return __builtin_shufflevector(lo, hi, 0, 1, 2, 3, 4, 5, 6, 7);
}
template <int CTRL> DI float dpp_get(float v) {
  return __builtin_bit_cast(float, __builtin_amdgcn_update_dpp(0, __builtin_bit_cast(int, v), CTRL, 0xf, 0xf, true));
}
DI float oct_sum(float v) {
  v += dpp_get<0xB1>(v); v += dpp_get<0x4E>(v); v += dpp_get<0x141>(v); return v;
}
DI int slot_row(int rs) { return (rs & ~31) + 16 * ((rs >> 2) & 1) + 4 * ((rs >> 3) & 3) + (rs & 3); }
DI unsigned flag_ld(const char* ws, unsigned off) {
  return __hip_atomic_load((GAS unsigned*)(ws + off), __ATOMIC_RELAXED, __HIP_MEMORY_SCOPE_AGENT);
}
DI void flag_st(char* ws, unsigned off, unsigned v) {
  __hip_atomic_store((GAS unsigned*)(ws + off), v, __ATOMIC_RELAXED, __HIP_MEMORY_SCOPE_AGENT);
}
DI bool all_eq(unsigned v, unsigned tag) { return __builtin_amdgcn_ballot_w64(v != tag) == 0; }
DI void spin_flag(const char* ws, unsigned off, unsigned tag) {
  for (unsigned i = 0; i < SPIN_MAX; ++i) {
    if (all_eq(flag_ld(ws, off), tag)) break;
    __builtin_amdgcn_s_sleep(1);
  }
  asm volatile("" ::: "memory");
}

constexpr int LDS_BYTES = 160 * 1024;
constexpr int L_SCR = 159 * 1024;
constexpr int A_XN = 15 * 1024;
constexpr int AZ_SLOT = 24 * 1024, AZ_PARK = 96 * 1024, AZ_ST = 144 * 1024, AZ_ST2 = 147 * 1024, AZ_TAB = 151 * 1024;
constexpr int AQ_SLOT = 18 * 1024, AQ_PK = 72 * 1024, AQ_V = 108 * 1024, AQ_KV = 144 * 1024;
constexpr int STG = 34 * 1024, B_KV = 4 * STG, B_HAND = B_KV + 9 * 1024;
#define ROTB 3
#define ROTC 3
#define ROT(n, r, m) (((n) & ~(m)) | (((n) + (r)) & (m)))

struct Ctx {
  int pa, quad, jj, p, c, wid, bid;
  unsigned tag;
};

template <int ROLE> struct Loader {
  rsrc_t rsws, rsw; unsigned voff[4]; unsigned l16; int p, c; LAS char* smem;
  DI void init(const Params& P, const Ctx& X, LAS char* sm) {
    smem = sm; p = X.p; c = X.c;
    const int ln = lane_id();
    l16 = ln * 16;
    rsws = MKRS(P.ws, WS_END);
    if (ROLE == 1) {
      rsw = MKRS((const char*)P.wsp + (size_t)p * 128 * 4096, 128 * 4096);
#pragma unroll
      for (int i = 0; i < 4; ++i) {
        const int rs = i * 8 + (ln >> 3);
        voff[i] = (unsigned)(slot_row(rs) * 4096 + (((ln & 7) ^ ((rs & 15) >> 1)) * 16) - i * 1024);
      }
    }
  }
  DI void issue_b(int N) {
    LAS char* d = smem + (N & 3) * STG;
    const int n = ROT(N, p + c, ROTB);
    if (ROLE == 0) {
      const unsigned so = WS_Z2N + c * (64 * 8 * 1024) + n * 16384;
#pragma unroll
      for (int i = 0; i < 4; ++i) BLDSx<4>(rsws, l16, so + i * 4096, d + i * 4096);
      BLDSx<1>(rsws, l16, WS_Z2O + (n * 32 + c) * 1024, d + 16384);
      BLDSx<1>(rsws, l16, WS_Z2O + (n * 32 + 16 + c) * 1024, d + 17408);
    } else {
#pragma unroll
      for (int i = 0; i < 4; ++i) BLDS4v(rsw, voff, n * 128 + i * (32 * 4096), d + 18432 + i * 4096);
    }
  }
  template <int NK> DI void issue_c(int N) {
    LAS char* d = smem + (N & 3) * STG;
    const int M = N - 32, rg = 2 * p + (c >> 3), cg = c & 7;
    const int kb = M == 0 ? 88 : M < 5 ? 72 + 4 * (M - 1) : 4 * (M < 21 ? ROT(M - 5, p + c, ROTC) : M - 5);
    if (ROLE == 0) {
      const unsigned so = WS_WX + (cg * 90 + kb) * 4608;
#pragma unroll
      for (int i = 0; i < NK; ++i) BLDSx<4>(rsws, l16, so + i * 4096, d + i * 4096);
      BLDSx<NK / 2>(rsws, l16, so + NK * 4096, d + NK * 4096);
    } else {
#pragma unroll
      for (int i = 0; i < 4; ++i) BLDSx<NK>(rsws, l16, WS_HX + ((rg * 4 + i) * 90 + kb) * 1024, d + 18432 + i * 4096);
    }
  }
};
DI void warm_code(const Ctx& X, LAS char* smem) {
  u64 pc; unsigned len;
  asm volatile("s_getpc_b64 %0\n1:\n\ts_mov_b32 %1, 1b-_Z2kF6Params" : "=s"(pc), "=s"(len));
  pc -= len;
  asm volatile("s_mov_b32 %0, kf_code_end-_Z2kF6Params" : "=s"(len));
  if ((int)len < 0) len = 0;
  TOUCH(MKRS((const void*)pc, len), lane_id() * 128, (X.bid >> 3) * 8192, smem + L_SCR);
}
DI unsigned f1_off(const Ctx& X) { const int l = lane_id(); return WS_F1 + X.c * 256 + (l < 48 ? l : 47) * 4; }
DI int f1_src(int c, int w) { return (w == 0 ? 8 * c : w == 1 ? 8 * c + 7 : 128 + c) / 12; }

#define Z_GAT 150
#define Z_XB2 163
#define Z_F1 194
struct ZS {
  static constexpr int XB1 = 136, PUB = 138, GAT = Z_GAT, XB2 = Z_XB2, F1 = Z_F1, POLL = 209;
  static constexpr int nrm(int ch) { return XB2 + 6 + 2 * ch + ch / 3; }
  static constexpr bool dma(int E) { return (E + 4) / 12 + 2 < 18 && E % 12 < 3; }
  static constexpr int other(int E) {
    int n = (E == PUB) + (E >= GAT && E < GAT + 3) + (E == F1) + (E == POLL) + (E == 155 || E == 164 || E == 201 || E == 214);
    for (int ch = 0; ch < 6; ++ch) n += E == nrm(ch) + 1;
    return n;
  }
  static constexpr int younger(int SN) {
    const int last = 12 * SN - 22, bar = 12 * SN - 4;
    int n = 0;
    for (int E = last < 0 ? 0 : last; E < bar; ++E) n += other(E) + ((E > last && dma(E)) ? 1 : 0);
    return n;
  }
  static constexpr int after_z2n() {
    int n = 0;
    for (int E = nrm(5) + 2; E < F1; ++E) n += other(E) + (dma(E) ? 1 : 0);
    return n;
  }
};
DI void a_zblock(const Params& P, const Ctx& X, LAS char* smem) {
  const int wid = X.wid, p = X.pa, j = X.quad * 3 + X.jj;
  int lane = lane_id();
  const int rw = wid & 3, G = wid >> 2;
  const int h = lane >> 5;
  const rsrc_t rsw = MKRS(P.win, 576 * 9216), rsws = MKRS(P.ws, WS_END);
  const brs_t ws = __builtin_amdgcn_make_buffer_rsrc(P.ws, 0, WS_END, 0x00020000);
  unsigned voff;
  {
    const int row = wid * 8 + (lane >> 3);
    voff = (unsigned)(row * 9216 + (((lane & 7) ^ (((row >> 1) & 1) << 2)) * 16));
  }
  const int cz2 = (2304 + 3 * j * 64) * 2, cz1 = 3 * j * 64 * 2;
  auto dma = [&](auto S_, auto I_) {
    constexpr int S = decltype(S_)::value, I = decltype(I_)::value, R = S / 3;
    BLDS(rsw, voff, (R < 3 ? cz2 + R * 128 : cz1 + (R - 3) * 128) + ((S % 3) * 192 + I * 64) * 9216,
         smem + (S & 3) * AZ_SLOT + I * 8192 + wid * 1024);
  };
  bf16x8 xf[36];
  {
    LAS char* xb = smem + A_XN + (rw * 32 + (lane & 31)) * 128;
    const int sw = (lane >> 1) & 7;
    LAS char* b[4];
#pragma unroll
    for (int i = 0; i < 4; ++i) b[i] = xb + (((2 * i + h) ^ sw) * 16);
#pragma unroll
    for (int ks = 0; ks < 12; ++ks) xf[ks] = *(LAS bf16x8*)(b[ks & 3] + (ks >> 2) * 16384);
    WAIT_L0(); BAR();
    sfor<0, 2>([&](auto S_) { sfor<0, 3>([&](auto I_) { dma(S_, I_); }); });
#pragma unroll
    for (int ks = 12; ks < 36; ++ks) xf[ks] = *(LAS bf16x8*)(b[ks & 3] + (ks >> 2) * 16384);
  }
  STAMP(0, 3);
  const int rq = (lane >> 2) & 3;
  LAS char* rd = smem + (8 * h + rq) * 128 + ((G ^ (rq >> 1)) * 64) + ((lane >> 4) & 1) * 32 + (lane & 3) * 8;
  LAS char* park = smem + AZ_PARK + wid * 2048 + h * 1024 + (lane & 31) * 16;
  LAS char* strd = smem;
  const unsigned zo = WS_Z1 + ((p * 8 + rw * 2) * 72 + 6 * j + G) * 1024;
  unsigned zv = 0, nvo = 0, fpoll = 0;
  const bf16x8 ones = {0x3f80, 0x3f80, 0x3f80, 0x3f80, 0x3f80, 0x3f80, 0x3f80, 0x3f80};
  f32x16 acc[2];
  bf16x8 wf[5], pf[2];
  f32x4 ssum[2], ssq[2];
  unsigned pk[4];
  float gt[4] = {0.f, 0.f, 0.f, 0.f}, gb = 0.f, xt[4];
  u32x4 gv[3];
  f32x4 tab[2][4];
  u32x4 nv[2];
  auto rdw = [&](auto E_) {
    constexpr int E = decltype(E_)::value, S = E / 12, o = (S & 3) * AZ_SLOT + (E % 12) * 2048;
    wf[E % 5] = tr_frag(rd + o, rd + o + 512);
  };
  auto stats_rd = [&](int rr) {
    if (rr == 0) {
      const int ln = lane_id();
      strd = smem + AZ_PARK + wid * 2048 + ((ln >> 1) & 1) * 512 + (8 * (ln >> 4) + ((ln >> 2) & 3)) * 16 + (ln & 1) * 8;
    }
#pragma unroll
    for (int u = 0; u < 2; ++u) pf[u] = tr_frag(strd + rr * 16384 + u * 1024, strd + rr * 16384 + u * 1024 + 64);
  };
  auto stats_mm = [&](auto RR_) {
    constexpr int RR = decltype(RR_)::value;
#pragma unroll
    for (int u = 0; u < 2; ++u) {
      const f32x4 zz = {0, 0, 0, 0};
      ssum[u] = MFMA(ones, pf[u], RR ? ssum[u] : zz);
      ssq[u] = MFMA(pf[u], pf[u], RR ? ssq[u] : zz);
      PIN(ssum[u]); PIN(ssq[u]);
    }
  };
  auto gelu_st = [&](auto RR_, auto EL_, auto ST_) {
    constexpr int RR = decltype(RR_)::value, EL = decltype(EL_)::value, ST = decltype(ST_)::value;
    if constexpr (EL >= 0 && EL < 16) {
      constexpr int reg = RR < 3 ? EL : (EL & 3) + ((EL >> 2) & 1) * 8 + (EL >> 3) * 4;
      const float x = acc[RR & 1][reg];
      float& g = gt[EL & 3];
      if (ST == 0) { g = __builtin_fmaf(x, x, GKK); PIN(g); }
      if (ST == 1) { g = __builtin_amdgcn_exp2f(-x * g); PIN(g); }
      if (ST == 2) { g = __builtin_amdgcn_rcpf(1.0f + g); PIN(g); }
      if (ST == 3) {
        const float y = x * g;
        if (EL & 1) { pk[(EL >> 1) & 3] = cvt_pk(gb, y); PIN(pk[(EL >> 1) & 3]); } else { gb = y; PIN(gb); }
      }
    }
  };
  auto put = [&](auto RR_, auto CC_) {
    constexpr int RR = decltype(RR_)::value, CC = decltype(CC_)::value;
    const u32x4 w = {pk[0], pk[1], pk[2], pk[3]};
    if constexpr (RR < 3) *(LAS u32x4*)(park + RR * 16384 + CC * 512) = w;
    else AST16<(RR - 3) * 2048 + CC * 512>(rsws, zv, zo, w);
  };
  auto gather = [&](int i) {
    const int t = wid * 64 + lane_id();
    gv[i] = LD16(ws, (t & 127) * 16 + ((t >> 7) * 3 + i) * 2048, WS_STAT + p * 12 * 2048);
  };
  FINE_DECL();
  wait_v<3>(); WAIT_L0(); BAR();
  FINE(0);
  sfor<0, 4>(rdw);
  sfor<0, 216>([&](auto E_) {
    constexpr int E = decltype(E_)::value, R = E / 36, ks = E % 36, EN = E + 4, SN = EN / 12;
    if constexpr (E == ZS::XB1 || E == ZS::XB2) { asm volatile("s_waitcnt lgkmcnt(4)" ::: "memory"); BAR(); }
    if constexpr (E == ZS::F1) {
      wait_v<ZS::after_z2n()>(); BAR();
      const int ln = lane_id();
      ST4(ws, (wid == 0 && ln < 48 && f1_src(ln & 15, ln >> 4) == j) ? (ln & 15) * 256 + ((ln >> 4) * 16 + p) * 4 : 0x7fffff00u, WS_F1, X.tag);
    }
    if constexpr (EN < 216) {
      if constexpr (EN % 12 == 0) { wait_v<ZS::younger(SN)>(); BAR(); FINE(SN); }
      rdw(IC<EN>{});
      if constexpr (ZS::dma(E)) dma(IC<SN + 2>{}, IC<EN % 12 - 4>{});
    }
    const f32x16 zz = {};
    if constexpr (R < 3) acc[R & 1] = MFMA32(xf[ks], wf[E % 5], ks ? acc[R & 1] : zz);
    else acc[R & 1] = MFMA32(wf[E % 5], xf[ks], ks ? acc[R & 1] : zz);
    PIN(acc[R & 1]);
    if constexpr ((R == 1 || R == 2 || R == 5) && ks >= 1) {
      gelu_st(IC<R - 1>{}, IC<(ks - 1) / 2>{}, IC<(ks - 1) & 1>{});
      gelu_st(IC<R - 1>{}, IC<(ks - 1) / 2 - 1>{}, IC<((ks - 1) & 1) + 2>{});
      if constexpr (ks == 21) put(IC<R - 1>{}, IC<0>{});
      if constexpr (ks == 34) put(IC<R - 1>{}, IC<1>{});
    }
    if constexpr (R == 3 || R == 4) {
      sfor<0, 4>([&](auto ST_) { gelu_st(IC<R - 1>{}, IC<ks - 1 - decltype(ST_)::value>{}, ST_); });
      if constexpr (ks == 11) put(IC<R - 1>{}, IC<0>{});
      if constexpr (ks == 20) put(IC<R - 1>{}, IC<1>{});
    }
    if constexpr (R == 3) {
      if constexpr (ks == 17 || ks == 20 || ks == 23) stats_mm(IC<(ks - 17) / 3>{});
      if constexpr (ks == 14 || ks == 17 || ks == 20) stats_rd((ks - 14) / 3);
      if constexpr (ks == 33) {
        const int ln = lane_id();
        zv = ((ln >> 4) & 1) * (72 * 1024) + (ln >> 5) * 256 + (ln & 15) * 16; PIN(zv);
        nvo = ((ln >> 4) & 1) * 1024 + (ln >> 5) * 512 + (ln & 15) * 16; PIN(nvo);
      }
    }
    if constexpr (E == ZS::XB1 - 3) {
      const int ln = lane_id();
      const int dst = (ln >> 4) == ((ln >> 2) & 3) ? G * 1024 + (rw * 32 + (ln & 15)) * 8 : 2048 + (ln & 15) * 8;
#pragma unroll
      for (int u = 0; u < 2; ++u) {
        const float q = (ln & 2) ? ((ln & 1) ? ssq[u][3] : ssq[u][2]) : ((ln & 1) ? ssq[u][1] : ssq[u][0]);
        *(LAS f32x2*)(smem + AZ_ST + dst + u * 128) = (f32x2){ssum[u][0], q};
      }
    }
    if constexpr (E == ZS::XB1) {
      const int t8 = rw * 64 + lane_id();
      xt[0] = *(LAS float*)(smem + AZ_ST + t8 * 4); xt[1] = *(LAS float*)(smem + AZ_ST + 1024 + t8 * 4);
    }
    if constexpr (E == ZS::PUB) {
      const int t8 = rw * 64 + lane_id();
      ST8(ws, t8 * 8, G ? 0x7fffff00u : WS_STAT + (p * 12 + j) * 2048, ((u32x2){__float_as_uint(xt[0] + xt[1]), X.tag}));
    }
    if constexpr (E >= ZS::GAT && E < ZS::GAT + 3) { asm volatile("" ::: "memory"); gather(E - ZS::GAT); asm volatile("" ::: "memory"); }
    if constexpr (E == ZS::XB2 - 3) {
      for (unsigned spins = 0; spins < SPIN_MAX; ++spins) {
        bool ok = true;
#pragma unroll
        for (int i = 0; i < 3; ++i) ok &= gv[i].y == X.tag && gv[i].w == X.tag;
        if (__builtin_amdgcn_ballot_w64(!ok) == 0) break;
        __builtin_amdgcn_s_sleep(1);
        asm volatile("" ::: "memory");
#pragma unroll
        for (int i = 0; i < 3; ++i) gather(i);
      }
      *(LAS f32x2*)(smem + AZ_ST2 + (wid * 64 + lane_id()) * 8) =
          (f32x2){__uint_as_float(gv[0].x) + __uint_as_float(gv[1].x) + __uint_as_float(gv[2].x),
                  __uint_as_float(gv[0].z) + __uint_as_float(gv[1].z) + __uint_as_float(gv[2].z)};
    }
    if constexpr (E == ZS::XB2) {
      LAS float* a = (LAS float*)(smem + AZ_ST2 + rw * 256 + lane_id() * 4);
#pragma unroll
      for (int i = 0; i < 4; ++i) xt[i] = a[i * 256];
    }
    if constexpr (E == ZS::XB2 + 3) {
      const int ln = lane_id();
      const float v = ((xt[0] + xt[1]) + (xt[2] + xt[3])) * (1.0f / 2304.0f), o = dpp_get<0xB1>(v);
      const float m = (ln & 1) ? o : v, q = (ln & 1) ? v : o;
      const float sc = __builtin_amdgcn_rsqf(__builtin_fmaxf(q - m * m, 0.f) + 1e-5f * GC * GC);
      *(LAS float*)(smem + AZ_TAB + wid * 256 + ln * 4) = (ln & 1) ? -m * sc : sc;
    }
    sfor<0, 6>([&](auto CH_) {
      constexpr int ch = decltype(CH_)::value, cc = ch / 3, rr = ch % 3, q0 = ZS::nrm(ch);
      if constexpr (E == q0 || E == q0 + 1) {
        constexpr int hf = E - q0;
#pragma unroll
        for (int i = 2 * hf; i < 2 * hf + 2; ++i) {
          unsigned t = nv[ch & 1][i];
          t = cvt_pk(__builtin_fmaf(bf_lo(t), tab[cc][i][0], tab[cc][i][1]), __builtin_fmaf(bf_hi(t), tab[cc][i][2], tab[cc][i][3]));
          PIN(t); nv[ch & 1][i] = t;
        }
        if constexpr (hf == 1) {
          const int tg = (3 * j + rr) * 4 + 2 * G, sb = p * 4 + rw;
          AST16<cc * 256>(rsws, nvo, tg < 128 ? WS_Z2N + (((tg >> 3) * 64 + sb) * 8 + (tg & 7)) * 1024 : WS_Z2O + (sb * 16 + tg - 128) * 1024, nv[ch & 1]);
        }
      }
      if constexpr (rr == 0 && E == q0 - 2) {
        const int hb = (lane_id() >> 5) * 128;
#pragma unroll
        for (int i = 0; i < 4; ++i) tab[cc][i] = *(LAS f32x4*)(smem + AZ_TAB + wid * 256 + hb + cc * 64 + i * 16);
      }
      if constexpr (E == q0 - 2) nv[ch & 1] = *(LAS u32x4*)(park + rr * 16384 + cc * 512);
    });
    if constexpr (E == ZS::POLL) {
      asm volatile("" ::: "memory");
      fpoll = __builtin_amdgcn_raw_buffer_load_b32(ws, (int)f1_off(X), wid == 4 ? 0 : 0x7ff00000, 16);
      asm volatile("" ::: "memory");
    }
    SB0();
  });
  FINE(18); FINE_OUT(19);
  STAMP(0, 9);
  BAR();
  STAMP(0, 10);
  auto g1 = [&](int cc) {
    u32x4 w;
#pragma unroll
    for (int i = 0; i < 4; ++i) {
      const int reg = ((2 * i) & 3) + (i >> 1) * 8 + cc * 4;
      w[i] = cvt_pk(gelu_s(acc[1][reg]), gelu_s(acc[1][reg + 1]));
    }
    PIN(w);
    return w;
  };
  if (wid == 4 || wid == 5) {
    __builtin_amdgcn_s_setprio(3);
    LAS char* d = smem + B_HAND + (wid - 4) * 2048 + lane_id() * 16;
    if (wid == 4) {
      Loader<0> L; L.init(P, X, smem);
      const unsigned fo = f1_off(X);
      const bool done = all_eq(fpoll, X.tag);
      if (done) { L.issue_b(0); L.issue_b(1); } else fpoll = flag_ld(P.ws, fo);
      *(LAS u32x4*)d = g1(0); *(LAS u32x4*)(d + 1024) = g1(1);
      STAMP(0, 12);
      if (!done) { if (!all_eq(fpoll, X.tag)) spin_flag(P.ws, fo, X.tag); STAMP(0, 13); L.issue_b(0); L.issue_b(1); }
    } else {
      Loader<1> L; L.init(P, X, smem);
      L.issue_b(0); L.issue_b(1);
      *(LAS u32x4*)d = g1(0); *(LAS u32x4*)(d + 1024) = g1(1);
    }
    WAIT_L0();
  } else {
    lane = lane_id();
    const int kb = ((p & 1) * 12 + j) * 4 + rw;
    const bool pack = wid < 4 && kb < 90;
    LAS char* pb = smem + 3 * STG + rw * 5120;
    if (pack) {
      const rsrc_t rs = kb < 72 ? MKRS(P.wout, 2304 * 1152) : MKRS(P.wo, 576 * 1152);
      const int r0 = kb < 72 ? 32 * kb : kb < 88 ? 36 * (kb - 72) : 288 * (kb - 88) + 32;
      const int mq = kb < 64 ? 4 : kb < 72 ? 8 : kb < 88 ? 4 : 72, mu = kb < 64 ? 16 : kb < 72 ? 4 : kb < 88 ? 16 : 36;
#pragma unroll
      for (int i = 0; i < 5; ++i) {
        const int pc = i < 4 ? i * 64 + lane : 256 + (lane & 31), k = (pc * 57) >> 9, ch = pc - k * 9;
        BLDS(rs, ((k >> 3) * mq + ((k >> 2) & 1) * mu + (k & 3)) * 1152 + ch * 16, r0 * 1152 + (p >> 1) * 144, pb + i * 1024);
      }
    }
    if (wid == 7) warm_code(X, smem);
    AST16<0>(rsws, zv, zo + 4096, g1(0)); AST16<512>(rsws, zv, zo + 4096, g1(1));
    if (pack) {
      wait_v<2>();
      LAS char* a = pb + (8 * (lane >> 4) + ((lane >> 2) & 3)) * 144;
      const unsigned so = WS_WX + ((p >> 1) * 90 + kb) * 4608;
      sfor<0, 4>([&](auto D_) {
        constexpr int dt = decltype(D_)::value;
        LAS char* b = a + (dt >> 1) * 64 + (lane & 3) * 16 + (dt & 1) * 8;
        AST16<dt * 1024>(rsws, lane * 16, so, __builtin_bit_cast(u32x4, tr_frag(b, b + 576)));
      });
      LAS char* b = a + 128 + (lane & 1) * 8;
      AST16<0>(rsws, (lane & 8) == 0 ? 4096 + (lane >> 4) * 128 + (lane & 7) * 16 : 0x7ff00000u, so, __builtin_bit_cast(u32x4, tr_frag(b, b + 576)));
    }
  }
}

template <int G>
DI void a_qkvblock(const Params& P, const Ctx& X, LAS char* smem) {
  constexpr int NT = G ? 4 : 5, T0 = G ? 5 : 0, SPS = 2 * NT, NE = 54 * NT, ND = G ? 3 : 2;
  const int wid = X.wid, p = X.pa, quad = X.quad;
  int lane = lane_id();
  const int rw = wid & 3, kq = lane >> 4, i16 = lane & 15;
  const rsrc_t rsw = MKRS(P.wqkv, 576 * 3456), rsws = MKRS(P.ws, WS_END);
  const brs_t ws = __builtin_amdgcn_make_buffer_rsrc(P.ws, 0, WS_END, 0x00020000);
  const int pc0 = G ? 384 + rw * 192 : rw * 96;
  unsigned voff[3];
#pragma unroll
  for (int i = 0; i < ND; ++i) {
    const int pc = pc0 + i * 64 + lane, sr = pc / 18, ch = pc - sr * 18;
    voff[i] = (unsigned)((sr ^ (((sr >> 3) & 1) << 2)) * 3456 + ch * 16 - i * 1024);
  }
  auto dma = [&](auto S_) {
    constexpr int S = decltype(S_)::value;
    const unsigned so = quad * 288 + (S % 9) * 64 * 3456 + ((S / 9 + 1) % 3) * 1152;
    LAS char* d = smem + (S & 3) * AQ_SLOT + pc0 * 16;
    if (G) BLDS3v(rsw, voff, so, d); else BLDS15v(rsw, voff, so, d);
  };
  bf16x8 xf[2][18];
  {
    LAS char* b0 = smem + A_XN + (rw * 32 + i16) * 128 + ((kq ^ (i16 >> 1)) * 16);
    LAS char* b1 = smem + A_XN + (rw * 32 + i16) * 128 + (((kq + 4) ^ (i16 >> 1)) * 16);
#pragma unroll
    for (int kb = 0; kb < 4; ++kb)
#pragma unroll
      for (int u = 0; u < 2; ++u) xf[u][kb] = *(LAS bf16x8*)(((kb & 1) ? b1 : b0) + u * 2048 + (kb >> 1) * 16384);
    WAIT_L0(); BAR();
    dma(IC<0>{}); dma(IC<1>{});
#pragma unroll
    for (int kb = 4; kb < 18; ++kb)
#pragma unroll
      for (int u = 0; u < 2; ++u) xf[u][kb] = *(LAS bf16x8*)(((kb & 1) ? b1 : b0) + u * 2048 + (kb >> 1) * 16384);
  }
  STAMP(0, 3);
  const int rq = (lane >> 2) & 3;
  LAS char* rdA = smem + (8 * kq + 4 * (kq & 1) + rq) * 288 + (lane & 3) * 8 + T0 * 32;
  LAS char* rdB = smem + (8 * kq + 4 * ((kq & 1) ^ 1) + rq) * 288 + (lane & 3) * 8 + T0 * 32;
  LAS char* img = smem + AQ_PK + lane * 16;
  f32x4 acc[NT][2], dd;
  u32x4 prev[NT];
  bf16x8 wf[4], ka[4], va[4];
  int ko[4], kt[4];
  unsigned fpoll = 0;
  float pa = 0.f, pe = 0.f, pt = 0.f, pm = 0.f, pl = 0.f;
  auto rdw = [&](auto E_) {
    constexpr int E = decltype(E_)::value, S = E / SPS, o = (S & 3) * AQ_SLOT + ((E % SPS) / NT) * 9216 + (E % NT) * 32;
    wf[E % 4] = tr_frag(rdA + o, rdB + o);
  };
  FINE_DECL();
  wait_v<ND>(); WAIT_L0(); BAR();
  FINE(0);
  sfor<0, 3>(rdw);
  sfor<0, NE>([&](auto E_) {
    constexpr int E = decltype(E_)::value, R = E / (18 * NT), er = E % (18 * NT), kb = er / NT, t = er % NT, EN = E + 3, SN = EN / SPS, ps = EN % SPS;
    if constexpr (EN < NE) {
      if constexpr (ps == 0) {
        wait_v<(SN < 26 ? ND : 0) + (G == 0 && SN >= 25 ? 3 : 0)>();
        if (SN == 19 || SN == 24) asm volatile("s_waitcnt lgkmcnt(2)" ::: "memory");
        BAR(); FINE(SN);
      }
      rdw(IC<EN>{});
      if constexpr (SN + 2 < 27 && ps == 3) dma(IC<SN + 2>{});
    }
    if constexpr (R >= 1 && kb == 0) {
      prev[t] = (u32x4){cvt_pk(acc[t][0][0], acc[t][0][1]), cvt_pk(acc[t][0][2], acc[t][0][3]),
                        cvt_pk(acc[t][1][0], acc[t][1][1]), cvt_pk(acc[t][1][2], acc[t][1][3])};
      PIN(prev[t]);
      if constexpr (R == 2) *(LAS u32x4*)(img + (AQ_V - AQ_PK) + ((T0 + t) * 4 + rw) * 1024) = prev[t];
    }
    const f32x4 zz = {0, 0, 0, 0};
    if constexpr (R == 2) { acc[t][0] = MFMA(wf[E % 4], xf[0][kb], kb ? acc[t][0] : zz); acc[t][1] = MFMA(wf[E % 4], xf[1][kb], kb ? acc[t][1] : zz); }
    else { acc[t][0] = MFMA(xf[0][kb], wf[E % 4], kb ? acc[t][0] : zz); acc[t][1] = MFMA(xf[1][kb], wf[E % 4], kb ? acc[t][1] : zz); }
    PIN(acc[t][0]); PIN(acc[t][1]);
    if constexpr (R == 1 && er >= NT) {
      constexpr int q = er - NT, el = q / 2;
      if constexpr ((q & 1) == 0) {
        if constexpr (el < 8 * NT) {
          const unsigned src = prev[el / 8][(el & 7) / 2];
          pt = (el & 1) ? bf_hi(src) : bf_lo(src); pm = pt * 1.4426950409f; PIN(pt); PIN(pm);
        }
        if constexpr (el >= 1 && el - 1 < 8 * NT) {
          constexpr int e1 = el - 1, tl = e1 / 8, w = (e1 & 7) / 2;
          const float r = pa + pe;
          if constexpr ((e1 & 1) == 0) { pl = r; PIN(pl); }
          else { unsigned o = cvt_pk(pl, r); PIN(o); prev[tl][w] = o; }
          if constexpr ((e1 & 7) == 7) *(LAS u32x4*)(img + ((T0 + tl) * 4 + rw) * 1024) = prev[tl];
        }
      } else if constexpr (el < 8 * NT) {
        pe = __builtin_amdgcn_fmed3f(__builtin_amdgcn_exp2f(pm), 0.f, 1.f); pa = __builtin_fmaxf(pt, 1e-6f); PIN(pe); PIN(pa);
      }
    }
    if constexpr (SN >= 19 && SN < 24) {
      constexpr int q = EN - 19 * SPS, un = q / 5, ph = q % 5, it = un / 4, dj = un % 4;
      if constexpr (un < (G ? 7 : 4)) {
        const int i = it ? 8 : wid;
        if constexpr (ph == 0) {
          if constexpr (dj == 0) {
#pragma unroll
            for (int k = 0; k < 4; ++k) ka[k] = *(LAS bf16x8*)(img + (i * 4 + k) * 1024);
          } else {
            const int jv = i + dj - 2 < 0 ? 0 : i + dj - 2;
#pragma unroll
            for (int k = 0; k < 4; ++k) va[k] = *(LAS bf16x8*)(img + (AQ_V - AQ_PK) + (jv * 4 + k) * 1024);
          }
        }
        if constexpr (dj == 0 && (ph == 1 || ph == 2)) {
          const int ln = lane_id();
#pragma unroll
          for (int e = 2 * ph - 2; e < 2 * ph; ++e) {
            const int kcol = 16 * i + 4 * (ln >> 4) + e, hk = (kcol * 57) >> 10;
            kt[e] = 16 * (i - 1) + (ln & 15) - 18 * hk;
            ko[e] = (hk >> 1) * 2816 + ((hk & 1) * 342 + (kcol - 18 * hk) * 19) * 4;
            PIN(kt[e]); PIN(ko[e]);
          }
        }
        if constexpr (ph == 2) {
          const bf16x8 ones = {0x3f80, 0x3f80, 0x3f80, 0x3f80, 0x3f80, 0x3f80, 0x3f80, 0x3f80};
          dd = zz;
#pragma unroll
          for (int k = 0; k < 4; ++k) dd = MFMA(ka[k], dj ? va[k] : ones, dd);
          PIN(dd);
        }
        if constexpr (ph == 4) {
#pragma unroll
          for (int e = 0; e < 4; ++e) {
            const int ee = kt[e] + 16 * (dj - 1);
            *(LAS float*)(smem + AQ_KV + (dj == 0 ? ko[e] + 72 : (unsigned)ee < 18u ? ko[e] + ee * 4 : 11264)) = dd[e];
          }
        }
      }
    }
    if constexpr (G == 0 && SN == 24) {
      if constexpr (ps == 1) {
#pragma unroll
        for (int i = 0; i < 3; ++i) prev[i] = *(LAS u32x4*)(img + (AQ_KV - AQ_PK) + wid * 2816 + i * 1024);
      }
      if constexpr (ps >= 4 && ps < 7) {
        const int ln = lane_id();
        AST16<(ps - 4) * 1024>(rsws, (ps < 6 || ln < 48) ? ln * 16 : 0x7ff00000u, WS_KVP + ((quad * 4 + wid) * 16 + p) * 2816, prev[ps - 4]);
      }
    }
    if constexpr (G == 1 && E == NE - 8) {
      asm volatile("" ::: "memory");
      fpoll = __builtin_amdgcn_raw_buffer_load_b32(ws, (int)f1_off(X), wid == 4 ? 0 : 0x7ff00000, 16);
      asm volatile("" ::: "memory");
    }
    SB0();
  });
  FINE(27); FINE_OUT(28);
  STAMP(0, 9);
  BAR();
  if (wid == 4) {
    Loader<0> L; L.init(P, X, smem);
    if (!all_eq(fpoll, X.tag)) spin_flag(P.ws, f1_off(X), X.tag);
    L.issue_b(0); L.issue_b(1);
  } else if (wid == 5) {
    Loader<1> L; L.init(P, X, smem); L.issue_b(0); L.issue_b(1);
  } else if (wid == 7) warm_code(X, smem);
  lane = lane_id();
#pragma unroll
  for (int t = 0; t < NT; ++t)
#pragma unroll
    for (int u = 0; u < 2; ++u)
      ST8(ws, lane * 8, WS_PQ + (p * 8 + rw * 2 + u) * 18432 + (quad * 9 + T0 + t) * 512,
          ((u32x2){cvt_pk(acc[t][u][0], acc[t][u][1]), cvt_pk(acc[t][u][2], acc[t][u][3])}));
  if (G == 0) {
    wait_v<2 * NT>();
    if (lane == 0) flag_st(P.ws, WS_F2K + (quad * 4 + wid) * 128 + p * 4, X.tag);
  }
}

template <int ROLE>
DI void bc_loader(const Params& P, const Ctx& X, LAS char* smem) {
  constexpr int CNT = ROLE ? 16 : 18;
  __builtin_amdgcn_s_setprio(3);
  Loader<ROLE> L; L.init(P, X, smem);
  const int lane = lane_id(), p = X.p, c = X.c;
#pragma unroll 1
  for (int n = 0; n < 30; ++n) { if (n < 2 && X.jj == 3) wait_v<CNT + 8>(); else wait_v<CNT>(); BAR(); L.issue_b(n + 2); }
  wait_v<CNT>(); BAR(); wait_v<0>(); BAR();
  if (ROLE == 0) spin_flag(P.ws, WS_F2Z + ((c & 7) * 2 + (lane >> 5)) * 128 + ((lane & 31) < 12 ? lane & 31 : 0) * 4, X.tag);
  else spin_flag(P.ws, WS_F3O + p * 128 + (lane & 15) * 4, X.tag);
  L.template issue_c<2>(32); L.template issue_c<4>(33); L.template issue_c<4>(34);
  int n = 32;
  if (ROLE == 1) {
    wait_v<2 * CNT>(); BAR(); L.template issue_c<4>(35);
    wait_v<2 * CNT>(); BAR(); L.template issue_c<4>(36);
    wait_v<2 * CNT>(); BAR(); wait_v<CNT>(); BAR(); wait_v<0>(); BAR();
    STAMP(2, 5);
    spin_flag(P.ws, WS_F4 + (2 * p + (c >> 3)) * 128 + (lane & 31) * 4, X.tag);
    STAMP(2, 6);
    L.template issue_c<4>(37); L.template issue_c<4>(38); L.template issue_c<4>(39);
    n = 37;
  }
#pragma unroll 1
  for (; n < 52; ++n) { wait_v<2 * CNT>(); BAR(); L.template issue_c<4>(n + 3); }
  wait_v<2 * CNT>(); BAR(); wait_v<CNT>(); BAR(); wait_v<0>(); BAR();
}

DI void bc_side(const Params& P, const Ctx& X, LAS char* smem) {
  __builtin_amdgcn_s_setprio(2);
  const brs_t ws = __builtin_amdgcn_make_buffer_rsrc(P.ws, 0, WS_END, 0x00020000);
  const int lane = lane_id();
  const int sw = X.wid - 6, lt = sw * 64 + lane, p = X.p, c = X.c, kq = lane >> 4, r16 = lane & 15;
  u32x4 part[3]; u32x2 qv[4][2], qx; int dst[6]; bf16x8 kvf[4][2]; float mine = 0.f;
  const unsigned oob = 0x7fffff00u;
  const unsigned f2k = WS_F2K + c * 128 + r16 * 4, f2z = WS_F2Z + p * 128 + (kq < 3 ? f1_src(c, kq) : 12 + (c >> 2)) * 4;
  unsigned fl = 0;
  auto kvf_ld = [&]() {
#pragma unroll
    for (int i = 0; i < 3; ++i) part[i] = LD16(ws, (lt + 128 * i < 352 ? lt + 128 * i : 351) * 16, WS_KVF + c * 5632);
  };
  auto phi2 = [&](u32x2& v) {
    v = (u32x2){cvt_pk(phi_f(bf_lo(v.x)), phi_f(bf_hi(v.x))), cvt_pk(phi_f(bf_lo(v.y)), phi_f(bf_hi(v.y)))};
    PIN(v);
  };
#pragma unroll
  for (int i = 0; i < 4; ++i) *(LAS u32x4*)(smem + B_KV + i * 2048 + lt * 16) = (u32x4){0, 0, 0, 0};
#pragma unroll
  for (int e6 = 0; e6 < 6; ++e6) {
    const int id = (lt + 128 * (e6 >> 1)) * 2 + (e6 & 1), hh = id >= 342, rem = id - hh * 342, d = (rem * 1725) >> 15, e = rem - d * 19;
    const int k = 18 * hh + d, n = 18 * hh + e;
    const int kof = (k >> 5) * 1024 + ((k >> 3) & 3) * 256 + (k & 7) * 2;
    dst[e6] = id >= 684 ? 8192 + lane * 2 : (e < 18 ? (n >> 4) * 2048 + (n & 15) * 16 : 6144 + hh * 16) + kof;
    PIN(dst[e6]);
  }
  sfor<0, 32>([&](auto N_) {
    constexpr int N = decltype(N_)::value;
    if constexpr (N == 4) wait_v<3>();
    if constexpr (N == 26) wait_v<0>();
    if constexpr (N == 0 || N == 15) WAIT_L0();
    BAR();
    if constexpr (N == 1) fl = flag_ld(P.ws, f2k);
    if constexpr (N == 0) {
      const unsigned zo = X.jj < 3 ? WS_Z1 + ((X.pa * 8 + sw * 2) * 72 + 6 * (X.quad * 3 + X.jj) + 5) * 1024 : 0x7ff00000u;
      const unsigned zv = (kq & 1) * (72 * 1024) + (kq >> 1) * 256 + r16 * 16;
#pragma unroll
      for (int i = 0; i < 2; ++i) ST16(ws, zv + i * 512, zo, *(LAS u32x4*)(smem + B_HAND + sw * 2048 + i * 1024 + lane * 16));
    }
    if constexpr (N == 4) {
      if (lt == 0) flag_st(P.ws, WS_F2Z + X.pa * 128 + (X.jj < 3 ? X.quad * 3 + X.jj : 12 + X.quad) * 4, X.tag);
    }
    if constexpr (N == 7) fl = flag_ld(P.ws, f2z);
    if constexpr (N == 3) {
      if (!all_eq(fl, X.tag)) spin_flag(P.ws, f2k, X.tag);
#pragma unroll
      for (int i = 0; i < 3; ++i) {
        const int f4 = (lane & 3) + 4 * i;
        const u32x2 v = LD8(ws, (lane >> 2) * 2816 + (p * 11 + (f4 < 11 ? f4 : 10)) * 16 + sw * 8, WS_KVP + c * 16 * 2816);
        part[i].x = v.x; part[i].y = v.y;
      }
    }
    if constexpr (N == 6) {
#pragma unroll
      for (int i = 0; i < 6; ++i) {
        const unsigned u = part[i >> 1][i & 1];
        float v = __uint_as_float(u);
        v += dpp_get<0x124>(v); v += dpp_get<0x128>(v);
        auto a = __builtin_amdgcn_permlane16_swap(__float_as_uint(v), __float_as_uint(v), false, false);
        v = __uint_as_float(a[0]) + __uint_as_float(a[1]);
        auto b = __builtin_amdgcn_permlane32_swap(__float_as_uint(v), __float_as_uint(v), false, false);
        v = __uint_as_float(b[0]) + __uint_as_float(b[1]);
        mine = (lane >> 2) == i ? v : mine;
      }
      const int f4 = (lane & 3) + (lane >> 3) * 4;
      ST8(ws, f4 < 11 ? ((p * 11 + f4) * 4 + sw * 2 + ((lane >> 2) & 1)) * 8 : oob, WS_KVF + c * 5632, ((u32x2){__float_as_uint(mine), X.tag}));
    }
    if constexpr (N == 9) {
      if (!all_eq(fl, X.tag)) spin_flag(P.ws, f2z, X.tag);
      qx = LD8(ws, kq * 18432 + 1024 + r16 * 8, WS_PQ + (p * 8 + sw * 4) * 18432 + c * 1152);
    }
    if constexpr (N == 9 || N == 10) {
#pragma unroll
      for (int tt = 2 * (N - 9); tt < 2 * (N - 8); ++tt) {
        const unsigned so = WS_PQ + (p * 8 + sw * 4 + tt) * 18432 + c * 1152;
        qv[tt][0] = LD8(ws, kq * 256 + r16 * 8, so); qv[tt][1] = LD8(ws, kq * 256 + 128 + r16 * 8, so);
      }
    }
    if constexpr (N == 11) kvf_ld();
    if constexpr (N == 12) { phi2(qx); phi2(qv[0][0]); }
    if constexpr (N == 13) { phi2(qv[0][1]); phi2(qv[1][0]); }
    if constexpr (N == 15) phi2(qv[1][1]);
    if constexpr (N == 16 || N == 17) { phi2(qv[N - 14][0]); phi2(qv[N - 14][1]); }
    if constexpr (N == 14) {
      for (unsigned spins = 0; spins < SPIN_MAX; ++spins) {
        bool ok = true;
#pragma unroll
        for (int i = 0; i < 3; ++i) ok &= part[i].y == X.tag && part[i].w == X.tag;
        if (__builtin_amdgcn_ballot_w64(!ok) == 0) break;
        __builtin_amdgcn_s_sleep(1);
        asm volatile("" ::: "memory");
        kvf_ld();
      }
#pragma unroll
      for (int e6 = 0; e6 < 6; ++e6) {
        const unsigned u = part[e6 >> 1][(e6 & 1) * 2];
        *(LAS u16*)(smem + B_KV + dst[e6]) = (u16)(cvt_pk(__uint_as_float(u), 0.f) & 0xffffu);
      }
    }
    if constexpr (N == 15) {
#pragma unroll
      for (int nt = 0; nt < 4; ++nt) {
        LAS char* kb = smem + B_KV + nt * 2048 + (nt < 3 ? r16 : r16 & 1) * 16;
        kvf[nt][0] = *(LAS bf16x8*)(kb + kq * 256); kvf[nt][1] = *(LAS bf16x8*)(kb + 1024);
      }
    }
    if constexpr (N >= 18 && N <= 21) {
      constexpr int tt = N - 18;
      const bf16x8 q0 = __builtin_bit_cast(bf16x8, ((u32x4){qv[tt][0].x, qv[tt][0].y, qv[tt][1].x, qv[tt][1].y}));
      const bf16x8 q1 = __builtin_bit_cast(bf16x8, ((u32x4){kq == tt ? qx.x : 0u, kq == tt ? qx.y : 0u, 0u, 0u}));
      const f32x4 z = {0, 0, 0, 0};
      const f32x4 den = MFMA(kvf[3][1], q1, MFMA(kvf[3][0], q0, z));
      f32x4 num[3];
#pragma unroll
      for (int nt = 0; nt < 3; ++nt) num[nt] = MFMA(kvf[nt][1], q1, MFMA(kvf[nt][0], q0, z));
      const float r0 = __builtin_amdgcn_rcpf(den[0] + 1e-6f), r1 = __builtin_amdgcn_rcpf(den[1] + 1e-6f), rm = kq == 0 ? r0 : r1;
      const unsigned so = WS_HX + (p * 8 + sw * 4 + tt) * (90 * 1024);
      ST16(ws, lane * 16, so + (72 + c) * 1024, ((u32x4){cvt_pk(num[0][0] * r0, num[0][1] * r0), cvt_pk(num[0][2] * r0, num[0][3] * r0),
                                                         cvt_pk(num[1][0] * rm, num[1][1] * rm), cvt_pk(num[1][2] * r1, num[1][3] * r1)}));
      ST8(ws, kq == 0 ? r16 * 16 : oob, so + (88 + (c >> 3)) * 1024 + ((c & 7) >> 1) * 256 + (c & 1) * 8,
          ((u32x2){cvt_pk(num[2][0] * r1, num[2][1] * r1), cvt_pk(num[2][2] * r1, num[2][3] * r1)}));
    }
    if constexpr (N == 26) { if (lt == 0) flag_st(P.ws, WS_F3O + p * 128 + c * 4, X.tag); }
    SB0();
  });
  if (X.bid == 0 && sw == 0) {
#pragma unroll 1
    for (int n = 32; n < 42; ++n) BAR();
    unsigned f4[4];
#pragma unroll
    for (int i = 0; i < 4; ++i) f4[i] = flag_ld(P.ws, WS_F3O + (i * 4 + kq) * 128 + r16 * 4);
    BAR(); BAR(); BAR();
    if (!(all_eq(f4[0], X.tag) && all_eq(f4[1], X.tag) && all_eq(f4[2], X.tag) && all_eq(f4[3], X.tag))) {
#pragma unroll 1
      for (int i = 0; i < 4; ++i) spin_flag(P.ws, WS_F3O + (i * 4 + kq) * 128 + r16 * 4, X.tag);
    }
    if (lane == 0) flag_st(P.ws, WS_EPOCH, X.tag);
  }
}

template <int DH>
DI void c_compute(const Params& P, const Ctx& X, LAS char* smem) {
  constexpr int ND = DH ? 2 : 3;
  int lane = lane_id();
  const int r16 = lane & 15, kq = lane >> 4, th = X.wid & 1;
  const int rg = 2 * X.p + (X.c >> 3), cg = X.c & 7;
  f32x4 acc[ND][2];
#pragma unroll
  for (int a = 0; a < ND; ++a) { acc[a][0] = (f32x4){0, 0, 0, 0}; acc[a][1] = (f32x4){0, 0, 0, 0}; }
  const int woff = DH * 2048 + lane * 16, woff4 = 4096 + kq * 128 + (r16 & 7) * 16;
  const int hoff = 18432 + th * 8192 + lane * 16;
  bf16x8 wa[2][ND], ha[2][2], wb[2][ND], hb[2][2];
#define C_RD(W, H, slot, kp) do { LAS char* b_ = smem + (slot) * STG; \
    _Pragma("unroll") for (int k = 0; k < 2; ++k) { \
      H[k][0] = *(LAS bf16x8*)(b_ + hoff + ((kp) * 2 + k) * 1024); H[k][1] = *(LAS bf16x8*)(b_ + hoff + 4096 + ((kp) * 2 + k) * 1024); \
      W[k][0] = *(LAS bf16x8*)(b_ + woff + ((kp) * 2 + k) * 4608); W[k][1] = *(LAS bf16x8*)(b_ + woff + 1024 + ((kp) * 2 + k) * 4608); \
      if (ND == 3) W[k][ND - 1] = *(LAS bf16x8*)(b_ + woff4 + ((kp) * 2 + k) * 4608); } } while (0)
#define C_MM(W, H) do { _Pragma("unroll") for (int k = 0; k < 2; ++k) _Pragma("unroll") for (int i = 0; i < ND; ++i) { \
    acc[i][0] = MFMA(W[k][i], H[k][0], acc[i][0]); acc[i][1] = MFMA(W[k][i], H[k][1], acc[i][1]); } } while (0)
#define C_ILV() do { _Pragma("unroll") for (int i_ = 0; i_ < ND + 2; ++i_) { SGB(0x100, 2); SGB(0x8, 1); } SGB(0x8, 3 * ND - 2); } while (0)
#define C_STAGE(n) do { WAIT_L0(); BAR(); const int slot = (n) & 3; \
    C_RD(wa, ha, slot, 0); C_MM(wb, hb); C_ILV(); SB0(); \
    C_RD(wb, hb, slot, 1); C_MM(wa, ha); C_ILV(); SB0(); } while (0)
  BAR();
  C_RD(wb, hb, 0, 0);
  C_STAGE(33);
  wait_v<0>();
  if (lane == 0) flag_st(P.ws, WS_F4 + (X.p * 2 + (X.wid >> 1)) * 128 + (X.c * 2 + (X.wid & 1)) * 4, X.tag);
  STAMP(2, 1);
  u32x4 xr[2]; u32x2 xo[2]; size_t orow[2];
#pragma unroll
  for (int tt = 0; tt < 2; ++tt) {
    orow[tt] = (size_t)(rg * 64 + slot_row(th * 32 + tt * 16 + r16)) * 1152 + cg * 144;
    xr[tt] = *(const u32x4*)((const char*)P.x + orow[tt] + DH * 64 + kq * 16);
    if (DH == 0) xo[tt] = *(const u32x2*)((const char*)P.x + orow[tt] + 128 + (kq & 1) * 8);
  }
  C_STAGE(34);
#pragma unroll 1
  for (int n = 35; n < 55; n += 4) { C_STAGE(3); C_STAGE(0); C_STAGE(1); C_STAGE(2); }
  C_MM(wb, hb);
  SB0();
  STAMP(2, 2);
#pragma unroll
  for (int tt = 0; tt < 2; ++tt) {
    const u32x4 x = xr[tt]; const f32x4 a0 = acc[0][tt], a1 = acc[1][tt];
    u32x4 v;
    v.x = cvt_pk(bf_lo(x.x) + a0[0], bf_hi(x.x) + a0[1]); v.y = cvt_pk(bf_lo(x.y) + a0[2], bf_hi(x.y) + a0[3]);
    v.z = cvt_pk(bf_lo(x.z) + a1[0], bf_hi(x.z) + a1[1]); v.w = cvt_pk(bf_lo(x.w) + a1[2], bf_hi(x.w) + a1[3]);
    *(u32x4*)((char*)P.out + orow[tt] + DH * 64 + kq * 16) = v;
    if (DH == 0) {
      const f32x4 a2 = acc[ND - 1][tt];
      u32x2 w; w.x = cvt_pk(bf_lo(xo[tt].x) + a2[0], bf_hi(xo[tt].x) + a2[1]); w.y = cvt_pk(bf_lo(xo[tt].y) + a2[2], bf_hi(xo[tt].y) + a2[3]);
      if (kq < 2) *(u32x2*)((char*)P.out + orow[tt] + 128 + kq * 8) = w;
    }
  }
  STAMP(2, 3);
}

DI void b_compute(const Params& P, const Ctx& X, LAS char* smem) {
  int lane = lane_id();
  const int wid = X.wid, p = X.p, c = X.c, r16 = lane & 15, kq = lane >> 4;
  const brs_t ws = __builtin_amdgcn_make_buffer_rsrc(P.ws, 0, WS_END, 0x00020000);
  f32x4 acc[9][2];
#pragma unroll
  for (int a = 0; a < 9; ++a) { acc[a][0] = (f32x4){0, 0, 0, 0}; acc[a][1] = (f32x4){0, 0, 0, 0}; }
  const int zoff = lane * 16;
  const int woff0 = 18432 + (wid * 32 + r16) * 128 + ((kq ^ (r16 >> 1)) * 16);
  const int woff1 = 18432 + (wid * 32 + r16) * 128 + (((kq + 4) ^ (r16 >> 1)) * 16);
  bf16x8 za[9], wa[2], zb[9], wb[2];
#define B_RD(Z, W, slot, kb) do { LAS char* b_ = smem + (slot) * STG; \
    W[0] = *(LAS bf16x8*)(b_ + ((kb) ? woff1 : woff0)); W[1] = *(LAS bf16x8*)(b_ + ((kb) ? woff1 : woff0) + 2048); \
    _Pragma("unroll") for (int i = 0; i < 9; ++i) Z[i] = *(LAS bf16x8*)(b_ + zoff + (i < 8 ? (kb) * 8192 + i * 1024 : 16384 + (kb) * 1024)); } while (0)
#define B_MM(Z, W) do { _Pragma("unroll") for (int i = 0; i < 9; ++i) { \
    acc[i][i & 1] = MFMA(Z[i], W[i & 1], acc[i][i & 1]); acc[i][(i & 1) ^ 1] = MFMA(Z[i], W[(i & 1) ^ 1], acc[i][(i & 1) ^ 1]); } } while (0)
#define B_ILV() do { _Pragma("unroll") for (int i_ = 0; i_ < 11; ++i_) { SGB(0x8, 1); SGB(0x100, 1); } SGB(0x8, 7); } while (0)
#define B_STAGE(n) do { WAIT_L0(); BAR(); const int slot = (n) & 3; \
    B_RD(za, wa, slot, 0); B_MM(zb, wb); B_ILV(); SB0(); \
    B_RD(zb, wb, slot, 1); B_MM(za, wa); B_ILV(); SB0(); } while (0)
  BAR();
  STAMP(1, 1);
  B_RD(za, wa, 0, 0);
  B_RD(zb, wb, 0, 1);
  B_MM(za, wa);
  B_STAGE(1);
#pragma unroll 1
  for (int n = 2; n < 18; n += 4) {
    B_STAGE(2); B_STAGE(3);
    if (n == 2) wait_v<0>();
    B_STAGE(0); B_STAGE(1);
  }
  u32x4 z1[2][4]; u32x2 z1o[2]; u16 bsv[2];
  sfor<18, 32>([&](auto N_) {
    constexpr int N = decltype(N_)::value, tt = (N - 18) / 6, m = (N - 18) % 6;
    if constexpr (N < 30) {
      const unsigned so = WS_Z1 + (p * 8 + wid * 2 + tt) * (72 * 1024);
      if constexpr (m < 4) z1[tt][m] = LD16(ws, zoff + m * 1024, so + c * 4096);
      else if constexpr (m == 4) z1o[tt] = LD8(ws, zoff, so + (64 + (c >> 1)) * 1024 + (c & 1) * 8);
      else bsv[tt] = P.bsp[p * 128 + slot_row(wid * 32 + tt * 16 + r16)];
      SB0();
    }
    B_STAGE(N);
  });
  B_MM(zb, wb);
  SB0();
  STAMP(1, 2);
#pragma unroll
  for (int tt = 0; tt < 2; ++tt) {
    const float b = bf_lo(bsv[tt]) * (1.0f / GC);
    const unsigned so = WS_HX + (p * 8 + wid * 2 + tt) * (90 * 1024);
#pragma unroll
    for (int m = 0; m < 4; ++m) {
      const u32x4 z = z1[tt][m]; const f32x4 g0 = acc[2 * m][tt] * (1.0f / GC) + b, g1 = acc[2 * m + 1][tt] * (1.0f / GC) + b;
      u32x4 v;
      v.x = cvt_pk(bf_lo(z.x) * g0[0], bf_hi(z.x) * g0[1]); v.y = cvt_pk(bf_lo(z.y) * g0[2], bf_hi(z.y) * g0[3]);
      v.z = cvt_pk(bf_lo(z.z) * g1[0], bf_hi(z.z) * g1[1]); v.w = cvt_pk(bf_lo(z.w) * g1[2], bf_hi(z.w) * g1[3]);
      ST16(ws, lane * 16 + m * 1024, so + c * 4096, v);
    }
    const u32x2 z = z1o[tt]; const f32x4 g0 = acc[8][tt] * (1.0f / GC) + b;
    u32x2 v; v.x = cvt_pk(bf_lo(z.x) * g0[0], bf_hi(z.x) * g0[1]); v.y = cvt_pk(bf_lo(z.y) * g0[2], bf_hi(z.y) * g0[3]);
    ST8(ws, (kq >> 1) * 256 + r16 * 16 + (kq & 1) * 8, so + (64 + (c >> 1)) * 1024 + (c & 1) * 512, v);
  }
  STAMP(1, 3);
}

__global__ NOPK __launch_bounds__(512, 2) void kF(const Params P) {
  extern __shared__ __attribute__((aligned(16))) char smem_[];
  LAS char* smem = (LAS char*)smem_;
  Ctx X;
  X.wid = __builtin_amdgcn_readfirstlane(threadIdx.x >> 6); X.bid = blockIdx.x;
  const int xcd = X.bid & 7, idx = X.bid >> 3, wid = X.wid, lane = threadIdx.x & 63;
  X.pa = (xcd >> 2) * 8 + (idx >> 2); X.quad = xcd & 3; X.jj = idx & 3;
  X.p = (xcd >> 1) * 4 + (idx >> 3); X.c = (xcd & 1) * 8 + (idx & 7);
  STAMP(0, 0);
  if (wid == 7) warm_code(X, smem);
  const int sl0 = ((wid + 2 * X.jj) & 7) * 16 + (lane >> 3);
  u32x4 xr[2][9];
#pragma unroll
  for (int ln = 0; ln < 9; ++ln)
#pragma unroll
    for (int hh = 0; hh < 2; ++hh)
      xr[hh][ln] = *(const u32x4*)((const char*)P.x + (size_t)(X.pa * 128 + slot_row(sl0 + hh * 8)) * 1152 + (lane & 7) * 16 + ln * 128);
  const unsigned ep = flag_ld(P.ws, WS_EPOCH);
  SB0();
  SB0();
  float rs_[2], sh_[2];
  {
    const bf16x2_t one = {(__bf16)1.0f, (__bf16)1.0f};
    float s[2] = {0.f, 0.f}, q[2] = {0.f, 0.f};
#pragma unroll
    for (int ln = 0; ln < 9; ++ln)
#pragma unroll
      for (int i = 0; i < 4; ++i)
#pragma unroll
        for (int hh = 0; hh < 2; ++hh) {
          const unsigned ua = xr[hh][ln][i];
          const bf16x2_t a = __builtin_bit_cast(bf16x2_t, ua);
          s[hh] = __builtin_amdgcn_fdot2_f32_bf16(a, one, s[hh], false); q[hh] = __builtin_amdgcn_fdot2_f32_bf16(a, a, q[hh], false);
        }
#pragma unroll
    for (int hh = 0; hh < 2; ++hh) {
      const float m = oct_sum(s[hh]) * (1.0f / 576.0f);
      const float var = __builtin_fmaxf(oct_sum(q[hh]) * (1.0f / 576.0f) - m * m, 0.f);
      rs_[hh] = __builtin_amdgcn_rsqf(var + 1e-5f) * (X.jj < 3 ? GC : 1.0f); sh_[hh] = -m * rs_[hh];
      PIN(rs_[hh]); PIN(sh_[hh]);
    }
  }
  X.tag = __builtin_amdgcn_readfirstlane(ep) + 1u;
  STAMP(0, 1);
  {
    const int r0 = ((idx >> 2) & 7) * 72;
    if (X.jj < 3) {
      if (wid < 6) {
        const rsrc_t rs = MKRS(P.win, 576 * 9216);
        const int col = (wid < 3 ? 2304 + (3 * (X.quad * 3 + X.jj) + wid) * 64 : (3 * (X.quad * 3 + X.jj) + wid - 3) * 64) * 2;
        TOUCH(rs, lane * 9216, r0 * 9216 + col, smem + L_SCR);
        if (lane < 8) TOUCH(rs, lane * 9216, (r0 + 64) * 9216 + col, smem + L_SCR);
      }
    } else {
      const rsrc_t rs = MKRS(P.wqkv, 576 * 3456);
      for (int i = wid; i < 9; i += 8) {
        const int col = (i / 3) * 1152 + (X.quad * 2 + i % 3) * 128;
        TOUCH(rs, lane * 3456, r0 * 3456 + col, smem + L_SCR);
        if (lane < 8) TOUCH(rs, lane * 3456, (r0 + 64) * 3456 + col, smem + L_SCR);
      }
    }
  }
  if (wid >= 4) TOUCH(MKRS(P.wsp, 2048 * 4096), lane * 128, (X.bid * 8 + (wid - 4) * 2) * 4096, smem + L_SCR);
  SB0();
#pragma unroll
  for (int ln = 0; ln < 9; ++ln)
#pragma unroll
    for (int hh = 0; hh < 2; ++hh) {
      u32x4 v = xr[hh][ln];
#pragma unroll
      for (int i = 0; i < 4; ++i) v[i] = cvt_pk(__builtin_fmaf(bf_lo(v[i]), rs_[hh], sh_[hh]), __builtin_fmaf(bf_hi(v[i]), rs_[hh], sh_[hh]));
      const int sl = sl0 + hh * 8;
      *(LAS u32x4*)(smem + A_XN + ln * 16384 + sl * 128 + (((lane & 7) ^ ((sl >> 1) & 7)) * 16)) = v;
      SB0();
    }
  WAIT_L0(); BAR();
  STAMP(0, 2);
  if (X.jj < 3) a_zblock(P, X, smem);
  else if (wid < 4) a_qkvblock<0>(P, X, smem);
  else a_qkvblock<1>(P, X, smem);
  STAMP(0, 11);
  if (wid < 4) {
    b_compute(P, X, smem);
    if (wid < 2) c_compute<0>(P, X, smem); else c_compute<1>(P, X, smem);
  } else if (wid == 4) bc_loader<0>(P, X, smem);
  else if (wid == 5) bc_loader<1>(P, X, smem);
  else bc_side(P, X, smem);
}

extern "C" __device__ __attribute__((used, noinline)) void kf_code_end() {}

extern "C" void kernel_launch(void* const* d_in, const int* in_sizes, int n_in, void* d_out, int out_size, void* d_ws,
                              size_t ws_size, hipStream_t stream) {
  Params P;
  P.x = (const u16*)d_in[0]; P.wqkv = (const u16*)d_in[1]; P.wo = (const u16*)d_in[2]; P.win = (const u16*)d_in[4];
  P.wsp = (const u16*)d_in[5]; P.bsp = (const u16*)d_in[6]; P.wout = (const u16*)d_in[7];
  P.out = (u16*)d_out; P.ws = (char*)d_ws;
  (void)hipFuncSetAttribute((const void*)kF, hipFuncAttributeMaxDynamicSharedMemorySize, LDS_BYTES);
  kF<<<256, 512, LDS_BYTES, stream>>>(P);
}
```

```cpp
#include <hip/hip_runtime.h>
#include <hip/hip_bf16.h>

#if defined(__HIP_DEVICE_COMPILE__)
#define NOPK __attribute__((target("no-packed-fp32-ops")))
#else
#define NOPK
#endif
#define LAS __attribute__((address_space(3)))
#define GAS __attribute__((address_space(1)))
typedef unsigned short u16;
typedef unsigned long long u64;
typedef short bf16x8 __attribute__((ext_vector_type(8)));
typedef short s16x4 __attribute__((ext_vector_type(4)));
typedef float f32x4 __attribute__((ext_vector_type(4)));
typedef float f32x2 __attribute__((ext_vector_type(2)));
typedef float f32x16 __attribute__((ext_vector_type(16)));
typedef unsigned u32x4 __attribute__((ext_vector_type(4)));
typedef unsigned u32x2 __attribute__((ext_vector_type(2)));
typedef __bf16 bf16x2_t __attribute__((ext_vector_type(2)));
typedef __amdgpu_buffer_rsrc_t brs_t;
typedef u32x4 rsrc_t;

#define WAIT_L0() asm volatile("s_waitcnt lgkmcnt(0)" ::: "memory")
#define BAR() do { asm volatile("" ::: "memory"); __builtin_amdgcn_s_barrier(); asm volatile("" ::: "memory"); } while (0)
#define SB0() __builtin_amdgcn_sched_barrier(0)
#define SGB(mask, n) __builtin_amdgcn_sched_group_barrier(mask, n, 0)
#define PIN(x) asm volatile("" : "+v"(x))
#define MFMA(a, b, c) __builtin_amdgcn_mfma_f32_16x16x32_bf16(a, b, c, 0, 0, 0)
#define MFMA32(a, b, c) __builtin_amdgcn_mfma_f32_32x32x16_bf16(a, b, c, 0, 0, 0)
#define DI __device__ __forceinline__
template <int N> DI void wait_v() { asm volatile("s_waitcnt vmcnt(%0)" :: "i"(N) : "memory"); }
template <int V> struct IC { static constexpr int value = V; };
template <int I, int N, class F> DI void sfor(F&& f) {
  if constexpr (I < N) { f(IC<I>{}); sfor<I + 1, N>(f); }
}

DI rsrc_t MKRS(const void* p, unsigned bytes) {
  const u64 a = (u64)p;
  return (rsrc_t){(unsigned)a, (unsigned)(a >> 32) & 0xffffu, bytes, 0x00020000u};
}
DI void BLDS(rsrc_t rs, unsigned voff, unsigned soff, LAS char* l) {
  asm volatile("s_mov_b32 m0, %3\n\ts_nop 0\n\tbuffer_load_dwordx4 %0, %1, %2 offen lds"
               :: "v"(voff), "s"(rs), "s"(soff), "s"((unsigned)(size_t)l) : "memory");
}
DI void BLDS1(rsrc_t rs, unsigned voff, unsigned soff, LAS char* l) {
  asm volatile("s_mov_b32 m0, %3\n\ts_nop 0\n\tbuffer_load_dwordx4 %0, %1, %2 offen sc1 lds"
               :: "v"(voff), "s"(rs), "s"(soff), "s"((unsigned)(size_t)l) : "memory");
}
template <int N> DI void BLDSx(rsrc_t rs, unsigned voff, unsigned soff, LAS char* l) {
  if (N == 4)
    asm volatile("s_mov_b32 m0, %3\n\ts_nop 0\n\tbuffer_load_dwordx4 %0, %1, %2 offen sc1 lds\n\tbuffer_load_dwordx4 %0, %1, %2 offen offset:1024 sc1 lds\n\t"
                 "buffer_load_dwordx4 %0, %1, %2 offen offset:2048 sc1 lds\n\tbuffer_load_dwordx4 %0, %1, %2 offen offset:3072 sc1 lds"
                 :: "v"(voff), "s"(rs), "s"(soff), "s"((unsigned)(size_t)l) : "memory");
  else if (N == 2)
    asm volatile("s_mov_b32 m0, %3\n\ts_nop 0\n\tbuffer_load_dwordx4 %0, %1, %2 offen sc1 lds\n\tbuffer_load_dwordx4 %0, %1, %2 offen offset:1024 sc1 lds"
                 :: "v"(voff), "s"(rs), "s"(soff), "s"((unsigned)(size_t)l) : "memory");
  else
    asm volatile("s_mov_b32 m0, %3\n\ts_nop 0\n\tbuffer_load_dwordx4 %0, %1, %2 offen sc1 lds"
                 :: "v"(voff), "s"(rs), "s"(soff), "s"((unsigned)(size_t)l) : "memory");
}
DI void BLDS4v(rsrc_t rs, const unsigned (&v)[4], unsigned soff, LAS char* l) {
  asm volatile("s_mov_b32 m0, %6\n\ts_nop 0\n\tbuffer_load_dwordx4 %0, %4, %5 offen lds\n\tbuffer_load_dwordx4 %1, %4, %5 offen offset:1024 lds\n\t"
               "buffer_load_dwordx4 %2, %4, %5 offen offset:2048 lds\n\tbuffer_load_dwordx4 %3, %4, %5 offen offset:3072 lds"
               :: "v"(v[0]), "v"(v[1]), "v"(v[2]), "v"(v[3]), "s"(rs), "s"(soff), "s"((unsigned)(size_t)l) : "memory");
}
DI void BLDS32(rsrc_t rs, unsigned voff, unsigned soff, LAS char* l) {
  asm volatile("s_mov_b32 m0, %3\n\ts_mov_b32 exec_hi, 0\n\tbuffer_load_dwordx4 %0, %1, %2 offen lds\n\ts_mov_b32 exec_hi, -1"
               :: "v"(voff), "s"(rs), "s"(soff), "s"((unsigned)(size_t)l) : "memory");
}
DI void BLDS3v(rsrc_t rs, const unsigned (&v)[3], unsigned soff, LAS char* l) {
  asm volatile("s_mov_b32 m0, %5\n\ts_nop 0\n\tbuffer_load_dwordx4 %0, %3, %4 offen lds\n\tbuffer_load_dwordx4 %1, %3, %4 offen offset:1024 lds\n\t"
               "buffer_load_dwordx4 %2, %3, %4 offen offset:2048 lds"
               :: "v"(v[0]), "v"(v[1]), "v"(v[2]), "s"(rs), "s"(soff), "s"((unsigned)(size_t)l) : "memory");
}
DI void BLDS15v(rsrc_t rs, const unsigned (&v)[3], unsigned soff, LAS char* l) {
  asm volatile("s_mov_b32 m0, %4\n\ts_nop 0\n\tbuffer_load_dwordx4 %0, %2, %3 offen lds\n\ts_mov_b32 exec_hi, 0\n\t"
               "buffer_load_dwordx4 %1, %2, %3 offen offset:1024 lds\n\ts_mov_b32 exec_hi, -1"
               :: "v"(v[0]), "v"(v[1]), "s"(rs), "s"(soff), "s"((unsigned)(size_t)l) : "memory");
}
template <int OFF> DI void AST16(rsrc_t rs, unsigned voff, unsigned soff, u32x4 v) {
  asm volatile("buffer_store_dwordx4 %0, %1, %2, %3 offen offset:%4 sc1\n\ts_nop 1" :: "v"(v), "v"(voff), "s"(rs), "s"(soff), "i"(OFF) : "memory");
}
DI void BLDS16(rsrc_t rs, unsigned voff, unsigned soff, LAS char* l) {
  asm volatile("s_mov_b32 m0, %3\n\ts_mov_b64 exec, 0xffff\n\tbuffer_load_dwordx4 %0, %1, %2 offen lds\n\ts_mov_b64 exec, -1"
               :: "v"(voff), "s"(rs), "s"(soff), "s"((unsigned)(size_t)l) : "memory");
}
DI void TOUCH(rsrc_t rs, unsigned voff, unsigned soff, LAS char* l) {
  asm volatile("s_mov_b32 m0, %3\n\ts_nop 0\n\tbuffer_load_dword %0, %1, %2 offen lds"
               :: "v"(voff), "s"(rs), "s"(soff), "s"((unsigned)(size_t)l) : "memory");
}
DI void ald16(u32x4& d, rsrc_t rs, unsigned voff, unsigned soff) {
  asm volatile("buffer_load_dwordx4 %0, %1, %2, %3 offen sc1" : "=v"(d) : "v"(voff), "s"(rs), "s"(soff) : "memory");
}
DI void ald8(u32x2& d, rsrc_t rs, unsigned voff, unsigned soff) {
  asm volatile("buffer_load_dwordx2 %0, %1, %2, %3 offen sc1" : "=v"(d) : "v"(voff), "s"(rs), "s"(soff) : "memory");
}
DI void ald4(unsigned& d, rsrc_t rs, unsigned voff, unsigned soff) {
  asm volatile("buffer_load_dword %0, %1, %2, %3 offen sc1" : "=v"(d) : "v"(voff), "s"(rs), "s"(soff) : "memory");
}
DI void gld8(u32x2& d, const void* p) { asm volatile("global_load_dwordx2 %0, %1, off" : "=v"(d) : "v"(p) : "memory"); }
#define ST16(rs, voff, soff, v) __builtin_amdgcn_raw_buffer_store_b128(v, rs, (int)((voff) + (soff)), 0, 16)
#define ST8(rs, voff, soff, v) __builtin_amdgcn_raw_buffer_store_b64(v, rs, (int)(voff), (int)(soff), 16)
#define ST4(rs, voff, soff, v) __builtin_amdgcn_raw_buffer_store_b32(v, rs, (int)(voff), (int)(soff), 16)
#define LD16(rs, voff, soff) __builtin_amdgcn_raw_buffer_load_b128(rs, (int)(voff), (int)(soff), 16)
#define LD8(rs, voff, soff) __builtin_amdgcn_raw_buffer_load_b64(rs, (int)(voff), (int)(soff), 16)

constexpr unsigned WS_EPOCH = 0;
constexpr unsigned WS_F1 = 1024;
constexpr unsigned WS_F2K = WS_F1 + 4096;
constexpr unsigned WS_F2Z = WS_F2K + 2048;
constexpr unsigned WS_F3O = WS_F2Z + 2048;
constexpr unsigned WS_F4 = WS_F3O + 2048;
constexpr unsigned WS_STAT = 20480;
constexpr unsigned WS_KVP = WS_STAT + 16 * 12 * 256 * 8;
constexpr unsigned WS_KVF = WS_KVP + 64 * 36864;
constexpr unsigned WS_PQ = WS_KVF + 16 * 5632;
constexpr unsigned WS_WX = WS_PQ + 128 * 16 * 1152;
constexpr unsigned WS_Z2N = WS_WX + 8 * 90 * 4608;
constexpr unsigned WS_Z2O = WS_Z2N + 16 * 64 * 8 * 1024;
constexpr unsigned WS_Z1 = WS_Z2O + 64 * 16 * 1024;
constexpr unsigned WS_HX = WS_Z1 + 128 * 72 * 1024;
constexpr unsigned WS_END = WS_HX + 128 * 90 * 1024;
constexpr unsigned SPIN_MAX = 1u << 15;

#define STAMP(K, i) do {} while (0)
#define STAMP_OUT() do {} while (0)
#define FINE_DECL() do {} while (0)
#define FINE(n) do {} while (0)
#define FINE_OUT(n) do {} while (0)

struct Params {
  const u16* x; const u16* wqkv; const u16* wo; const u16* win; const u16* wsp; const u16* bsp; const u16* wout;
  u16* out; char* ws;
};

DI int lane_id() {
  int l; asm volatile("v_mbcnt_lo_u32_b32 %0, -1, 0\n\tv_mbcnt_hi_u32_b32 %0, -1, %0" : "=v"(l)); return l;
}
DI unsigned cvt_pk(float lo, float hi) {
  f32x2 v = {lo, hi}; bf16x2_t b = __builtin_convertvector(v, bf16x2_t); return __builtin_bit_cast(unsigned, b);
}
DI u32x4 norm4(u32x4 v, unsigned rlo, unsigned rhi, float sh) {
  unsigned a = v.x, b = v.y, c = v.z, d = v.w; float t0, t1, t2, t3;
  asm("v_dot2_f32_bf16 %4, %0, %8, %10\n\tv_dot2_f32_bf16 %0, %0, %9, %10\n\t"
      "v_dot2_f32_bf16 %5, %1, %8, %10\n\tv_dot2_f32_bf16 %1, %1, %9, %10\n\t"
      "v_dot2_f32_bf16 %6, %2, %8, %10\n\tv_dot2_f32_bf16 %2, %2, %9, %10\n\t"
      "v_dot2_f32_bf16 %7, %3, %8, %10\n\tv_dot2_f32_bf16 %3, %3, %9, %10\n\t"
      "v_cvt_pk_bf16_f32 %0, %4, %0\n\tv_cvt_pk_bf16_f32 %1, %5, %1\n\tv_cvt_pk_bf16_f32 %2, %6, %2\n\tv_cvt_pk_bf16_f32 %3, %7, %3"
      : "+v"(a), "+v"(b), "+v"(c), "+v"(d), "=&v"(t0), "=&v"(t1), "=&v"(t2), "=&v"(t3) : "v"(rlo), "v"(rhi), "v"(sh));
  return (u32x4){a, b, c, d};
}
DI float bf_lo(unsigned v) { return __uint_as_float(v << 16); }
DI float bf_hi(unsigned v) { return __uint_as_float(v & 0xffff0000u); }
constexpr float GC = 0.4686686931f, GKK = 4.912229539f;
DI float gelu_s(float x) {
  const float e = __builtin_amdgcn_exp2f(-x * __builtin_fmaf(x, x, GKK));
  return x * __builtin_amdgcn_rcpf(1.0f + e);
}
DI float phi_f(float t) {
  return __builtin_fmaxf(t, 1e-6f) + __builtin_amdgcn_fmed3f(__builtin_amdgcn_exp2f(t * 1.4426950409f), 0.f, 1.f);
}
DI bf16x8 tr_frag(LAS char* a0, LAS char* a1) {
  const s16x4 lo = __builtin_amdgcn_ds_read_tr16_b64_v4i16((LAS s16x4*)a0);
  const s16x4 hi = __builtin_amdgcn_ds_read_tr16_b64_v4i16((LAS s16x4*)a1);
  return __builtin_shufflevector(lo, hi, 0, 1, 2, 3, 4, 5, 6, 7);
}
template <int CTRL> DI float dpp_get(float v) {
  return __builtin_bit_cast(float, __builtin_amdgcn_update_dpp(0, __builtin_bit_cast(int, v), CTRL, 0xf, 0xf, true));
}
DI float oct_sum(float v) {
  v += dpp_get<0xB1>(v); v += dpp_get<0x4E>(v); v += dpp_get<0x141>(v); return v;
}
DI int slot_row(int rs) { return (rs & ~31) + 16 * ((rs >> 2) & 1) + 4 * ((rs >> 3) & 3) + (rs & 3); }
DI unsigned flag_ld(const char* ws, unsigned off) {
  return __hip_atomic_load((GAS unsigned*)(ws + off), __ATOMIC_RELAXED, __HIP_MEMORY_SCOPE_AGENT);
}
DI void flag_st(char* ws, unsigned off, unsigned v) {
  __hip_atomic_store((GAS unsigned*)(ws + off), v, __ATOMIC_RELAXED, __HIP_MEMORY_SCOPE_AGENT);
}
DI bool all_eq(unsigned v, unsigned tag) { return __builtin_amdgcn_ballot_w64(v != tag) == 0; }
DI void spin_flag(const char* ws, unsigned off, unsigned tag) {
  const rsrc_t rs = MKRS(ws, WS_END);
#pragma unroll 1
  for (unsigned i = 0; i < SPIN_MAX; ++i) {
    unsigned v;
    asm volatile("buffer_load_dword %0, %1, %2, 0 offen sc1\n\ts_waitcnt vmcnt(0)" : "=&v"(v) : "v"(off), "s"(rs) : "memory");
    if (all_eq(v, tag)) break;
    __builtin_amdgcn_s_sleep(1);
  }
}

constexpr int LDS_BYTES = 160 * 1024;
constexpr int L_SCR = 159 * 1024;
constexpr int A_XN = 15 * 1024;
constexpr int AZ_SLOT = 24 * 1024, AZ_PARK = 96 * 1024, AZ_ST = 144 * 1024, AZ_ST2 = 147 * 1024, AZ_TAB = 151 * 1024;
constexpr int AQ_SLOT = 20 * 1024, AQ_PK = 80 * 1024, AQ_V = 116 * 1024;
constexpr int STG = 34 * 1024, B_KV = 4 * STG;
#define TMODE 1
#define OUTAUX 2
#define XRD(ks) (*(LAS bf16x8*)(xb[(ks) & 3] + ((ks) >> 2) * 16384))
#define SLOT(n) ((((n) + 2) & 3) * STG)
#define ROTB 3
#define ROTC 3
#define ROT0 0
#define ROT(n, r, m) ((n) < ROT0 ? (n) : (((n) & ~(m)) | (((n) + (r)) & (m))))

struct Ctx {
  int pa, quad, jj, p, c, wid, bid;
  unsigned tag;
};

template <int ROLE> struct Loader {
  rsrc_t rsws, rsw; unsigned voff[4]; unsigned l16; int p, c; LAS char* smem;
  DI void init(const Params& P, const Ctx& X, LAS char* sm) {
    smem = sm; p = X.p; c = X.c;
    const int ln = lane_id();
    l16 = ln * 16;
    rsws = MKRS(P.ws, WS_END);
    if (ROLE == 1) {
      rsw = MKRS((const char*)P.wsp + (size_t)p * 128 * 4096, 128 * 4096);
#pragma unroll
      for (int i = 0; i < 4; ++i) {
        const int rs = i * 8 + (ln >> 3);
        voff[i] = (unsigned)(slot_row(rs) * 4096 + (((ln & 7) ^ ((rs & 15) >> 1)) * 16) - i * 1024);
      }
    }
  }
  DI void issue_b(int N) {
    LAS char* d = smem + SLOT(N);
    const int n = ROT(N, p + c, ROTB);
    if (ROLE == 0) {
      const unsigned so = WS_Z2N + c * (64 * 8 * 1024) + n * 16384;
#pragma unroll
      for (int i = 0; i < 4; ++i) BLDSx<4>(rsws, l16, so + i * 4096, d + i * 4096);
      BLDSx<1>(rsws, l16, WS_Z2O + (n * 32 + c) * 1024, d + 16384);
      BLDSx<1>(rsws, l16, WS_Z2O + (n * 32 + 16 + c) * 1024, d + 17408);
    } else {
#pragma unroll
      for (int i = 0; i < 4; ++i) BLDS4v(rsw, voff, n * 128 + i * (32 * 4096), d + 18432 + i * 4096);
    }
  }
  template <int NK> DI void issue_c(int N) {
    LAS char* d = smem + SLOT(N);
    const int M = N - 32, rg = 2 * p + (c >> 3), cg = c & 7;
    const int kb = M == 0 ? 88 : M < 5 ? 72 + 4 * (M - 1) : 4 * (M < 21 ? ROT(M - 5, p + c, ROTC) : M - 5);
    if (ROLE == 0) {
      const unsigned so = WS_WX + (cg * 90 + kb) * 4608;
#pragma unroll
      for (int i = 0; i < NK; ++i) BLDSx<4>(rsws, l16, so + i * 4096, d + i * 4096);
      BLDSx<NK / 2>(rsws, l16, so + NK * 4096, d + NK * 4096);
    } else {
#pragma unroll
      for (int i = 0; i < 4; ++i) BLDSx<NK>(rsws, l16, WS_HX + ((rg * 4 + i) * 90 + kb) * 1024, d + 18432 + i * 4096);
    }
  }
};
DI void first_w(const Params& P, const Ctx& X, LAS char* smem) {
  const int ln = lane_id(), st = X.wid >> 2, g = X.wid & 3;
  unsigned voff[4];
#pragma unroll
  for (int i = 0; i < 4; ++i) {
    const int rs = i * 8 + (ln >> 3);
    voff[i] = (unsigned)(slot_row(rs) * 4096 + (((ln & 7) ^ ((rs & 15) >> 1)) * 16) - i * 1024);
  }
  BLDS4v(MKRS((const char*)P.wsp + (size_t)X.p * 128 * 4096, 128 * 4096), voff, ROT(st, X.p + X.c, ROTB) * 128 + g * (32 * 4096),
         smem + SLOT(st) + 18432 + g * 4096);
}
DI void first_z(const Params& P, const Ctx& X, LAS char* smem) {
  const int st = X.wid >> 2, g = X.wid & 3, n = ROT(st, X.p + X.c, ROTB);
  const unsigned l16 = lane_id() * 16;
  const rsrc_t rs = MKRS(P.ws, WS_END);
  BLDSx<4>(rs, l16, WS_Z2N + X.c * (64 * 8 * 1024) + n * 16384 + g * 4096, smem + SLOT(st) + g * 4096);
  BLDSx<1>(rs, l16, WS_Z2O + (n * 32 + (g & 1) * 16 + X.c) * 1024, smem + SLOT(st) + 16384 + (g & 1) * 1024);
}
DI void warm_code(const Ctx& X, LAS char* smem) {
  u64 pc; unsigned len;
  asm volatile("s_getpc_b64 %0\n1:\n\ts_mov_b32 %1, 1b-_Z2kF6Params" : "=s"(pc), "=s"(len));
  pc -= len;
  asm volatile("s_mov_b32 %0, kf_code_end-_Z2kF6Params" : "=s"(len));
  if ((int)len < 0) len = 0;
  TOUCH(MKRS((const void*)pc, len), lane_id() * 128, (X.bid >> 3) * 8192, smem + L_SCR);
}
DI void warm_b(const Params& P, const Ctx& X, LAS char* smem) {
  u64 pc; unsigned len;
  asm volatile("s_getpc_b64 %0\n1:\n\ts_mov_b32 %1, 1b-_Z2kF6Params" : "=s"(pc), "=s"(len));
  pc -= len;
  asm volatile("s_mov_b32 %0, kf_code_end-_Z2kF6Params" : "=s"(len));
  if ((int)len < 0) len = 0;
  const bool code = X.wid == 7;
  const int l = lane_id();
  TOUCH(MKRS(code ? (const void*)pc : (const void*)P.wsp, code ? len : X.wid == 6 ? 2048u * 4096u : 0u), code ? l * 128 : (l >> 2) * 4096 + (l & 3) * 128,
        code ? (X.bid >> 3) * 8192 : (X.p * 128 + (X.c & 7) * 16) * 4096, smem + L_SCR);
}
DI unsigned f1_off(const Ctx& X) { const int l = lane_id(); return WS_F1 + X.c * 256 + (l < 48 ? l : 47) * 4; }
DI int f1_src(int c, int w) { return (w == 0 ? 8 * c : w == 1 ? 8 * c + 7 : 128 + c) / 12; }

#define Z_XB1 140
#define Z_GAT 151
#define Z_XB2 164
#define Z_F1 200
#define Z_FW 211
#define Z_POLL 210
struct ZS {
  static constexpr int XB1 = Z_XB1, PUB = Z_XB1 + 2, GAT = Z_GAT, XB2 = Z_XB2, F1 = Z_F1, FW = Z_FW, POLL = Z_POLL, WARM = 135;
  static constexpr bool sbar(int E) { return E % 12 == 8 && E < 212; }
  static constexpr bool wsp(int E) { return !(TMODE & 4) && (E == 45 || E == 57 || E == 69); }
  static constexpr int nrm(int ch) { return XB2 + 6 + 2 * ch + ch / 3; }
  static constexpr bool dma(int E) { return E >= 12 && (E + 4) / 12 + 2 < 18 && E % 12 < 3; }
  static constexpr int other(int E) {
    int n = (E == PUB) + (E >= GAT && E < GAT + 3) + (E == F1) + (E == POLL) + (E == WARM) + wsp(E) + (E == 155 || E == 164 || E == 201);
    for (int ch = 0; ch < 6; ++ch) n += E == nrm(ch) + 1;
    return n;
  }
  static constexpr int younger(int SN) {
    const int last = 12 * SN - 22, bar = 12 * SN - 4;
    int n = (SN == 1 ? 3 : 0) + ((TMODE & 1) && SN <= 2 ? 1 : 0);
    for (int E = last < 0 ? 0 : last; E < bar; ++E) n += other(E) + ((E > last && dma(E)) ? 1 : 0);
    return n;
  }
  static constexpr int after_gather() {
    int n = 0;
    for (int E = GAT + 3; E < XB2 - 3; ++E) n += other(E) + (dma(E) ? 1 : 0);
    return n;
  }
  static constexpr int after_z2n() {
    int n = 0;
    for (int E = nrm(5) + 2; E < F1; ++E) n += other(E) + (dma(E) ? 1 : 0);
    return n;
  }
};
DI unsigned z_voff(int wid, int lane) {
  const int row = wid * 8 + (lane >> 3);
  return (unsigned)(row * 9216 + (((lane & 7) ^ (((row >> 1) & 1) << 2)) * 16));
}
DI void a_zblock(const Params& P, const Ctx& X, LAS char* smem, bf16x8 (&xf)[36], LAS char* (&xb)[4]) {
  const int wid = X.wid, p = X.pa, j = X.quad * 3 + X.jj;
  int lane = lane_id();
  const int rw = wid & 3, G = wid >> 2;
  const int h = lane >> 5;
  const rsrc_t rsw = MKRS(P.win, 576 * 9216), rsws = MKRS(P.ws, WS_END);
  const brs_t ws = __builtin_amdgcn_make_buffer_rsrc(P.ws, 0, WS_END, 0x00020000);
  const unsigned voff = z_voff(wid, lane);
  const int cz2 = (2304 + 3 * j * 64) * 2, cz1 = 3 * j * 64 * 2;
  auto dma = [&](auto S_, auto I_) {
    constexpr int S = decltype(S_)::value, I = decltype(I_)::value, R = S / 3;
    BLDS(rsw, voff, (R < 3 ? cz2 + R * 128 : cz1 + (R - 3) * 128) + ((S % 3) * 192 + I * 64) * 9216,
         smem + (S & 3) * AZ_SLOT + I * 8192 + wid * 1024);
  };
  const int rq = (lane >> 2) & 3;
  LAS char* rd = smem + (8 * h + rq) * 128 + ((G ^ (rq >> 1)) * 64) + ((lane >> 4) & 1) * 32 + (lane & 3) * 8;
  LAS char* park = smem + AZ_PARK + wid * 2048 + h * 1024 + (lane & 31) * 16;
  LAS char* strd = smem;
  const unsigned zo = WS_Z1 + ((p * 8 + rw * 2) * 72 + 6 * j + G) * 1024;
  unsigned zv = 0, nvo = 0, fpoll = 0, f1v = 0;
  const bf16x8 ones = {0x3f80, 0x3f80, 0x3f80, 0x3f80, 0x3f80, 0x3f80, 0x3f80, 0x3f80};
  f32x16 acc[2];
  bf16x8 wf[5], pf[2];
  f32x4 ssum[2], ssq[2];
  unsigned pk[4];
  float gt[4] = {0.f, 0.f, 0.f, 0.f}, gb = 0.f, xt[4];
  u32x4 gv[3];
  f32x4 tab[2][4];
  u32x4 nv[2];
  auto rdw = [&](auto E_) {
    constexpr int E = decltype(E_)::value, S = E / 12, o = (S & 3) * AZ_SLOT + (E % 12) * 2048;
    wf[E % 5] = tr_frag(rd + o, rd + o + 512);
  };
  auto stats_rd = [&](int rr) {
    if (rr == 0) {
      const int ln = lane_id();
      strd = smem + AZ_PARK + wid * 2048 + ((ln >> 1) & 1) * 512 + (8 * (ln >> 4) + ((ln >> 2) & 3)) * 16 + (ln & 1) * 8;
    }
#pragma unroll
    for (int u = 0; u < 2; ++u) pf[u] = tr_frag(strd + rr * 16384 + u * 1024, strd + rr * 16384 + u * 1024 + 64);
  };
  auto stats_mm = [&](auto RR_) {
    constexpr int RR = decltype(RR_)::value;
#pragma unroll
    for (int u = 0; u < 2; ++u) {
      const f32x4 zz = {0, 0, 0, 0};
      ssum[u] = MFMA(ones, pf[u], RR ? ssum[u] : zz);
      ssq[u] = MFMA(pf[u], pf[u], RR ? ssq[u] : zz);
      PIN(ssum[u]); PIN(ssq[u]);
    }
  };
  auto gelu_st = [&](auto RR_, auto EL_, auto ST_) {
    constexpr int RR = decltype(RR_)::value, EL = decltype(EL_)::value, ST = decltype(ST_)::value;
    if constexpr (EL >= 0 && EL < 16) {
      constexpr int reg = RR < 3 ? EL : (EL & 3) + ((EL >> 2) & 1) * 8 + (EL >> 3) * 4;
      const float x = acc[RR & 1][reg];
      float& g = gt[EL & 3];
      if (ST == 0) { g = __builtin_fmaf(x, x, GKK); PIN(g); }
      if (ST == 1) { g = __builtin_amdgcn_exp2f(-x * g); PIN(g); }
      if (ST == 2) { g = __builtin_amdgcn_rcpf(1.0f + g); PIN(g); }
      if (ST == 3) {
        const float y = x * g;
        if (EL & 1) { pk[(EL >> 1) & 3] = cvt_pk(gb, y); PIN(pk[(EL >> 1) & 3]); } else { gb = y; PIN(gb); }
      }
    }
  };
  auto put = [&](auto RR_, auto CC_) {
    constexpr int RR = decltype(RR_)::value, CC = decltype(CC_)::value;
    const u32x4 w = {pk[0], pk[1], pk[2], pk[3]};
    if constexpr (RR < 3) *(LAS u32x4*)(park + RR * 16384 + CC * 512) = w;
    else AST16<(RR - 3) * 2048 + CC * 512>(rsws, zv, zo, w);
  };
  auto gather = [&](int i) {
    const int t = wid * 64 + lane_id();
    ald16(gv[i], rsws, (t & 127) * 16 + ((t >> 7) * 3 + i) * 2048, WS_STAT + p * 12 * 2048);
  };
  FINE_DECL();
  FINE(0);
  sfor<0, 4>(rdw);
  sfor<0, 216>([&](auto E_) {
    constexpr int E = decltype(E_)::value, R = E / 36, ks = E % 36, EN = E + 4, SN = EN / 12;
    if constexpr (E == ZS::XB1 || E == ZS::XB2) asm volatile("s_waitcnt lgkmcnt(4)" ::: "memory");
    if constexpr (E == 8) asm volatile("s_waitcnt lgkmcnt(8)" ::: "memory");
    if constexpr (E == ZS::F1) wait_v<ZS::after_z2n()>();
    if constexpr (ZS::sbar(E)) wait_v<ZS::younger(SN)>();
    if constexpr (ZS::sbar(E) || E == ZS::XB1 || E == ZS::XB2 || E == ZS::F1) BAR();
    if constexpr (ZS::sbar(E)) FINE(SN);
    if constexpr (E == ZS::F1) ST4(ws, f1v, WS_F1, X.tag);
    if constexpr (E == ZS::F1 - 10) {
      const int ln = lane_id();
      f1v = (wid == 0 && ln < 48 && f1_src(ln & 15, ln >> 4) == j) ? (ln & 15) * 256 + ((ln >> 4) * 16 + p) * 4 : 0x7fffff00u; PIN(f1v);
    }
    if constexpr (EN < 216) {
      rdw(IC<EN>{});
      if constexpr (E < 10) { xf[16 + 2 * E] = XRD(16 + 2 * E); xf[17 + 2 * E] = XRD(17 + 2 * E); }
      if constexpr (ZS::dma(E)) dma(IC<SN + 2>{}, IC<EN % 12 - 4>{});
    }
    const f32x16 zz = {};
    if constexpr (R < 3) acc[R & 1] = MFMA32(xf[ks], wf[E % 5], ks ? acc[R & 1] : zz);
    else acc[R & 1] = MFMA32(wf[E % 5], xf[ks], ks ? acc[R & 1] : zz);
    PIN(acc[R & 1]);
    if constexpr ((R == 1 || R == 2 || R == 5) && ks >= 1) {
      gelu_st(IC<R - 1>{}, IC<(ks - 1) / 2>{}, IC<(ks - 1) & 1>{});
      gelu_st(IC<R - 1>{}, IC<(ks - 1) / 2 - 1>{}, IC<((ks - 1) & 1) + 2>{});
      if constexpr (ks == 21) put(IC<R - 1>{}, IC<0>{});
      if constexpr (ks == 34 && R < 5) put(IC<R - 1>{}, IC<1>{});
    }
    if constexpr (R == 3 || R == 4) {
      sfor<0, 4>([&](auto ST_) { gelu_st(IC<R - 1>{}, IC<ks - 1 - decltype(ST_)::value>{}, ST_); });
      if constexpr (ks == 11) put(IC<R - 1>{}, IC<0>{});
      if constexpr (ks == 20) put(IC<R - 1>{}, IC<1>{});
    }
    if constexpr (R == 3) {
      if constexpr (ks == 17 || ks == 20 || ks == 23) stats_mm(IC<(ks - 17) / 3>{});
      if constexpr (ks == 14 || ks == 17 || ks == 20) stats_rd((ks - 14) / 3);
      if constexpr (ks == 33) {
        const int ln = lane_id();
        zv = ((ln >> 4) & 1) * (72 * 1024) + (ln >> 5) * 256 + (ln & 15) * 16; PIN(zv);
        nvo = ((ln >> 4) & 1) * 1024 + (ln >> 5) * 512 + (ln & 15) * 16; PIN(nvo);
      }
    }
    if constexpr (E == ZS::XB1 - 3) {
      const int ln = lane_id();
      const int dst = (ln >> 4) == ((ln >> 2) & 3) ? G * 1024 + (rw * 32 + (ln & 15)) * 8 : 2048 + (ln & 15) * 8;
#pragma unroll
      for (int u = 0; u < 2; ++u) {
        const float q = (ln & 2) ? ((ln & 1) ? ssq[u][3] : ssq[u][2]) : ((ln & 1) ? ssq[u][1] : ssq[u][0]);
        *(LAS f32x2*)(smem + AZ_ST + dst + u * 128) = (f32x2){ssum[u][0], q};
      }
    }
    if constexpr (E == ZS::XB1) {
      const int t8 = rw * 64 + lane_id();
      xt[0] = *(LAS float*)(smem + AZ_ST + t8 * 4); xt[1] = *(LAS float*)(smem + AZ_ST + 1024 + t8 * 4);
    }
    if constexpr (E == ZS::PUB) {
      const int t8 = rw * 64 + lane_id();
      ST8(ws, t8 * 8, G ? 0x7fffff00u : WS_STAT + (p * 12 + j) * 2048, ((u32x2){__float_as_uint(xt[0] + xt[1]), X.tag}));
    }
    if constexpr (E >= ZS::GAT && E < ZS::GAT + 3) { asm volatile("" ::: "memory"); gather(E - ZS::GAT); asm volatile("" ::: "memory"); }
    if constexpr (E == ZS::XB2 - 3) {
      wait_v<ZS::after_gather()>();
#pragma unroll 1
      for (unsigned spins = 0; spins < SPIN_MAX; ++spins) {
        PIN(gv[0]); PIN(gv[1]); PIN(gv[2]);
        bool ok = true;
#pragma unroll
        for (int i = 0; i < 3; ++i) ok &= gv[i].y == X.tag && gv[i].w == X.tag;
        if (__builtin_amdgcn_ballot_w64(!ok) == 0) break;
        __builtin_amdgcn_s_sleep(1);
        asm volatile("" ::: "memory");
#pragma unroll
        for (int i = 0; i < 3; ++i) gather(i);
        wait_v<0>();
      }
      *(LAS f32x2*)(smem + AZ_ST2 + (wid * 64 + lane_id()) * 8) =
          (f32x2){__uint_as_float(gv[0].x) + __uint_as_float(gv[1].x) + __uint_as_float(gv[2].x),
                  __uint_as_float(gv[0].z) + __uint_as_float(gv[1].z) + __uint_as_float(gv[2].z)};
    }
    if constexpr (E == ZS::XB2) {
      LAS float* a = (LAS float*)(smem + AZ_ST2 + rw * 256 + lane_id() * 4);
#pragma unroll
      for (int i = 0; i < 4; ++i) xt[i] = a[i * 256];
    }
    if constexpr (E == ZS::XB2 + 3) {
      const int ln = lane_id();
      const float v = ((xt[0] + xt[1]) + (xt[2] + xt[3])) * (1.0f / 2304.0f), o = dpp_get<0xB1>(v);
      const float m = (ln & 1) ? o : v, q = (ln & 1) ? v : o;
      const float sc = __builtin_amdgcn_rsqf(__builtin_fmaxf(q - m * m, 0.f) + 1e-5f * GC * GC);
      *(LAS float*)(smem + AZ_TAB + wid * 256 + ln * 4) = (ln & 1) ? -m * sc : sc;
    }
    sfor<0, 6>([&](auto CH_) {
      constexpr int ch = decltype(CH_)::value, cc = ch / 3, rr = ch % 3, q0 = ZS::nrm(ch);
      if constexpr (E == q0 || E == q0 + 1) {
        constexpr int hf = E - q0;
#pragma unroll
        for (int i = 2 * hf; i < 2 * hf + 2; ++i) {
          unsigned t = nv[ch & 1][i];
          t = cvt_pk(__builtin_fmaf(bf_lo(t), tab[cc][i][0], tab[cc][i][1]), __builtin_fmaf(bf_hi(t), tab[cc][i][2], tab[cc][i][3]));
          PIN(t); nv[ch & 1][i] = t;
        }
        if constexpr (hf == 1) {
          const int tg = (3 * j + rr) * 4 + 2 * G, sb = p * 4 + rw;
          AST16<cc * 256>(rsws, nvo, tg < 128 ? WS_Z2N + (((tg >> 3) * 64 + sb) * 8 + (tg & 7)) * 1024 : WS_Z2O + (sb * 16 + tg - 128) * 1024, nv[ch & 1]);
        }
      }
      if constexpr (rr == 0 && E == q0 - 2) {
        const int hb = (lane_id() >> 5) * 128;
#pragma unroll
        for (int i = 0; i < 4; ++i) tab[cc][i] = *(LAS f32x4*)(smem + AZ_TAB + wid * 256 + hb + cc * 64 + i * 16);
      }
      if constexpr (E == q0 - 2) nv[ch & 1] = *(LAS u32x4*)(park + rr * 16384 + cc * 512);
    });
    if constexpr (E == ZS::POLL) {
      asm volatile("" ::: "memory");
      ald4(fpoll, rsws, f1_off(X), 0);
    }
    if constexpr (E == ZS::FW) first_w(P, X, smem);
    if constexpr (E == ZS::WARM) warm_b(P, X, smem);
    if constexpr (ZS::wsp(E)) {
      constexpr int ev = (E - 45) / 12;
      TOUCH(MKRS(P.wsp, (wid >> 1) == ev ? 2048u * 4096u : 0u), lane_id() * 128, ((p * 12 + j) * 6 + wid) * 8192, smem + L_SCR);
    }
    SB0();
  });
  FINE(18); FINE_OUT(19);
  STAMP(0, 9);
  wait_v<4>(); PIN(fpoll);
  const bool rdy = all_eq(fpoll, X.tag);
  if (rdy) first_z(P, X, smem);
  STAMP(0, 10);
  u32x4 w[2];
#pragma unroll
  for (int cc = 0; cc < 2; ++cc) {
#pragma unroll
    for (int i = 0; i < 4; ++i) {
      const int reg = ((2 * i) & 3) + (i >> 1) * 8 + cc * 4;
      w[cc][i] = cvt_pk(gelu_s(acc[1][reg]), gelu_s(acc[1][reg + 1]));
    }
    PIN(w[cc]);
    if (cc == 0 && !rdy) { spin_flag(P.ws, f1_off(X), X.tag); STAMP(0, 13); first_z(P, X, smem); }
  }
  put(IC<4>{}, IC<1>{});
  AST16<0>(rsws, zv, zo + 4096, w[0]); AST16<512>(rsws, zv, zo + 4096, w[1]);
  if (wid < 4) {
    lane = lane_id();
    const int kb0 = ((p & 1) * 12 + j) * 4 + rw, kb = kb0 < 90 ? kb0 : 0;
    const rsrc_t rs = kb < 72 ? MKRS(P.wout, 2304 * 1152) : MKRS(P.wo, 576 * 1152);
    const int r0 = kb < 72 ? 32 * kb : kb < 88 ? 36 * (kb - 72) : 288 * (kb - 88) + 32;
    const int mq = kb < 64 ? 4 : kb < 72 ? 8 : kb < 88 ? 4 : 72, mu = kb < 64 ? 16 : kb < 72 ? 4 : kb < 88 ? 16 : 36;
#pragma unroll
    for (int i = 0; i < 5; ++i) {
      const int pc = i < 4 ? i * 64 + lane : 256 + (lane & 31), k = (pc * 57) >> 9, ch = pc - k * 9;
      BLDS(rs, ((k >> 3) * mq + ((k >> 2) & 1) * mu + (k & 3)) * 1152 + ch * 16, r0 * 1152 + (p >> 1) * 144, smem + B_KV + rw * 5120 + i * 1024);
    }
    wait_v<8>();
  } else {
    wait_v<3>();
  }
}

template <int G> struct QS {
  static constexpr int nt(int R) { return R == (G ? 0 : 2) ? 3 : 2; }
  static constexpr int t0(int R) { return G ? (R == 1 ? 4 : 5) - nt(R) : 0; }
  static constexpr int base(int R) { return R == 0 ? 0 : base(R - 1) + 36 * nt(R - 1); }
  static constexpr int NE = base(3);
  static constexpr int round(int E) { return E < base(1) ? 0 : E < base(2) ? 1 : 2; }
  static constexpr int ks(int E) { return (E - base(round(E))) / nt(round(E)); }
  static constexpr int tile(int E) { return (E - base(round(E))) % nt(round(E)); }
  static constexpr int stage(int E) { return 9 * round(E) + ks(E) / 4; }
  static constexpr int pos(int E) { return (E - base(round(E))) % (4 * nt(round(E))); }
  static constexpr int ent(int el) { return base(1) + 4 + (G ? el + el / 3 : 2 * el); }
  static constexpr int KV0 = base(2) + 8 * nt(2) - 4;
  static constexpr int NU = G ? 7 : 4, UL = G ? 4 : 6;
  static constexpr int ND = G ? 2 : 3, PRE = 3;
  static constexpr int dma(int E) {
    return (E + 4 < NE && pos(E + 4) >= 1 && pos(E + 4) <= 2 * ND && (pos(E + 4) & 1) && stage(E + 4) + 2 < 27 && stage(E + 4) + 2 >= PRE) ? 1 + pos(E + 4) / 2 : 0;
  }
  static constexpr int WARM = base(2) - 10;
  static constexpr int other(int E) { return ((E >= KV0 && E < KV0 + NU * UL && (E - KV0) % UL == UL - 1) ? 1 : 0) + (E == WARM); }
  static constexpr int younger(int Eb) {
    const int SN = stage(Eb + 4);
    int n = 0;
    for (int E = Eb - 1; E >= 0; --E) {
      n += other(E);
      if (dma(E)) { if (stage(E + 4) + 2 == SN) return n; n += 1; }
    }
    return n + (PRE - 1 - SN) * ND + ((TMODE & 1) ? 2 : 0);
  }
};
template <int G> DI void q_voff(unsigned (&voff)[3], int rw, int lane) {
#pragma unroll
  for (int i = 0; i < QS<G>::ND; ++i) {
    const int pc = (G ? 768 + rw * 128 : rw * 192) + i * 64 + lane, sr = (pc * 3277) >> 16, ch = pc - sr * 20;
    voff[i] = (unsigned)(sr * 3456 + ch * 16 + (ch >= 18 ? 1120 : 0));
  }
}
template <int G> DI void q_first(const Params& P, const Ctx& X, LAS char* smem) {
  const rsrc_t rsw = MKRS(P.wqkv, 576 * 3456);
  const int rw = X.wid & 3;
  unsigned voff[3];
  q_voff<G>(voff, rw, lane_id());
#pragma unroll
  for (int S = 0; S < QS<G>::PRE; ++S)
#pragma unroll
    for (int i = 0; i < QS<G>::ND; ++i)
      BLDS(rsw, voff[i], X.quad * 288 + S * 64 * 3456 + 1152, smem + S * AQ_SLOT + (G ? 12288 + rw * 2048 : rw * 3072) + i * 1024);
}
template <int G>
DI void a_qkvblock(const Params& P, const Ctx& X, LAS char* smem, bf16x8 (&xf)[36], LAS char* (&xb)[4]) {
  typedef QS<G> Q;
  const int wid = X.wid, p = X.pa, quad = X.quad;
  int lane = lane_id();
  const int rw = wid & 3, h = lane >> 5;
  const rsrc_t rsw = MKRS(P.wqkv, 576 * 3456), rsws = MKRS(P.ws, WS_END);
  const brs_t ws = __builtin_amdgcn_make_buffer_rsrc(P.ws, 0, WS_END, 0x00020000);
  unsigned voff[3];
  q_voff<G>(voff, rw, lane);
  auto dma = [&](auto S_, auto I_) {
    constexpr int S = decltype(S_)::value, I = decltype(I_)::value;
    if constexpr (S == 9) {
      const unsigned c = voff[I] % 3456u;
      voff[I] -= c >= 1408u ? 1152u : 0u; PIN(voff[I]);
    }
    BLDS(rsw, voff[I], quad * 288 + (S % 9) * 64 * 3456 + ((S / 9 + 1) % 3) * 1152,
         smem + (S & 3) * AQ_SLOT + (G ? 12288 + rw * 2048 : rw * 3072) + I * 1024);
  };
  LAS char* rd = smem + (8 * h + ((lane >> 2) & 3)) * 320 + ((lane >> 4) & 1) * 32 + (lane & 3) * 8;
  const bool isv = (lane >> 4) & 1;
  LAS char* wimg = smem + AQ_PK + ((lane >> 4) & 1) * 4096 + rw * 1024 + h * 512 + (lane & 15) * 16;
  LAS char* rimg = smem;
  f32x16 acc[3];
  f32x4 dd;
  unsigned prev[3][8], fpoll = 0, l16 = 0;
  bf16x8 wf[5], ka[4], va[4];
  float pa[2], pe[2], pt[2], pm[2], pl = 0.f;
  auto rdw = [&](auto E_) {
    constexpr int E = decltype(E_)::value, o = (Q::stage(E) & 3) * AQ_SLOT + (Q::ks(E) & 3) * 5120 + (Q::t0(Q::round(E)) + Q::tile(E)) * 64;
    wf[E % 5] = tr_frag(rd + o, rd + o + 1280);
  };
  auto topk = [&](auto T_) {
    constexpr int t = decltype(T_)::value;
#pragma unroll
    for (int i = 0; i < 8; ++i) { prev[t][i] = cvt_pk(acc[t][2 * i], acc[t][2 * i + 1]); PIN(prev[t][i]); }
  };
  FINE_DECL();
  FINE(0);
  sfor<0, 4>(rdw);
  sfor<0, Q::NE>([&](auto E_) {
    constexpr int E = decltype(E_)::value, R = Q::round(E), ks = Q::ks(E), t = Q::tile(E), EN = E + 4, er = E - Q::base(R);
    if constexpr (EN < Q::NE) {
      if constexpr (Q::pos(EN) == 0) {
        wait_v<Q::younger(E)>();
        if (Q::stage(EN) == 20) asm volatile("s_waitcnt lgkmcnt(2)" ::: "memory");
        if (Q::stage(EN) == 1) WAIT_L0();
        BAR(); FINE(Q::stage(EN));
        if constexpr (G == 0 && Q::stage(EN) == 26) {
          const int ln = lane_id();
          ST4(ws, (wid == 0 && ln < 4) ? (quad * 4 + ln) * 128 + p * 4 : 0x7fffff00u, WS_F2K, X.tag);
        }
      }
      rdw(IC<EN>{});
      if constexpr (E < 20) xf[16 + E] = XRD(16 + E);
      if constexpr (Q::dma(E)) dma(IC<Q::stage(EN) + 2>{}, IC<Q::dma(E) - 1>{});
    }
    if constexpr (R >= 1 && er < Q::nt(R - 1)) topk(IC<er>{});
    const f32x16 zz = {};
    if constexpr (R == 2) acc[t] = MFMA32(wf[E % 5], xf[ks], ks ? acc[t] : zz);
    else acc[t] = MFMA32(xf[ks], wf[E % 5], ks ? acc[t] : zz);
    PIN(acc[t]);
    if constexpr (R == 1) {
      sfor<0, 16 * Q::nt(0)>([&](auto L_) {
        constexpr int el = decltype(L_)::value, tl = el / 16, w = (el & 15) / 2, b = el & 1;
        if constexpr (E == Q::ent(el) + 2) {
          const float r = pa[b] + pe[b];
          constexpr bool mixed = Q::t0(0) + tl == 4;
          if constexpr (b == 0) { pl = r; PIN(pl); }
          else { const unsigned o = cvt_pk(pl, r); prev[tl][w] = (mixed && isv) ? prev[tl][w] : o; PIN(prev[tl][w]); }
          if constexpr ((el & 7) == 7)
            *(LAS u32x4*)(wimg + (mixed && isv ? AQ_V - AQ_PK - 4096 : 0) + (Q::t0(0) + tl) * 8192 + (w >> 2) * 256) =
                (u32x4){prev[tl][w - 3], prev[tl][w - 2], prev[tl][w - 1], prev[tl][w]};
        }
        if constexpr (E == Q::ent(el) + 1) {
          pe[b] = __builtin_amdgcn_fmed3f(__builtin_amdgcn_exp2f(pm[b]), 0.f, 1.f); pa[b] = __builtin_fmaxf(pt[b], 1e-6f); PIN(pe[b]); PIN(pa[b]);
        }
        if constexpr (E == Q::ent(el)) { pt[b] = b ? bf_hi(prev[tl][w]) : bf_lo(prev[tl][w]); pm[b] = pt[b] * 1.4426950409f; PIN(pt[b]); PIN(pm[b]); }
      });
    }
    if constexpr (R == 2 && er >= 3 && er < 3 + 2 * Q::nt(1)) {
      constexpr int tl = (er - 3) / 2, cc = (er - 3) & 1;
      *(LAS u32x4*)(wimg + (AQ_V - AQ_PK) + (Q::t0(1) + tl) * 8192 + cc * 256) =
          (u32x4){prev[tl][4 * cc], prev[tl][4 * cc + 1], prev[tl][4 * cc + 2], prev[tl][4 * cc + 3]};
    }
    if constexpr (E >= Q::KV0 && E < Q::KV0 + Q::NU * Q::UL) {
      constexpr int un = (E - Q::KV0) / Q::UL, ph = (E - Q::KV0) % Q::UL, it = un / 4, dj = un % 4;
      const int i = it ? 8 : wid;
      if constexpr (ph == 0) {
        if constexpr (un == 0) { l16 = lane_id() * 16; rimg = smem + AQ_PK + l16; }
        if constexpr (dj == 0) {
#pragma unroll
          for (int k = 0; k < 4; ++k) ka[k] = *(LAS bf16x8*)(rimg + (i * 4 + k) * 1024);
        } else {
          const int jv = i + dj - 2 < 0 ? 0 : i + dj - 2;
#pragma unroll
          for (int k = 0; k < 4; ++k) va[k] = *(LAS bf16x8*)(rimg + (AQ_V - AQ_PK) + (jv * 4 + k) * 1024);
        }
      }
      if constexpr (ph == Q::UL - 2) {
        const bf16x8 ones = {0x3f80, 0x3f80, 0x3f80, 0x3f80, 0x3f80, 0x3f80, 0x3f80, 0x3f80};
        dd = (f32x4){0, 0, 0, 0};
#pragma unroll
        for (int k = 0; k < 4; ++k) dd = MFMA(ka[k], dj ? va[k] : ones, dd);
        PIN(dd);
      }
      if constexpr (ph == Q::UL - 1)
        ST16(ws, l16 + dj * 1024, (it && wid != 4) ? 0x7ff00000u : WS_KVP + (quad * 16 + p) * 36864 + i * 4096, __builtin_bit_cast(u32x4, dd));
    }
    if constexpr (E == Q::WARM) warm_b(P, X, smem);
    if constexpr (EN < Q::NE && Q::stage(EN) == 26 && Q::pos(EN) == 1) { ald4(fpoll, rsws, f1_off(X), 0); first_w(P, X, smem); }
    SB0();
  });
  FINE(27); FINE_OUT(28);
  STAMP(0, 9);
  wait_v<4>(); PIN(fpoll);
  if (!all_eq(fpoll, X.tag)) spin_flag(P.ws, f1_off(X), X.tag);
  first_z(P, X, smem);
  lane = lane_id();
  const unsigned qo = ((lane >> 4) & 1) * 18432 + (lane >> 5) * 128 + (lane & 15) * 8;
  int nst = 0;
#pragma unroll
  for (int t = 0; t < Q::nt(2); ++t)
#pragma unroll
    for (int a = 0; a < 4; ++a)
      if ((Q::t0(2) + t) * 8 + 2 * a < 36)
        ST8(ws, qo + ((Q::t0(2) + t) * 8 + 2 * a) * 128, WS_PQ + (p * 8 + rw * 2) * 18432 + quad * 4608,
            ((u32x2){cvt_pk(acc[t][4 * a], acc[t][4 * a + 1]), cvt_pk(acc[t][4 * a + 2], acc[t][4 * a + 3])}));
  wait_v<(G ? 6 : 12)>();
}

template <int ROLE>
DI void bc_loader(const Params& P, const Ctx& X, LAS char* smem) {
  constexpr int CNT = ROLE ? 16 : 18;
  __builtin_amdgcn_s_setprio(3);
  Loader<ROLE> L; L.init(P, X, smem);
  const int lane = lane_id(), p = X.p, c = X.c;
  BAR(); L.issue_b(2);
  BAR(); L.issue_b(3);
#pragma unroll 1
  for (int n = 2; n < 30; ++n) { wait_v<CNT>(); BAR(); L.issue_b(n + 2); }
  wait_v<CNT>(); BAR(); wait_v<0>(); BAR();
  if (ROLE == 0) spin_flag(P.ws, WS_F2Z + ((c & 7) * 2 + (lane >> 5)) * 128 + (16 + ((lane & 31) < 12 ? lane & 31 : 0)) * 4, X.tag);
  else spin_flag(P.ws, WS_F3O + p * 128 + (lane & 15) * 4, X.tag);
  L.template issue_c<2>(32); L.template issue_c<4>(33); L.template issue_c<4>(34);
  int n = 32;
  if (ROLE == 1) {
    wait_v<2 * CNT>(); BAR(); L.template issue_c<4>(35);
    wait_v<2 * CNT>(); BAR(); L.template issue_c<4>(36);
    wait_v<2 * CNT>(); BAR(); wait_v<CNT>(); BAR(); wait_v<0>(); BAR();
    STAMP(2, 5);
    spin_flag(P.ws, WS_F4 + (2 * p + (c >> 3)) * 128 + (lane & 31) * 4, X.tag);
    STAMP(2, 6);
    L.template issue_c<4>(37); L.template issue_c<4>(38); L.template issue_c<4>(39);
    n = 37;
  }
#pragma unroll 1
  for (; n < 52; ++n) { wait_v<2 * CNT>(); BAR(); L.template issue_c<4>(n + 3); }
  wait_v<2 * CNT>(); BAR(); wait_v<CNT>(); BAR(); wait_v<0>(); BAR();
}

DI void bc_side(const Params& P, const Ctx& X, LAS char* smem) {
  __builtin_amdgcn_s_setprio(2);
  const brs_t ws = __builtin_amdgcn_make_buffer_rsrc(P.ws, 0, WS_END, 0x00020000);
  const int lane = lane_id();
  const int sw = X.wid - 6, lt = sw * 64 + lane, p = X.p, c = X.c, kq = lane >> 4, r16 = lane & 15;
  u32x4 part[3]; u32x2 qv[4][2], qx; int dst[6], src[6]; bf16x8 kvf[4][2]; float mine = 0.f;
  const unsigned oob = 0x7fffff00u;
  const unsigned f2k = WS_F2K + c * 128 + r16 * 4, f2z = WS_F2Z + p * 128 + (kq < 3 ? f1_src(c, kq) : 12 + (c >> 2)) * 4;
  unsigned fl = 0;
  auto kvf_ld = [&]() {
#pragma unroll
    for (int i = 0; i < 3; ++i) part[i] = LD16(ws, (lt + 128 * i < 352 ? lt + 128 * i : 351) * 16, WS_KVF + c * 5632);
  };
  auto phi2 = [&](u32x2& v) {
    v = (u32x2){cvt_pk(phi_f(bf_lo(v.x)), phi_f(bf_hi(v.x))), cvt_pk(phi_f(bf_lo(v.y)), phi_f(bf_hi(v.y)))};
    PIN(v);
  };
  sfor<0, 32>([&](auto N_) {
    constexpr int N = decltype(N_)::value;
    if constexpr (N == 4) wait_v<6>();
    if constexpr (N == 26) wait_v<0>();
    if constexpr (N == 9 || N == 15) WAIT_L0();
    BAR();
    if constexpr (N == 22) { if (sw == 1) warm_code(X, smem); }
    if constexpr (N == 1) fl = flag_ld(P.ws, f2k);
    if constexpr (N < 3) {
#pragma unroll
      for (int i = 2 * N; i < 2 * N + 2; ++i) {
        const int l = (lane & 3) + 4 * i, id0 = p * 44 + sw * 22 + (l < 22 ? l : 21), id = id0 < 684 ? id0 : 683;
        const int hh = id >= 342, rem = id - hh * 342, e = (rem * 3641) >> 16, d = rem - e * 18;
        const int kc = 36 * (c & 3) + 18 * hh + d, vc = kc - d + e;
        src[i] = (lane >> 2) * 36864 + (kc >> 4) * 4096 + (e == 18 ? 0 : ((vc >> 4) - (kc >> 4) + 2) * 1024 + (vc & 15) * 16) + ((kc >> 2) & 3) * 256 + (kc & 3) * 4;
        PIN(src[i]);
      }
    }
    if constexpr (N == 4 || N == 5) {
      const int r = sw * 2 + N - 4, kb = ((X.pa & 1) * 12 + X.quad * 3 + X.jj) * 4 + r;
      {
        LAS char* a = smem + B_KV + r * 5120 + (8 * kq + ((lane >> 2) & 3)) * 144;
        const unsigned so = (X.jj < 3 && kb < 90) ? WS_WX + ((X.pa >> 1) * 90 + kb) * 4608 : 0x7ff00000u;
#pragma unroll
        for (int dt = 0; dt < 4; ++dt) {
          LAS char* b = a + (dt >> 1) * 64 + (lane & 3) * 16 + (dt & 1) * 8;
          ST16(ws, lane * 16 + dt * 1024, so, __builtin_bit_cast(u32x4, tr_frag(b, b + 576)));
        }
        LAS char* b = a + 128 + (lane & 1) * 8;
        ST16(ws, r16 < 8 ? 4096 + kq * 128 + r16 * 16 : 0x7ff00000u, so, __builtin_bit_cast(u32x4, tr_frag(b, b + 576)));
      }
    }
    if constexpr (N == 8) {
#pragma unroll
      for (int i = 0; i < 4; ++i) *(LAS u32x4*)(smem + B_KV + i * 2048 + lt * 16) = (u32x4){0, 0, 0, 0};
    }
    if constexpr (N == 7 || N == 8 || N == 11) {
      constexpr int e0 = N == 11 ? 4 : 2 * (N - 7);
#pragma unroll
      for (int e6 = e0; e6 < e0 + 2; ++e6) {
        const int id = (lt + 128 * (e6 >> 1)) * 2 + (e6 & 1), hh = id >= 342, rem = id - hh * 342, e = (rem * 3641) >> 16, d = rem - e * 18;
        const int k = 18 * hh + d, n = 18 * hh + e;
        const int kof = (k >> 5) * 1024 + ((k >> 3) & 3) * 256 + (k & 7) * 2;
        dst[e6] = id >= 684 ? 8192 + lane * 2 : (e < 18 ? (n >> 4) * 2048 + (n & 15) * 16 : 6144 + hh * 16) + kof;
        PIN(dst[e6]);
      }
    }
    if constexpr (N == 4) {
      ST4(ws, lt == 0 ? 0u : oob, WS_F2Z + X.pa * 128 + (X.jj < 3 ? X.quad * 3 + X.jj : 12 + X.quad) * 4, X.tag);
    }
    if constexpr (N == 7) fl = flag_ld(P.ws, f2z);
    if constexpr (N == 3) {
      if (!all_eq(fl, X.tag)) spin_flag(P.ws, f2k, X.tag);
#pragma unroll
      for (int i = 0; i < 6; ++i) src[i] = __builtin_amdgcn_raw_buffer_load_b32(ws, src[i], WS_KVP + (c >> 2) * 16 * 36864, 16);
    }
    if constexpr (N == 6) {
#pragma unroll
      for (int i = 0; i < 6; ++i) {
        float v = __int_as_float(src[i]);
        v += dpp_get<0x124>(v); v += dpp_get<0x128>(v);
        auto a = __builtin_amdgcn_permlane16_swap(__float_as_uint(v), __float_as_uint(v), false, false);
        v = __uint_as_float(a[0]) + __uint_as_float(a[1]);
        auto b = __builtin_amdgcn_permlane32_swap(__float_as_uint(v), __float_as_uint(v), false, false);
        v = __uint_as_float(b[0]) + __uint_as_float(b[1]);
        mine = (lane >> 2) == i ? v : mine;
      }
      ST8(ws, lane < 22 ? (p * 44 + sw * 22 + lane) * 8 : oob, WS_KVF + c * 5632, ((u32x2){__float_as_uint(mine), X.tag}));
    }
    if constexpr (N == 9) {
      if (!all_eq(fl, X.tag)) spin_flag(P.ws, f2z, X.tag);
      qx = LD8(ws, kq * 18432 + 1024 + r16 * 8, WS_PQ + (p * 8 + sw * 4) * 18432 + c * 1152);
    }
    if constexpr (N == 9 || N == 10) {
#pragma unroll
      for (int tt = 2 * (N - 9); tt < 2 * (N - 8); ++tt) {
        const unsigned so = WS_PQ + (p * 8 + sw * 4 + tt) * 18432 + c * 1152;
        qv[tt][0] = LD8(ws, kq * 256 + r16 * 8, so); qv[tt][1] = LD8(ws, kq * 256 + 128 + r16 * 8, so);
      }
    }
    if constexpr (N == 11) kvf_ld();
    if constexpr (N == 12) { phi2(qx); phi2(qv[0][0]); }
    if constexpr (N == 13) { phi2(qv[0][1]); phi2(qv[1][0]); }
    if constexpr (N == 15) phi2(qv[1][1]);
    if constexpr (N == 16 || N == 17) { phi2(qv[N - 14][0]); phi2(qv[N - 14][1]); }
    if constexpr (N == 14) {
#pragma unroll 1
      for (unsigned spins = 0; spins < SPIN_MAX; ++spins) {
        bool ok = true;
#pragma unroll
        for (int i = 0; i < 3; ++i) ok &= part[i].y == X.tag && part[i].w == X.tag;
        if (__builtin_amdgcn_ballot_w64(!ok) == 0) break;
        __builtin_amdgcn_s_sleep(1);
        asm volatile("" ::: "memory");
        kvf_ld();
      }
#pragma unroll
      for (int e6 = 0; e6 < 6; ++e6) {
        const unsigned u = part[e6 >> 1][(e6 & 1) * 2];
        *(LAS u16*)(smem + B_KV + dst[e6]) = (u16)(cvt_pk(__uint_as_float(u), 0.f) & 0xffffu);
      }
    }
    if constexpr (N == 15) {
#pragma unroll
      for (int nt = 0; nt < 4; ++nt) {
        LAS char* kb = smem + B_KV + nt * 2048 + (nt < 3 ? r16 : r16 & 1) * 16;
        kvf[nt][0] = *(LAS bf16x8*)(kb + kq * 256); kvf[nt][1] = *(LAS bf16x8*)(kb + 1024);
      }
    }
    if constexpr (N >= 18 && N <= 21) {
      constexpr int tt = N - 18;
      const bf16x8 q0 = __builtin_bit_cast(bf16x8, ((u32x4){qv[tt][0].x, qv[tt][0].y, qv[tt][1].x, qv[tt][1].y}));
      const bf16x8 q1 = __builtin_bit_cast(bf16x8, ((u32x4){kq == tt ? qx.x : 0u, kq == tt ? qx.y : 0u, 0u, 0u}));
      const f32x4 z = {0, 0, 0, 0};
      const f32x4 den = MFMA(kvf[3][1], q1, MFMA(kvf[3][0], q0, z));
      f32x4 num[3];
#pragma unroll
      for (int nt = 0; nt < 3; ++nt) num[nt] = MFMA(kvf[nt][1], q1, MFMA(kvf[nt][0], q0, z));
      const float r0 = __builtin_amdgcn_rcpf(den[0] + 1e-6f), r1 = __builtin_amdgcn_rcpf(den[1] + 1e-6f), rm = kq == 0 ? r0 : r1;
      const unsigned so = WS_HX + (p * 8 + sw * 4 + tt) * (90 * 1024);
      ST16(ws, lane * 16, so + (72 + c) * 1024, ((u32x4){cvt_pk(num[0][0] * r0, num[0][1] * r0), cvt_pk(num[0][2] * r0, num[0][3] * r0),
                                                         cvt_pk(num[1][0] * rm, num[1][1] * rm), cvt_pk(num[1][2] * r1, num[1][3] * r1)}));
      ST8(ws, kq == 0 ? r16 * 16 : oob, so + (88 + (c >> 3)) * 1024 + ((c & 7) >> 1) * 256 + (c & 1) * 8,
          ((u32x2){cvt_pk(num[2][0] * r1, num[2][1] * r1), cvt_pk(num[2][2] * r1, num[2][3] * r1)}));
    }
    if constexpr (N == 26) {
      if (lt == 0) flag_st(P.ws, WS_F3O + p * 128 + c * 4, X.tag);
      if (lt == 64 && X.jj < 3) flag_st(P.ws, WS_F2Z + X.pa * 128 + (16 + X.quad * 3 + X.jj) * 4, X.tag);
    }
    SB0();
  });
  if (X.bid == 0 && sw == 0) {
#pragma unroll 1
    for (int n = 32; n < 42; ++n) BAR();
    unsigned f4[4];
#pragma unroll
    for (int i = 0; i < 4; ++i) f4[i] = flag_ld(P.ws, WS_F3O + (i * 4 + kq) * 128 + r16 * 4);
    BAR(); BAR(); BAR();
    if (!(all_eq(f4[0], X.tag) && all_eq(f4[1], X.tag) && all_eq(f4[2], X.tag) && all_eq(f4[3], X.tag))) {
#pragma unroll 1
      for (int i = 0; i < 4; ++i) spin_flag(P.ws, WS_F3O + (i * 4 + kq) * 128 + r16 * 4, X.tag);
    }
    if (lane == 0) flag_st(P.ws, WS_EPOCH, X.tag);
  }
}

template <int DH>
DI void c_compute(const Params& P, const Ctx& X, LAS char* smem) {
  constexpr int ND = DH ? 2 : 3;
  int lane = lane_id();
  const int r16 = lane & 15, kq = lane >> 4, th = X.wid & 1;
  const int rg = 2 * X.p + (X.c >> 3), cg = X.c & 7;
  f32x4 acc[ND][2];
#pragma unroll
  for (int a = 0; a < ND; ++a) { acc[a][0] = (f32x4){0, 0, 0, 0}; acc[a][1] = (f32x4){0, 0, 0, 0}; }
  const int woff = DH * 2048 + lane * 16, woff4 = 4096 + kq * 128 + (r16 & 7) * 16;
  const int hoff = 18432 + th * 8192 + lane * 16;
  bf16x8 wa[2][ND], ha[2][2], wb[2][ND], hb[2][2];
#define C_RD(W, H, slot, kp) do { LAS char* b_ = smem + SLOT(slot); \
    _Pragma("unroll") for (int k = 0; k < 2; ++k) { \
      H[k][0] = *(LAS bf16x8*)(b_ + hoff + ((kp) * 2 + k) * 1024); H[k][1] = *(LAS bf16x8*)(b_ + hoff + 4096 + ((kp) * 2 + k) * 1024); \
      W[k][0] = *(LAS bf16x8*)(b_ + woff + ((kp) * 2 + k) * 4608); W[k][1] = *(LAS bf16x8*)(b_ + woff + 1024 + ((kp) * 2 + k) * 4608); \
      if (ND == 3) W[k][ND - 1] = *(LAS bf16x8*)(b_ + woff4 + ((kp) * 2 + k) * 4608); } } while (0)
#define C_MM(W, H) do { _Pragma("unroll") for (int k = 0; k < 2; ++k) _Pragma("unroll") for (int i = 0; i < ND; ++i) { \
    acc[i][0] = MFMA(W[k][i], H[k][0], acc[i][0]); acc[i][1] = MFMA(W[k][i], H[k][1], acc[i][1]); } } while (0)
#define C_ILV() do { _Pragma("unroll") for (int i_ = 0; i_ < ND + 2; ++i_) { SGB(0x100, 2); SGB(0x8, 1); } SGB(0x8, 3 * ND - 2); } while (0)
#define C_STAGE(n) do { WAIT_L0(); BAR(); const int slot = (n) & 3; \
    C_RD(wa, ha, slot, 0); C_MM(wb, hb); C_ILV(); SB0(); \
    C_RD(wb, hb, slot, 1); C_MM(wa, ha); C_ILV(); SB0(); } while (0)
  BAR();
  C_RD(wb, hb, 0, 0);
  C_STAGE(33);
  wait_v<0>();
  if (lane == 0) flag_st(P.ws, WS_F4 + (X.p * 2 + (X.wid >> 1)) * 128 + (X.c * 2 + (X.wid & 1)) * 4, X.tag);
  STAMP(2, 1);
  u32x4 xr[2]; u32x2 xo[2]; size_t orow[2];
#pragma unroll
  for (int tt = 0; tt < 2; ++tt) {
    orow[tt] = (size_t)(rg * 64 + slot_row(th * 32 + tt * 16 + r16)) * 1152 + cg * 144;
    xr[tt] = *(const u32x4*)((const char*)P.x + orow[tt] + DH * 64 + kq * 16);
    if (DH == 0) xo[tt] = *(const u32x2*)((const char*)P.x + orow[tt] + 128 + (kq & 1) * 8);
  }
  C_STAGE(34);
#pragma unroll 1
  for (int n = 35; n < 55; n += 4) { C_STAGE(3); C_STAGE(0); C_STAGE(1); C_STAGE(2); }
  C_MM(wb, hb);
  SB0();
  STAMP(2, 2);
  const brs_t ro = __builtin_amdgcn_make_buffer_rsrc(P.out, 0, 2048 * 1152, 0x00020000);
#pragma unroll
  for (int tt = 0; tt < 2; ++tt) {
    const u32x4 x = xr[tt]; const f32x4 a0 = acc[0][tt], a1 = acc[1][tt];
    u32x4 v;
    v.x = cvt_pk(bf_lo(x.x) + a0[0], bf_hi(x.x) + a0[1]); v.y = cvt_pk(bf_lo(x.y) + a0[2], bf_hi(x.y) + a0[3]);
    v.z = cvt_pk(bf_lo(x.z) + a1[0], bf_hi(x.z) + a1[1]); v.w = cvt_pk(bf_lo(x.w) + a1[2], bf_hi(x.w) + a1[3]);
    __builtin_amdgcn_raw_buffer_store_b128(v, ro, (int)(orow[tt] + DH * 64 + kq * 16), 0, OUTAUX);
    if (DH == 0) {
      const f32x4 a2 = acc[ND - 1][tt];
      u32x2 w; w.x = cvt_pk(bf_lo(xo[tt].x) + a2[0], bf_hi(xo[tt].x) + a2[1]); w.y = cvt_pk(bf_lo(xo[tt].y) + a2[2], bf_hi(xo[tt].y) + a2[3]);
      __builtin_amdgcn_raw_buffer_store_b64(w, ro, kq < 2 ? (int)(orow[tt] + 128 + kq * 8) : 0x7fffff00, 0, OUTAUX);
    }
  }
  STAMP(2, 3);
}

DI void b_compute(const Params& P, const Ctx& X, LAS char* smem) {
  int lane = lane_id();
  const int wid = X.wid, p = X.p, c = X.c, r16 = lane & 15, kq = lane >> 4;
  const brs_t ws = __builtin_amdgcn_make_buffer_rsrc(P.ws, 0, WS_END, 0x00020000);
  f32x4 acc[9][2];
#pragma unroll
  for (int a = 0; a < 9; ++a) { acc[a][0] = (f32x4){0, 0, 0, 0}; acc[a][1] = (f32x4){0, 0, 0, 0}; }
  const int zoff = lane * 16;
  const int woff0 = 18432 + (wid * 32 + r16) * 128 + ((kq ^ (r16 >> 1)) * 16);
  const int woff1 = 18432 + (wid * 32 + r16) * 128 + (((kq + 4) ^ (r16 >> 1)) * 16);
  bf16x8 za[9], wa[2], zb[9], wb[2];
#define B_RD(Z, W, slot, kb) do { LAS char* b_ = smem + SLOT(slot); \
    W[0] = *(LAS bf16x8*)(b_ + ((kb) ? woff1 : woff0)); W[1] = *(LAS bf16x8*)(b_ + ((kb) ? woff1 : woff0) + 2048); \
    _Pragma("unroll") for (int i = 0; i < 9; ++i) Z[i] = *(LAS bf16x8*)(b_ + zoff + (i < 8 ? (kb) * 8192 + i * 1024 : 16384 + (kb) * 1024)); } while (0)
#define B_MM(Z, W) do { _Pragma("unroll") for (int i = 0; i < 9; ++i) { \
    acc[i][i & 1] = MFMA(Z[i], W[i & 1], acc[i][i & 1]); acc[i][(i & 1) ^ 1] = MFMA(Z[i], W[(i & 1) ^ 1], acc[i][(i & 1) ^ 1]); } } while (0)
#define B_ILV() do { _Pragma("unroll") for (int i_ = 0; i_ < 11; ++i_) { SGB(0x8, 1); SGB(0x100, 1); } SGB(0x8, 7); } while (0)
#define B_STAGE(n) do { WAIT_L0(); BAR(); const int slot = (n) & 3; \
    B_RD(za, wa, slot, 0); B_MM(zb, wb); B_ILV(); SB0(); \
    B_RD(zb, wb, slot, 1); B_MM(za, wa); B_ILV(); SB0(); } while (0)
  BAR();
  STAMP(1, 1);
  B_RD(za, wa, 0, 0); SB0();
  B_RD(zb, wb, 0, 1); B_MM(za, wa); B_ILV(); SB0();
  B_STAGE(1);
#pragma unroll 1
  for (int n = 2; n < 18; n += 4) {
    B_STAGE(2); B_STAGE(3);
    if (n == 2) wait_v<0>();
    B_STAGE(0); B_STAGE(1);
  }
  u32x4 z1[2][4]; u32x2 z1o[2]; u16 bsv[2];
  sfor<18, 32>([&](auto N_) {
    constexpr int N = decltype(N_)::value, tt = (N - 18) / 6, m = (N - 18) % 6;
    if constexpr (N < 30) {
      const unsigned so = WS_Z1 + (p * 8 + wid * 2 + tt) * (72 * 1024);
      if constexpr (m < 4) z1[tt][m] = LD16(ws, zoff + m * 1024, so + c * 4096);
      else if constexpr (m == 4) z1o[tt] = LD8(ws, zoff, so + (64 + (c >> 1)) * 1024 + (c & 1) * 8);
      else bsv[tt] = P.bsp[p * 128 + slot_row(wid * 32 + tt * 16 + r16)];
      SB0();
    }
    B_STAGE(N);
  });
  B_MM(zb, wb);
  SB0();
  STAMP(1, 2);
#pragma unroll
  for (int tt = 0; tt < 2; ++tt) {
    const float b = bf_lo(bsv[tt]) * (1.0f / GC);
    const unsigned so = WS_HX + (p * 8 + wid * 2 + tt) * (90 * 1024);
#pragma unroll
    for (int m = 0; m < 4; ++m) {
      const u32x4 z = z1[tt][m]; const f32x4 g0 = acc[2 * m][tt] * (1.0f / GC) + b, g1 = acc[2 * m + 1][tt] * (1.0f / GC) + b;
      u32x4 v;
      v.x = cvt_pk(bf_lo(z.x) * g0[0], bf_hi(z.x) * g0[1]); v.y = cvt_pk(bf_lo(z.y) * g0[2], bf_hi(z.y) * g0[3]);
      v.z = cvt_pk(bf_lo(z.z) * g1[0], bf_hi(z.z) * g1[1]); v.w = cvt_pk(bf_lo(z.w) * g1[2], bf_hi(z.w) * g1[3]);
      ST16(ws, lane * 16 + m * 1024, so + c * 4096, v);
    }
    const u32x2 z = z1o[tt]; const f32x4 g0 = acc[8][tt] * (1.0f / GC) + b;
    u32x2 v; v.x = cvt_pk(bf_lo(z.x) * g0[0], bf_hi(z.x) * g0[1]); v.y = cvt_pk(bf_lo(z.y) * g0[2], bf_hi(z.y) * g0[3]);
    ST8(ws, (kq >> 1) * 256 + r16 * 16 + (kq & 1) * 8, so + (64 + (c >> 1)) * 1024 + (c & 1) * 512, v);
  }
  STAMP(1, 3);
}

template <int TY>
DI void a_prologue(const Params& P, const Ctx& X, LAS char* smem, bf16x8 (&xf)[36], LAS char* (&xb)[4]) {
  const int wid = X.wid, lane = lane_id(), idx = X.bid >> 3;
  const int sl0 = ((wid + 2 * X.jj) & 7) * 16 + (lane >> 3);
  u32x4 xr[2][9];
#pragma unroll
  for (int ln = 0; ln < 9; ++ln)
#pragma unroll
    for (int hh = 0; hh < 2; ++hh)
      xr[hh][ln] = *(const u32x4*)((const char*)P.x + (size_t)(X.pa * 128 + slot_row(sl0 + hh * 8)) * 1152 + (lane & 7) * 16 + ln * 128);
  SB0();
  unsigned rlo[2], rhi[2]; float sh_[2];
  {
    const bf16x2_t one = {(__bf16)1.0f, (__bf16)1.0f};
    float s[2] = {0.f, 0.f}, q[2] = {0.f, 0.f};
#pragma unroll
    for (int ln = 0; ln < 9; ++ln)
#pragma unroll
      for (int i = 0; i < 4; ++i)
#pragma unroll
        for (int hh = 0; hh < 2; ++hh) {
          const unsigned ua = xr[hh][ln][i];
          const bf16x2_t a = __builtin_bit_cast(bf16x2_t, ua);
          s[hh] = __builtin_amdgcn_fdot2_f32_bf16(a, one, s[hh], false); q[hh] = __builtin_amdgcn_fdot2_f32_bf16(a, a, q[hh], false);
        }
#pragma unroll
    for (int hh = 0; hh < 2; ++hh) {
      const float m = oct_sum(s[hh]) * (1.0f / 576.0f);
      const float var = __builtin_fmaxf(oct_sum(q[hh]) * (1.0f / 576.0f) - m * m, 0.f);
      unsigned r = cvt_pk(__builtin_amdgcn_rsqf(var + 1e-5f) * (TY == 0 ? GC : 1.0f), 0.f);
      sh_[hh] = -m * bf_lo(r);
      unsigned r2 = r << 16;
      PIN(r); PIN(r2); PIN(sh_[hh]);
      rlo[hh] = r; rhi[hh] = r2;
    }
  }
  STAMP(0, 1);
  if (wid == 3) warm_code(X, smem);
  SB0();
#pragma unroll
  for (int i = 0; i < 4; ++i) xb[i] = smem + A_XN + ((wid & 3) * 32 + (lane & 31)) * 128 + (((2 * i + (lane >> 5)) ^ ((lane >> 1) & 7)) * 16);
#pragma unroll
  for (int ln = 0; ln < 9; ++ln) {
    if (ln == 4) {
      WAIT_L0(); BAR();
#pragma unroll
      for (int ks = 0; ks < 16; ++ks) xf[ks] = XRD(ks);
      SB0();
    }
    if (ln == 6) {
      asm volatile("s_waitcnt lgkmcnt(4)" ::: "memory"); BAR();
      if (TY == 0) {
        const rsrc_t rsw = MKRS(P.win, 576 * 9216);
        const unsigned voff = z_voff(wid, lane);
#pragma unroll
        for (int i = 0; i < 9; ++i) BLDS(rsw, voff, (2304 + (X.quad * 3 + X.jj) * 192) * 2 + i * 64 * 9216, smem + i * 8192 + wid * 1024);
      } else q_first<TY == 2>(P, X, smem);
      SB0();
    }
#pragma unroll
    for (int hh = 0; hh < 2; ++hh) {
      const u32x4 v = norm4(xr[hh][ln], rlo[hh], rhi[hh], sh_[hh]);
      const int sl = sl0 + hh * 8;
      *(LAS u32x4*)(smem + A_XN + ln * 16384 + sl * 128 + (((lane & 7) ^ ((sl >> 1) & 7)) * 16)) = v;
      SB0();
    }
  }
  STAMP(0, 2);
  {
    const int r0 = ((idx >> 2) & 7) * 72, t = wid * 64 + lane, col = (t * 911) >> 16, row = t - col * 72;
    if (TY == 0) TOUCH(MKRS(P.win, 576 * 9216), col < 5 ? row * 9216 + (col < 2 ? 4736 + col * 128 : (col - 2) * 128) : 0x7ff00000u,
                       r0 * 9216 + (X.quad * 3 + X.jj) * 384, smem + L_SCR);
    else {
      TOUCH(MKRS(P.wqkv, 576 * 3456), col < 6 ? row * 3456 + 1152 + (col < 3 ? col * 128 : 1152 + (col - 3) * 128) : 0x7ff00000u,
            r0 * 3456 + X.quad * 256, smem + L_SCR);
      TOUCH(MKRS(P.wqkv, 576 * 3456), col < 3 ? row * 3456 + col * 128 : 0x7ff00000u, r0 * 3456 + X.quad * 256, smem + L_SCR);
    }
  }
  wait_v<(TY == 0 ? 7 : TY == 1 ? 8 : 6)>();
  WAIT_L0(); BAR();
  STAMP(0, 3);
}

__global__ NOPK __launch_bounds__(512, 2) void kF(const Params P) {
  extern __shared__ __attribute__((aligned(16))) char smem_[];
  LAS char* smem = (LAS char*)smem_;
  Ctx X;
  X.wid = __builtin_amdgcn_readfirstlane(threadIdx.x >> 6); X.bid = blockIdx.x;
  const int xcd = X.bid & 7, idx = X.bid >> 3, wid = X.wid, lane = threadIdx.x & 63;
  X.pa = (xcd >> 2) * 8 + (idx >> 2); X.quad = xcd & 3; X.jj = idx & 3;
  X.p = (xcd >> 1) * 4 + (idx >> 3); X.c = (xcd & 1) * 8 + (idx & 7);
  STAMP(0, 0);
  X.tag = *(const unsigned*)(P.ws + WS_EPOCH) + 1u;
  if (X.jj < 3) { bf16x8 xf[36]; LAS char* xb[4]; a_prologue<0>(P, X, smem, xf, xb); a_zblock(P, X, smem, xf, xb); }
  else if (wid < 4) { bf16x8 xf[36]; LAS char* xb[4]; a_prologue<1>(P, X, smem, xf, xb); a_qkvblock<0>(P, X, smem, xf, xb); }
  else { bf16x8 xf[36]; LAS char* xb[4]; a_prologue<2>(P, X, smem, xf, xb); a_qkvblock<1>(P, X, smem, xf, xb); }
  STAMP(0, 11);
  if (wid == 4) { bc_loader<0>(P, X, smem); STAMP_OUT(); __builtin_amdgcn_endpgm(); }
  if (wid == 5) { bc_loader<1>(P, X, smem); STAMP_OUT(); __builtin_amdgcn_endpgm(); }
  if (wid >= 6) { bc_side(P, X, smem); STAMP_OUT(); __builtin_amdgcn_endpgm(); }
  b_compute(P, X, smem);
  if (wid < 2) { c_compute<0>(P, X, smem); STAMP_OUT(); __builtin_amdgcn_endpgm(); }
  c_compute<1>(P, X, smem);
  STAMP_OUT();
}

extern "C" __device__ __attribute__((used, noinline)) void kf_code_end() {}

extern "C" void kernel_launch(void* const* d_in, const int* in_sizes, int n_in, void* d_out, int out_size, void* d_ws,
                              size_t ws_size, hipStream_t stream) {
  Params P;
  P.x = (const u16*)d_in[0]; P.wqkv = (const u16*)d_in[1]; P.wo = (const u16*)d_in[2]; P.win = (const u16*)d_in[4];
  P.wsp = (const u16*)d_in[5]; P.bsp = (const u16*)d_in[6]; P.wout = (const u16*)d_in[7];
  P.out = (u16*)d_out; P.ws = (char*)d_ws;
  (void)hipFuncSetAttribute((const void*)kF, hipFuncAttributeMaxDynamicSharedMemorySize, LDS_BYTES);
  kF<<<256, 512, LDS_BYTES, stream>>>(P);
}
```

```cpp
#define DIAG 0
#include <hip/hip_runtime.h>
#include <hip/hip_bf16.h>

#if defined(__HIP_DEVICE_COMPILE__)
#define NOPK __attribute__((target("no-packed-fp32-ops")))
#else
#define NOPK
#endif
#define LAS __attribute__((address_space(3)))
#define GAS __attribute__((address_space(1)))
typedef unsigned short u16;
typedef unsigned long long u64;
typedef short bf16x8 __attribute__((ext_vector_type(8)));
typedef short s16x4 __attribute__((ext_vector_type(4)));
typedef float f32x4 __attribute__((ext_vector_type(4)));
typedef float f32x2 __attribute__((ext_vector_type(2)));
typedef float f32x16 __attribute__((ext_vector_type(16)));
typedef unsigned u32x4 __attribute__((ext_vector_type(4)));
typedef unsigned u32x2 __attribute__((ext_vector_type(2)));
typedef __bf16 bf16x2_t __attribute__((ext_vector_type(2)));
typedef __amdgpu_buffer_rsrc_t brs_t;
typedef u32x4 rsrc_t;

#define WAIT_L0() asm volatile("s_waitcnt lgkmcnt(0)" ::: "memory")
#define BAR() do { asm volatile("" ::: "memory"); __builtin_amdgcn_s_barrier(); asm volatile("" ::: "memory"); } while (0)
#define SB0() __builtin_amdgcn_sched_barrier(0)
#define SGB(mask, n) __builtin_amdgcn_sched_group_barrier(mask, n, 0)
#define PIN(x) asm volatile("" : "+v"(x))
#define MFMA(a, b, c) __builtin_amdgcn_mfma_f32_16x16x32_bf16(a, b, c, 0, 0, 0)
#define MFMA32(a, b, c) __builtin_amdgcn_mfma_f32_32x32x16_bf16(a, b, c, 0, 0, 0)
#define DI __device__ __forceinline__
template <int N> DI void wait_v() { asm volatile("s_waitcnt vmcnt(%0)" :: "i"(N) : "memory"); }
template <int V> struct IC { static constexpr int value = V; };
template <int I, int N, class F> DI void sfor(F&& f) {
  if constexpr (I < N) { f(IC<I>{}); sfor<I + 1, N>(f); }
}

DI rsrc_t MKRS(const void* p, unsigned bytes) {
  const u64 a = (u64)p;
  return (rsrc_t){(unsigned)a, (unsigned)(a >> 32) & 0xffffu, bytes, 0x00020000u};
}
DI void BLDS(rsrc_t rs, unsigned voff, unsigned soff, LAS char* l) {
  asm volatile("s_mov_b32 m0, %3\n\ts_nop 0\n\tbuffer_load_dwordx4 %0, %1, %2 offen lds"
               :: "v"(voff), "s"(rs), "s"(soff), "s"((unsigned)(size_t)l) : "memory");
}
DI void BLDS1(rsrc_t rs, unsigned voff, unsigned soff, LAS char* l) {
  asm volatile("s_mov_b32 m0, %3\n\ts_nop 0\n\tbuffer_load_dwordx4 %0, %1, %2 offen sc1 lds"
               :: "v"(voff), "s"(rs), "s"(soff), "s"((unsigned)(size_t)l) : "memory");
}
template <int N> DI void BLDSx(rsrc_t rs, unsigned voff, unsigned soff, LAS char* l) {
  if (N == 4)
    asm volatile("s_mov_b32 m0, %3\n\ts_nop 0\n\tbuffer_load_dwordx4 %0, %1, %2 offen sc1 lds\n\tbuffer_load_dwordx4 %0, %1, %2 offen offset:1024 sc1 lds\n\t"
                 "buffer_load_dwordx4 %0, %1, %2 offen offset:2048 sc1 lds\n\tbuffer_load_dwordx4 %0, %1, %2 offen offset:3072 sc1 lds"
                 :: "v"(voff), "s"(rs), "s"(soff), "s"((unsigned)(size_t)l) : "memory");
  else if (N == 2)
    asm volatile("s_mov_b32 m0, %3\n\ts_nop 0\n\tbuffer_load_dwordx4 %0, %1, %2 offen sc1 lds\n\tbuffer_load_dwordx4 %0, %1, %2 offen offset:1024 sc1 lds"
                 :: "v"(voff), "s"(rs), "s"(soff), "s"((unsigned)(size_t)l) : "memory");
  else
    asm volatile("s_mov_b32 m0, %3\n\ts_nop 0\n\tbuffer_load_dwordx4 %0, %1, %2 offen sc1 lds"
                 :: "v"(voff), "s"(rs), "s"(soff), "s"((unsigned)(size_t)l) : "memory");
}
DI void BLDS4v(rsrc_t rs, const unsigned (&v)[4], unsigned soff, LAS char* l) {
  asm volatile("s_mov_b32 m0, %6\n\ts_nop 0\n\tbuffer_load_dwordx4 %0, %4, %5 offen lds\n\tbuffer_load_dwordx4 %1, %4, %5 offen offset:1024 lds\n\t"
               "buffer_load_dwordx4 %2, %4, %5 offen offset:2048 lds\n\tbuffer_load_dwordx4 %3, %4, %5 offen offset:3072 lds"
               :: "v"(v[0]), "v"(v[1]), "v"(v[2]), "v"(v[3]), "s"(rs), "s"(soff), "s"((unsigned)(size_t)l) : "memory");
}
DI void BLDS32(rsrc_t rs, unsigned voff, unsigned soff, LAS char* l) {
  asm volatile("s_mov_b32 m0, %3\n\ts_mov_b32 exec_hi, 0\n\tbuffer_load_dwordx4 %0, %1, %2 offen lds\n\ts_mov_b32 exec_hi, -1"
               :: "v"(voff), "s"(rs), "s"(soff), "s"((unsigned)(size_t)l) : "memory");
}
DI void BLDS3v(rsrc_t rs, const unsigned (&v)[3], unsigned soff, LAS char* l) {
  asm volatile("s_mov_b32 m0, %5\n\ts_nop 0\n\tbuffer_load_dwordx4 %0, %3, %4 offen lds\n\tbuffer_load_dwordx4 %1, %3, %4 offen offset:1024 lds\n\t"
               "buffer_load_dwordx4 %2, %3, %4 offen offset:2048 lds"
               :: "v"(v[0]), "v"(v[1]), "v"(v[2]), "s"(rs), "s"(soff), "s"((unsigned)(size_t)l) : "memory");
}
DI void BLDS15v(rsrc_t rs, const unsigned (&v)[3], unsigned soff, LAS char* l) {
  asm volatile("s_mov_b32 m0, %4\n\ts_nop 0\n\tbuffer_load_dwordx4 %0, %2, %3 offen lds\n\ts_mov_b32 exec_hi, 0\n\t"
               "buffer_load_dwordx4 %1, %2, %3 offen offset:1024 lds\n\ts_mov_b32 exec_hi, -1"
               :: "v"(v[0]), "v"(v[1]), "s"(rs), "s"(soff), "s"((unsigned)(size_t)l) : "memory");
}
template <int OFF> DI void AST16(rsrc_t rs, unsigned voff, unsigned soff, u32x4 v) {
  asm volatile("buffer_store_dwordx4 %0, %1, %2, %3 offen offset:%4 sc1\n\ts_nop 1" :: "v"(v), "v"(voff), "s"(rs), "s"(soff), "i"(OFF) : "memory");
}
DI void BLDS16(rsrc_t rs, unsigned voff, unsigned soff, LAS char* l) {
  asm volatile("s_mov_b32 m0, %3\n\ts_mov_b64 exec, 0xffff\n\tbuffer_load_dwordx4 %0, %1, %2 offen lds\n\ts_mov_b64 exec, -1"
               :: "v"(voff), "s"(rs), "s"(soff), "s"((unsigned)(size_t)l) : "memory");
}
DI void TOUCH(rsrc_t rs, unsigned voff, unsigned soff, LAS char* l) {
  asm volatile("s_mov_b32 m0, %3\n\ts_nop 0\n\tbuffer_load_dword %0, %1, %2 offen lds"
               :: "v"(voff), "s"(rs), "s"(soff), "s"((unsigned)(size_t)l) : "memory");
}
DI void ald16(u32x4& d, rsrc_t rs, unsigned voff, unsigned soff) {
  asm volatile("buffer_load_dwordx4 %0, %1, %2, %3 offen sc1" : "=v"(d) : "v"(voff), "s"(rs), "s"(soff) : "memory");
}
DI void ald8(u32x2& d, rsrc_t rs, unsigned voff, unsigned soff) {
  asm volatile("buffer_load_dwordx2 %0, %1, %2, %3 offen sc1" : "=v"(d) : "v"(voff), "s"(rs), "s"(soff) : "memory");
}
DI void ald4(unsigned& d, rsrc_t rs, unsigned voff, unsigned soff) {
  asm volatile("buffer_load_dword %0, %1, %2, %3 offen sc1" : "=v"(d) : "v"(voff), "s"(rs), "s"(soff) : "memory");
}
DI void gld8(u32x2& d, const void* p) { asm volatile("global_load_dwordx2 %0, %1, off" : "=v"(d) : "v"(p) : "memory"); }
#define ST16(rs, voff, soff, v) __builtin_amdgcn_raw_buffer_store_b128(v, rs, (int)((voff) + (soff)), 0, 16)
#define ST8(rs, voff, soff, v) __builtin_amdgcn_raw_buffer_store_b64(v, rs, (int)(voff), (int)(soff), 16)
#define ST4(rs, voff, soff, v) __builtin_amdgcn_raw_buffer_store_b32(v, rs, (int)(voff), (int)(soff), 16)
#define LD16(rs, voff, soff) __builtin_amdgcn_raw_buffer_load_b128(rs, (int)(voff), (int)(soff), 16)
#define LD8(rs, voff, soff) __builtin_amdgcn_raw_buffer_load_b64(rs, (int)(voff), (int)(soff), 16)

constexpr unsigned WS_EPOCH = 0;
constexpr unsigned WS_F1 = 1024;
constexpr unsigned WS_F2K = WS_F1 + 4096;
constexpr unsigned WS_F2Z = WS_F2K + 2048;
constexpr unsigned WS_F3O = WS_F2Z + 2048;
constexpr unsigned WS_F4 = WS_F3O + 2048;
constexpr unsigned WS_STAT = 20480;
constexpr unsigned WS_KVP = WS_STAT + 16 * 12 * 256 * 8;
constexpr unsigned WS_KVF = WS_KVP + 64 * 36864;
constexpr unsigned WS_PQ = WS_KVF + 16 * 5632;
constexpr unsigned WS_WX = WS_PQ + 128 * 16 * 1152;
constexpr unsigned WS_Z2N = WS_WX + 8 * 90 * 4608;
constexpr unsigned WS_Z2O = WS_Z2N + 16 * 64 * 8 * 1024;
constexpr unsigned WS_Z1 = WS_Z2O + 64 * 16 * 1024;
constexpr unsigned WS_HX = WS_Z1 + 128 * 72 * 1024;
constexpr unsigned WS_END = WS_HX + 128 * 90 * 1024;
constexpr unsigned SPIN_MAX = 1u << 15;

#define STAMPS
constexpr size_t WS_STAMP = 64u << 20;
constexpr size_t WS_FINE = 80u << 20;
#define STAMPX(K, i) do { if (lane_id() == 0) ((u64*)(P.ws + WS_STAMP))[(((K) * 256 + blockIdx.x) * 8 + X.wid) * 32 + (i)] = __builtin_amdgcn_s_memrealtime(); } while (0)
#define ST_SLOT(K, i) ((K) == 0 ? ((i) < 4 ? (i) : (i) - 5) : (K) == 1 ? 8 + (i) : 11 + (i))
#define STAMP(K, i) do { u64 t_; asm volatile("s_memrealtime %0\n\ts_waitcnt lgkmcnt(0)" : "=s"(t_) :: "memory"); \
    *(volatile LAS unsigned*)(smem + 159 * 1024 + 256 + X.wid * 80 + ST_SLOT(K, i) * 4) = (unsigned)t_; } while (0)
#define STAMP_OUT() do { if (blockIdx.x == 8 && X.wid == 0) { \
    const unsigned t1_ = *(volatile LAS unsigned*)(smem + 159 * 1024 + 256 + DIAG * 4) + 10000u; \
    for (int i_ = 0; i_ < 100000; ++i_) { if ((int)((unsigned)__builtin_amdgcn_s_memrealtime() - __builtin_amdgcn_readfirstlane(t1_)) >= 0) break; __builtin_amdgcn_s_sleep(2); } } } while (0)
#define FINE_DECL() do {} while (0)
#define FINE(n) do {} while (0)
#define FINE_OUT(n) do {} while (0)

struct Params {
  const u16* x; const u16* wqkv; const u16* wo; const u16* win; const u16* wsp; const u16* bsp; const u16* wout;
  u16* out; char* ws;
};

DI int lane_id() {
  int l; asm volatile("v_mbcnt_lo_u32_b32 %0, -1, 0\n\tv_mbcnt_hi_u32_b32 %0, -1, %0" : "=v"(l)); return l;
}
DI unsigned cvt_pk(float lo, float hi) {
  f32x2 v = {lo, hi}; bf16x2_t b = __builtin_convertvector(v, bf16x2_t); return __builtin_bit_cast(unsigned, b);
}
DI u32x4 norm4(u32x4 v, unsigned rlo, unsigned rhi, float sh) {
  unsigned a = v.x, b = v.y, c = v.z, d = v.w; float t0, t1, t2, t3;
  asm("v_dot2_f32_bf16 %4, %0, %8, %10\n\tv_dot2_f32_bf16 %0, %0, %9, %10\n\t"
      "v_dot2_f32_bf16 %5, %1, %8, %10\n\tv_dot2_f32_bf16 %1, %1, %9, %10\n\t"
      "v_dot2_f32_bf16 %6, %2, %8, %10\n\tv_dot2_f32_bf16 %2, %2, %9, %10\n\t"
      "v_dot2_f32_bf16 %7, %3, %8, %10\n\tv_dot2_f32_bf16 %3, %3, %9, %10\n\t"
      "v_cvt_pk_bf16_f32 %0, %4, %0\n\tv_cvt_pk_bf16_f32 %1, %5, %1\n\tv_cvt_pk_bf16_f32 %2, %6, %2\n\tv_cvt_pk_bf16_f32 %3, %7, %3"
      : "+v"(a), "+v"(b), "+v"(c), "+v"(d), "=&v"(t0), "=&v"(t1), "=&v"(t2), "=&v"(t3) : "v"(rlo), "v"(rhi), "v"(sh));
  return (u32x4){a, b, c, d};
}
DI float bf_lo(unsigned v) { return __uint_as_float(v << 16); }
DI float bf_hi(unsigned v) { return __uint_as_float(v & 0xffff0000u); }
constexpr float GC = 0.4686686931f, GKK = 4.912229539f;
DI float gelu_s(float x) {
  const float e = __builtin_amdgcn_exp2f(-x * __builtin_fmaf(x, x, GKK));
  return x * __builtin_amdgcn_rcpf(1.0f + e);
}
DI float phi_f(float t) {
  return __builtin_fmaxf(t, 1e-6f) + __builtin_amdgcn_fmed3f(__builtin_amdgcn_exp2f(t * 1.4426950409f), 0.f, 1.f);
}
DI bf16x8 tr_frag(LAS char* a0, LAS char* a1) {
  const s16x4 lo = __builtin_amdgcn_ds_read_tr16_b64_v4i16((LAS s16x4*)a0);
  const s16x4 hi = __builtin_amdgcn_ds_read_tr16_b64_v4i16((LAS s16x4*)a1);
  return __builtin_shufflevector(lo, hi, 0, 1, 2, 3, 4, 5, 6, 7);
}
template <int CTRL> DI float dpp_get(float v) {
  return __builtin_bit_cast(float, __builtin_amdgcn_update_dpp(0, __builtin_bit_cast(int, v), CTRL, 0xf, 0xf, true));
}
DI float oct_sum(float v) {
  v += dpp_get<0xB1>(v); v += dpp_get<0x4E>(v); v += dpp_get<0x141>(v); return v;
}
DI int slot_row(int rs) { return (rs & ~31) + 16 * ((rs >> 2) & 1) + 4 * ((rs >> 3) & 3) + (rs & 3); }
DI unsigned flag_ld(const char* ws, unsigned off) {
  return __hip_atomic_load((GAS unsigned*)(ws + off), __ATOMIC_RELAXED, __HIP_MEMORY_SCOPE_AGENT);
}
DI void flag_st(char* ws, unsigned off, unsigned v) {
  __hip_atomic_store((GAS unsigned*)(ws + off), v, __ATOMIC_RELAXED, __HIP_MEMORY_SCOPE_AGENT);
}
DI bool all_eq(unsigned v, unsigned tag) { return __builtin_amdgcn_ballot_w64(v != tag) == 0; }
DI void spin_flag(const char* ws, unsigned off, unsigned tag) {
  const rsrc_t rs = MKRS(ws, WS_END);
#pragma unroll 1
  for (unsigned i = 0; i < SPIN_MAX; ++i) {
    unsigned v;
    asm volatile("buffer_load_dword %0, %1, %2, 0 offen sc1\n\ts_waitcnt vmcnt(0)" : "=&v"(v) : "v"(off), "s"(rs) : "memory");
    if (all_eq(v, tag)) break;
    __builtin_amdgcn_s_sleep(1);
  }
}

constexpr int LDS_BYTES = 160 * 1024;
constexpr int L_SCR = 159 * 1024;
constexpr int A_XN = 15 * 1024;
constexpr int AZ_SLOT = 24 * 1024, AZ_PARK = 96 * 1024, AZ_ST = 144 * 1024, AZ_ST2 = 147 * 1024, AZ_TAB = 151 * 1024;
constexpr int AQ_SLOT = 20 * 1024, AQ_PK = 80 * 1024, AQ_V = 116 * 1024;
constexpr int STG = 34 * 1024, B_KV = 4 * STG;
#define TMODE 1
#define OUTAUX 0
#define XRD(ks) (*(LAS bf16x8*)(xb[(ks) & 3] + ((ks) >> 2) * 16384))
#define SLOT(n) ((((n) + 2) & 3) * STG)
#define ROTB 3
#define ROTC 3
#define ROT0 0
#define ROT(n, r, m) ((n) < ROT0 ? (n) : (((n) & ~(m)) | (((n) + (r)) & (m))))

struct Ctx {
  int pa, quad, jj, p, c, wid, bid;
  unsigned tag;
};

template <int ROLE> struct Loader {
  rsrc_t rsws, rsw; unsigned voff[4]; unsigned l16; int p, c; LAS char* smem;
  DI void init(const Params& P, const Ctx& X, LAS char* sm) {
    smem = sm; p = X.p; c = X.c;
    const int ln = lane_id();
    l16 = ln * 16;
    rsws = MKRS(P.ws, WS_END);
    if (ROLE == 1) {
      rsw = MKRS((const char*)P.wsp + (size_t)p * 128 * 4096, 128 * 4096);
#pragma unroll
      for (int i = 0; i < 4; ++i) {
        const int rs = i * 8 + (ln >> 3);
        voff[i] = (unsigned)(slot_row(rs) * 4096 + (((ln & 7) ^ ((rs & 15) >> 1)) * 16) - i * 1024);
      }
    }
  }
  DI void issue_b(int N) {
    LAS char* d = smem + SLOT(N);
    const int n = ROT(N, p + c, ROTB);
    if (ROLE == 0) {
      const unsigned so = WS_Z2N + c * (64 * 8 * 1024) + n * 16384;
#pragma unroll
      for (int i = 0; i < 4; ++i) BLDSx<4>(rsws, l16, so + i * 4096, d + i * 4096);
      BLDSx<1>(rsws, l16, WS_Z2O + (n * 32 + c) * 1024, d + 16384);
      BLDSx<1>(rsws, l16, WS_Z2O + (n * 32 + 16 + c) * 1024, d + 17408);
    } else {
#pragma unroll
      for (int i = 0; i < 4; ++i) BLDS4v(rsw, voff, n * 128 + i * (32 * 4096), d + 18432 + i * 4096);
    }
  }
  template <int NK> DI void issue_c(int N) {
    LAS char* d = smem + SLOT(N);
    const int M = N - 32, rg = 2 * p + (c >> 3), cg = c & 7;
    const int kb = M == 0 ? 88 : M < 5 ? 72 + 4 * (M - 1) : 4 * (M < 21 ? ROT(M - 5, p + c, ROTC) : M - 5);
    if (ROLE == 0) {
      const unsigned so = WS_WX + (cg * 90 + kb) * 4608;
#pragma unroll
      for (int i = 0; i < NK; ++i) BLDSx<4>(rsws, l16, so + i * 4096, d + i * 4096);
      BLDSx<NK / 2>(rsws, l16, so + NK * 4096, d + NK * 4096);
    } else {
#pragma unroll
      for (int i = 0; i < 4; ++i) BLDSx<NK>(rsws, l16, WS_HX + ((rg * 4 + i) * 90 + kb) * 1024, d + 18432 + i * 4096);
    }
  }
};
DI void first_w(const Params& P, const Ctx& X, LAS char* smem) {
  const int ln = lane_id(), st = X.wid >> 2, g = X.wid & 3;
  unsigned voff[4];
#pragma unroll
  for (int i = 0; i < 4; ++i) {
    const int rs = i * 8 + (ln >> 3);
    voff[i] = (unsigned)(slot_row(rs) * 4096 + (((ln & 7) ^ ((rs & 15) >> 1)) * 16) - i * 1024);
  }
  BLDS4v(MKRS((const char*)P.wsp + (size_t)X.p * 128 * 4096, 128 * 4096), voff, ROT(st, X.p + X.c, ROTB) * 128 + g * (32 * 4096),
         smem + SLOT(st) + 18432 + g * 4096);
}
DI void first_z(const Params& P, const Ctx& X, LAS char* smem) {
  const int st = X.wid >> 2, g = X.wid & 3, n = ROT(st, X.p + X.c, ROTB);
  const unsigned l16 = lane_id() * 16;
  const rsrc_t rs = MKRS(P.ws, WS_END);
  BLDSx<4>(rs, l16, WS_Z2N + X.c * (64 * 8 * 1024) + n * 16384 + g * 4096, smem + SLOT(st) + g * 4096);
  BLDSx<1>(rs, l16, WS_Z2O + (n * 32 + (g & 1) * 16 + X.c) * 1024, smem + SLOT(st) + 16384 + (g & 1) * 1024);
}
DI void warm_code(const Ctx& X, LAS char* smem) {
  u64 pc; unsigned len;
  asm volatile("s_getpc_b64 %0\n1:\n\ts_mov_b32 %1, 1b-_Z2kF6Params" : "=s"(pc), "=s"(len));
  pc -= len;
  asm volatile("s_mov_b32 %0, kf_code_end-_Z2kF6Params" : "=s"(len));
  if ((int)len < 0) len = 0;
  TOUCH(MKRS((const void*)pc, len), lane_id() * 128, (X.bid >> 3) * 8192, smem + L_SCR);
}
DI void warm_b(const Params& P, const Ctx& X, LAS char* smem) {
  u64 pc; unsigned len;
  asm volatile("s_getpc_b64 %0\n1:\n\ts_mov_b32 %1, 1b-_Z2kF6Params" : "=s"(pc), "=s"(len));
  pc -= len;
  asm volatile("s_mov_b32 %0, kf_code_end-_Z2kF6Params" : "=s"(len));
  if ((int)len < 0) len = 0;
  const bool code = X.wid == 7;
  const int l = lane_id();
  TOUCH(MKRS(code ? (const void*)pc : (const void*)P.wsp, code ? len : X.wid == 6 ? 2048u * 4096u : 0u), code ? l * 128 : (l >> 2) * 4096 + (l & 3) * 128,
        code ? (X.bid >> 3) * 8192 : (X.p * 128 + (X.c & 7) * 16) * 4096, smem + L_SCR);
}
DI unsigned f1_off(const Ctx& X) { const int l = lane_id(); return WS_F1 + X.c * 256 + (l < 48 ? l : 47) * 4; }
DI int f1_src(int c, int w) { return (w == 0 ? 8 * c : w == 1 ? 8 * c + 7 : 128 + c) / 12; }

#define Z_XB1 140
#define Z_GAT 151
#define Z_XB2 164
#define Z_F1 200
#define Z_FW 211
#define Z_POLL 210
struct ZS {
  static constexpr int XB1 = Z_XB1, PUB = Z_XB1 + 2, GAT = Z_GAT, XB2 = Z_XB2, F1 = Z_F1, FW = Z_FW, POLL = Z_POLL, WARM = 135;
  static constexpr bool sbar(int E) { return E % 12 == 8 && E < 212; }
  static constexpr bool wsp(int E) { return !(TMODE & 4) && (E == 45 || E == 57 || E == 69); }
  static constexpr int nrm(int ch) { return XB2 + 6 + 2 * ch + ch / 3; }
  static constexpr bool dma(int E) { return E >= 12 && (E + 4) / 12 + 2 < 18 && E % 12 < 3; }
  static constexpr int other(int E) {
    int n = (E == PUB) + (E >= GAT && E < GAT + 3) + (E == F1) + (E == POLL) + (E == WARM) + wsp(E) + (E == 155 || E == 164 || E == 201);
    for (int ch = 0; ch < 6; ++ch) n += E == nrm(ch) + 1;
    return n;
  }
  static constexpr int younger(int SN) {
    const int last = 12 * SN - 22, bar = 12 * SN - 4;
    int n = (SN == 1 ? 3 : 0) + ((TMODE & 1) && SN <= 2 ? 1 : 0);
    for (int E = last < 0 ? 0 : last; E < bar; ++E) n += other(E) + ((E > last && dma(E)) ? 1 : 0);
    return n;
  }
  static constexpr int after_gather() {
    int n = 0;
    for (int E = GAT + 3; E < XB2 - 3; ++E) n += other(E) + (dma(E) ? 1 : 0);
    return n;
  }
  static constexpr int after_z2n() {
    int n = 0;
    for (int E = nrm(5) + 2; E < F1; ++E) n += other(E) + (dma(E) ? 1 : 0);
    return n;
  }
};
DI unsigned z_voff(int wid, int lane) {
  const int row = wid * 8 + (lane >> 3);
  return (unsigned)(row * 9216 + (((lane & 7) ^ (((row >> 1) & 1) << 2)) * 16));
}
DI void a_zblock(const Params& P, const Ctx& X, LAS char* smem, bf16x8 (&xf)[36], LAS char* (&xb)[4]) {
  const int wid = X.wid, p = X.pa, j = X.quad * 3 + X.jj;
  int lane = lane_id();
  const int rw = wid & 3, G = wid >> 2;
  const int h = lane >> 5;
  const rsrc_t rsw = MKRS(P.win, 576 * 9216), rsws = MKRS(P.ws, WS_END);
  const brs_t ws = __builtin_amdgcn_make_buffer_rsrc(P.ws, 0, WS_END, 0x00020000);
  const unsigned voff = z_voff(wid, lane);
  const int cz2 = (2304 + 3 * j * 64) * 2, cz1 = 3 * j * 64 * 2;
  auto dma = [&](auto S_, auto I_) {
    constexpr int S = decltype(S_)::value, I = decltype(I_)::value, R = S / 3;
    BLDS(rsw, voff, (R < 3 ? cz2 + R * 128 : cz1 + (R - 3) * 128) + ((S % 3) * 192 + I * 64) * 9216,
         smem + (S & 3) * AZ_SLOT + I * 8192 + wid * 1024);
  };
  const int rq = (lane >> 2) & 3;
  LAS char* rd = smem + (8 * h + rq) * 128 + ((G ^ (rq >> 1)) * 64) + ((lane >> 4) & 1) * 32 + (lane & 3) * 8;
  LAS char* park = smem + AZ_PARK + wid * 2048 + h * 1024 + (lane & 31) * 16;
  LAS char* strd = smem;
  const unsigned zo = WS_Z1 + ((p * 8 + rw * 2) * 72 + 6 * j + G) * 1024;
  unsigned zv = 0, nvo = 0, fpoll = 0, f1v = 0;
  const bf16x8 ones = {0x3f80, 0x3f80, 0x3f80, 0x3f80, 0x3f80, 0x3f80, 0x3f80, 0x3f80};
  f32x16 acc[2];
  bf16x8 wf[5], pf[2];
  f32x4 ssum[2], ssq[2];
  unsigned pk[4];
  float gt[4] = {0.f, 0.f, 0.f, 0.f}, gb = 0.f, xt[4];
  u32x4 gv[3];
  f32x4 tab[2][4];
  u32x4 nv[2];
  auto rdw = [&](auto E_) {
    constexpr int E = decltype(E_)::value, S = E / 12, o = (S & 3) * AZ_SLOT + (E % 12) * 2048;
    wf[E % 5] = tr_frag(rd + o, rd + o + 512);
  };
  auto stats_rd = [&](int rr) {
    if (rr == 0) {
      const int ln = lane_id();
      strd = smem + AZ_PARK + wid * 2048 + ((ln >> 1) & 1) * 512 + (8 * (ln >> 4) + ((ln >> 2) & 3)) * 16 + (ln & 1) * 8;
    }
#pragma unroll
    for (int u = 0; u < 2; ++u) pf[u] = tr_frag(strd + rr * 16384 + u * 1024, strd + rr * 16384 + u * 1024 + 64);
  };
  auto stats_mm = [&](auto RR_) {
    constexpr int RR = decltype(RR_)::value;
#pragma unroll
    for (int u = 0; u < 2; ++u) {
      const f32x4 zz = {0, 0, 0, 0};
      ssum[u] = MFMA(ones, pf[u], RR ? ssum[u] : zz);
      ssq[u] = MFMA(pf[u], pf[u], RR ? ssq[u] : zz);
      PIN(ssum[u]); PIN(ssq[u]);
    }
  };
  auto gelu_st = [&](auto RR_, auto EL_, auto ST_) {
    constexpr int RR = decltype(RR_)::value, EL = decltype(EL_)::value, ST = decltype(ST_)::value;
    if constexpr (EL >= 0 && EL < 16) {
      constexpr int reg = RR < 3 ? EL : (EL & 3) + ((EL >> 2) & 1) * 8 + (EL >> 3) * 4;
      const float x = acc[RR & 1][reg];
      float& g = gt[EL & 3];
      if (ST == 0) { g = __builtin_fmaf(x, x, GKK); PIN(g); }
      if (ST == 1) { g = __builtin_amdgcn_exp2f(-x * g); PIN(g); }
      if (ST == 2) { g = __builtin_amdgcn_rcpf(1.0f + g); PIN(g); }
      if (ST == 3) {
        const float y = x * g;
        if (EL & 1) { pk[(EL >> 1) & 3] = cvt_pk(gb, y); PIN(pk[(EL >> 1) & 3]); } else { gb = y; PIN(gb); }
      }
    }
  };
  auto put = [&](auto RR_, auto CC_) {
    constexpr int RR = decltype(RR_)::value, CC = decltype(CC_)::value;
    const u32x4 w = {pk[0], pk[1], pk[2], pk[3]};
    if constexpr (RR < 3) *(LAS u32x4*)(park + RR * 16384 + CC * 512) = w;
    else AST16<(RR - 3) * 2048 + CC * 512>(rsws, zv, zo, w);
  };
  auto gather = [&](int i) {
    const int t = wid * 64 + lane_id();
    ald16(gv[i], rsws, (t & 127) * 16 + ((t >> 7) * 3 + i) * 2048, WS_STAT + p * 12 * 2048);
  };
  FINE_DECL();
  FINE(0);
  sfor<0, 4>(rdw);
  sfor<0, 216>([&](auto E_) {
    constexpr int E = decltype(E_)::value, R = E / 36, ks = E % 36, EN = E + 4, SN = EN / 12;
    if constexpr (E == ZS::XB1 || E == ZS::XB2) asm volatile("s_waitcnt lgkmcnt(4)" ::: "memory");
    if constexpr (E == 8) asm volatile("s_waitcnt lgkmcnt(8)" ::: "memory");
    if constexpr (E == ZS::F1) wait_v<ZS::after_z2n()>();
    if constexpr (ZS::sbar(E)) wait_v<ZS::younger(SN)>();
    if constexpr (ZS::sbar(E) || E == ZS::XB1 || E == ZS::XB2 || E == ZS::F1) BAR();
    if constexpr (ZS::sbar(E)) FINE(SN);
    if constexpr (E == ZS::F1) ST4(ws, f1v, WS_F1, X.tag);
    if constexpr (E == ZS::F1 - 10) {
      const int ln = lane_id();
      f1v = (wid == 0 && ln < 48 && f1_src(ln & 15, ln >> 4) == j) ? (ln & 15) * 256 + ((ln >> 4) * 16 + p) * 4 : 0x7fffff00u; PIN(f1v);
    }
    if constexpr (EN < 216) {
      rdw(IC<EN>{});
      if constexpr (E < 10) { xf[16 + 2 * E] = XRD(16 + 2 * E); xf[17 + 2 * E] = XRD(17 + 2 * E); }
      if constexpr (ZS::dma(E)) dma(IC<SN + 2>{}, IC<EN % 12 - 4>{});
    }
    const f32x16 zz = {};
    if constexpr (R < 3) acc[R & 1] = MFMA32(xf[ks], wf[E % 5], ks ? acc[R & 1] : zz);
    else acc[R & 1] = MFMA32(wf[E % 5], xf[ks], ks ? acc[R & 1] : zz);
    PIN(acc[R & 1]);
    if constexpr ((R == 1 || R == 2 || R == 5) && ks >= 1) {
      gelu_st(IC<R - 1>{}, IC<(ks - 1) / 2>{}, IC<(ks - 1) & 1>{});
      gelu_st(IC<R - 1>{}, IC<(ks - 1) / 2 - 1>{}, IC<((ks - 1) & 1) + 2>{});
      if constexpr (ks == 21) put(IC<R - 1>{}, IC<0>{});
      if constexpr (ks == 34 && R < 5) put(IC<R - 1>{}, IC<1>{});
    }
    if constexpr (R == 3 || R == 4) {
      sfor<0, 4>([&](auto ST_) { gelu_st(IC<R - 1>{}, IC<ks - 1 - decltype(ST_)::value>{}, ST_); });
      if constexpr (ks == 11) put(IC<R - 1>{}, IC<0>{});
      if constexpr (ks == 20) put(IC<R - 1>{}, IC<1>{});
    }
    if constexpr (R == 3) {
      if constexpr (ks == 17 || ks == 20 || ks == 23) stats_mm(IC<(ks - 17) / 3>{});
      if constexpr (ks == 14 || ks == 17 || ks == 20) stats_rd((ks - 14) / 3);
      if constexpr (ks == 33) {
        const int ln = lane_id();
        zv = ((ln >> 4) & 1) * (72 * 1024) + (ln >> 5) * 256 + (ln & 15) * 16; PIN(zv);
        nvo = ((ln >> 4) & 1) * 1024 + (ln >> 5) * 512 + (ln & 15) * 16; PIN(nvo);
      }
    }
    if constexpr (E == ZS::XB1 - 3) {
      const int ln = lane_id();
      const int dst = (ln >> 4) == ((ln >> 2) & 3) ? G * 1024 + (rw * 32 + (ln & 15)) * 8 : 2048 + (ln & 15) * 8;
#pragma unroll
      for (int u = 0; u < 2; ++u) {
        const float q = (ln & 2) ? ((ln & 1) ? ssq[u][3] : ssq[u][2]) : ((ln & 1) ? ssq[u][1] : ssq[u][0]);
        *(LAS f32x2*)(smem + AZ_ST + dst + u * 128) = (f32x2){ssum[u][0], q};
      }
    }
    if constexpr (E == ZS::XB1) {
      const int t8 = rw * 64 + lane_id();
      xt[0] = *(LAS float*)(smem + AZ_ST + t8 * 4); xt[1] = *(LAS float*)(smem + AZ_ST + 1024 + t8 * 4);
    }
    if constexpr (E == ZS::PUB) {
      const int t8 = rw * 64 + lane_id();
      ST8(ws, t8 * 8, G ? 0x7fffff00u : WS_STAT + (p * 12 + j) * 2048, ((u32x2){__float_as_uint(xt[0] + xt[1]), X.tag}));
    }
    if constexpr (E >= ZS::GAT && E < ZS::GAT + 3) { asm volatile("" ::: "memory"); gather(E - ZS::GAT); asm volatile("" ::: "memory"); }
    if constexpr (E == ZS::XB2 - 3) {
      wait_v<ZS::after_gather()>();
#pragma unroll 1
      for (unsigned spins = 0; spins < SPIN_MAX; ++spins) {
        PIN(gv[0]); PIN(gv[1]); PIN(gv[2]);
        bool ok = true;
#pragma unroll
        for (int i = 0; i < 3; ++i) ok &= gv[i].y == X.tag && gv[i].w == X.tag;
        if (__builtin_amdgcn_ballot_w64(!ok) == 0) break;
        __builtin_amdgcn_s_sleep(1);
        asm volatile("" ::: "memory");
#pragma unroll
        for (int i = 0; i < 3; ++i) gather(i);
        wait_v<0>();
      }
      *(LAS f32x2*)(smem + AZ_ST2 + (wid * 64 + lane_id()) * 8) =
          (f32x2){__uint_as_float(gv[0].x) + __uint_as_float(gv[1].x) + __uint_as_float(gv[2].x),
                  __uint_as_float(gv[0].z) + __uint_as_float(gv[1].z) + __uint_as_float(gv[2].z)};
    }
    if constexpr (E == ZS::XB2) {
      LAS float* a = (LAS float*)(smem + AZ_ST2 + rw * 256 + lane_id() * 4);
#pragma unroll
      for (int i = 0; i < 4; ++i) xt[i] = a[i * 256];
    }
    if constexpr (E == ZS::XB2 + 3) {
      const int ln = lane_id();
      const float v = ((xt[0] + xt[1]) + (xt[2] + xt[3])) * (1.0f / 2304.0f), o = dpp_get<0xB1>(v);
      const float m = (ln & 1) ? o : v, q = (ln & 1) ? v : o;
      const float sc = __builtin_amdgcn_rsqf(__builtin_fmaxf(q - m * m, 0.f) + 1e-5f * GC * GC);
      *(LAS float*)(smem + AZ_TAB + wid * 256 + ln * 4) = (ln & 1) ? -m * sc : sc;
    }
    sfor<0, 6>([&](auto CH_) {
      constexpr int ch = decltype(CH_)::value, cc = ch / 3, rr = ch % 3, q0 = ZS::nrm(ch);
      if constexpr (E == q0 || E == q0 + 1) {
        constexpr int hf = E - q0;
#pragma unroll
        for (int i = 2 * hf; i < 2 * hf + 2; ++i) {
          unsigned t = nv[ch & 1][i];
          t = cvt_pk(__builtin_fmaf(bf_lo(t), tab[cc][i][0], tab[cc][i][1]), __builtin_fmaf(bf_hi(t), tab[cc][i][2], tab[cc][i][3]));
          PIN(t); nv[ch & 1][i] = t;
        }
        if constexpr (hf == 1) {
          const int tg = (3 * j + rr) * 4 + 2 * G, sb = p * 4 + rw;
          AST16<cc * 256>(rsws, nvo, tg < 128 ? WS_Z2N + (((tg >> 3) * 64 + sb) * 8 + (tg & 7)) * 1024 : WS_Z2O + (sb * 16 + tg - 128) * 1024, nv[ch & 1]);
        }
      }
      if constexpr (rr == 0 && E == q0 - 2) {
        const int hb = (lane_id() >> 5) * 128;
#pragma unroll
        for (int i = 0; i < 4; ++i) tab[cc][i] = *(LAS f32x4*)(smem + AZ_TAB + wid * 256 + hb + cc * 64 + i * 16);
      }
      if constexpr (E == q0 - 2) nv[ch & 1] = *(LAS u32x4*)(park + rr * 16384 + cc * 512);
    });
    if constexpr (E == ZS::POLL) {
      asm volatile("" ::: "memory");
      ald4(fpoll, rsws, f1_off(X), 0);
    }
    if constexpr (E == ZS::FW) first_w(P, X, smem);
    if constexpr (E == ZS::WARM) warm_b(P, X, smem);
    if constexpr (ZS::wsp(E)) {
      constexpr int ev = (E - 45) / 12;
      TOUCH(MKRS(P.wsp, (wid >> 1) == ev ? 2048u * 4096u : 0u), lane_id() * 128, ((p * 12 + j) * 6 + wid) * 8192, smem + L_SCR);
    }
    SB0();
  });
  FINE(18); FINE_OUT(19);
  STAMP(0, 9);
  wait_v<4>(); PIN(fpoll);
  const bool rdy = all_eq(fpoll, X.tag);
  if (rdy) first_z(P, X, smem);
  STAMP(0, 10);
  u32x4 w[2];
#pragma unroll
  for (int cc = 0; cc < 2; ++cc) {
#pragma unroll
    for (int i = 0; i < 4; ++i) {
      const int reg = ((2 * i) & 3) + (i >> 1) * 8 + cc * 4;
      w[cc][i] = cvt_pk(gelu_s(acc[1][reg]), gelu_s(acc[1][reg + 1]));
    }
    PIN(w[cc]);
    if (cc == 0 && !rdy) { spin_flag(P.ws, f1_off(X), X.tag); STAMP(0, 13); first_z(P, X, smem); }
  }
  put(IC<4>{}, IC<1>{});
  AST16<0>(rsws, zv, zo + 4096, w[0]); AST16<512>(rsws, zv, zo + 4096, w[1]);
  if (wid < 4) {
    lane = lane_id();
    const int kb0 = ((p & 1) * 12 + j) * 4 + rw, kb = kb0 < 90 ? kb0 : 0;
    const rsrc_t rs = kb < 72 ? MKRS(P.wout, 2304 * 1152) : MKRS(P.wo, 576 * 1152);
    const int r0 = kb < 72 ? 32 * kb : kb < 88 ? 36 * (kb - 72) : 288 * (kb - 88) + 32;
    const int mq = kb < 64 ? 4 : kb < 72 ? 8 : kb < 88 ? 4 : 72, mu = kb < 64 ? 16 : kb < 72 ? 4 : kb < 88 ? 16 : 36;
#pragma unroll
    for (int i = 0; i < 5; ++i) {
      const int pc = i < 4 ? i * 64 + lane : 256 + (lane & 31), k = (pc * 57) >> 9, ch = pc - k * 9;
      BLDS(rs, ((k >> 3) * mq + ((k >> 2) & 1) * mu + (k & 3)) * 1152 + ch * 16, r0 * 1152 + (p >> 1) * 144, smem + B_KV + rw * 5120 + i * 1024);
    }
    wait_v<8>();
  } else {
    wait_v<3>();
  }
}

template <int G> struct QS {
  static constexpr int nt(int R) { return R == (G ? 0 : 2) ? 3 : 2; }
  static constexpr int t0(int R) { return G ? (R == 1 ? 4 : 5) - nt(R) : 0; }
  static constexpr int base(int R) { return R == 0 ? 0 : base(R - 1) + 36 * nt(R - 1); }
  static constexpr int NE = base(3);
  static constexpr int round(int E) { return E < base(1) ? 0 : E < base(2) ? 1 : 2; }
  static constexpr int ks(int E) { return (E - base(round(E))) / nt(round(E)); }
  static constexpr int tile(int E) { return (E - base(round(E))) % nt(round(E)); }
  static constexpr int stage(int E) { return 9 * round(E) + ks(E) / 4; }
  static constexpr int pos(int E) { return (E - base(round(E))) % (4 * nt(round(E))); }
  static constexpr int ent(int el) { return base(1) + 4 + (G ? el + el / 3 : 2 * el); }
  static constexpr int KV0 = base(2) + 8 * nt(2) - 4;
  static constexpr int NU = G ? 7 : 4, UL = G ? 4 : 6;
  static constexpr int ND = G ? 2 : 3, PRE = 3;
  static constexpr int dma(int E) {
    return (E + 4 < NE && pos(E + 4) >= 1 && pos(E + 4) <= 2 * ND && (pos(E + 4) & 1) && stage(E + 4) + 2 < 27 && stage(E + 4) + 2 >= PRE) ? 1 + pos(E + 4) / 2 : 0;
  }
  static constexpr int WARM = base(2) - 10;
  static constexpr int other(int E) { return ((E >= KV0 && E < KV0 + NU * UL && (E - KV0) % UL == UL - 1) ? 1 : 0) + (E == WARM); }
  static constexpr int younger(int Eb) {
    const int SN = stage(Eb + 4);
    int n = 0;
    for (int E = Eb - 1; E >= 0; --E) {
      n += other(E);
      if (dma(E)) { if (stage(E + 4) + 2 == SN) return n; n += 1; }
    }
    return n + (PRE - 1 - SN) * ND + ((TMODE & 1) ? 2 : 0);
  }
};
template <int G> DI void q_voff(unsigned (&voff)[3], int rw, int lane) {
#pragma unroll
  for (int i = 0; i < QS<G>::ND; ++i) {
    const int pc = (G ? 768 + rw * 128 : rw * 192) + i * 64 + lane, sr = (pc * 3277) >> 16, ch = pc - sr * 20;
    voff[i] = (unsigned)(sr * 3456 + ch * 16 + (ch >= 18 ? 1120 : 0));
  }
}
template <int G> DI void q_first(const Params& P, const Ctx& X, LAS char* smem) {
  const rsrc_t rsw = MKRS(P.wqkv, 576 * 3456);
  const int rw = X.wid & 3;
  unsigned voff[3];
  q_voff<G>(voff, rw, lane_id());
#pragma unroll
  for (int S = 0; S < QS<G>::PRE; ++S)
#pragma unroll
    for (int i = 0; i < QS<G>::ND; ++i)
      BLDS(rsw, voff[i], X.quad * 288 + S * 64 * 3456 + 1152, smem + S * AQ_SLOT + (G ? 12288 + rw * 2048 : rw * 3072) + i * 1024);
}
template <int G>
DI void a_qkvblock(const Params& P, const Ctx& X, LAS char* smem, bf16x8 (&xf)[36], LAS char* (&xb)[4]) {
  typedef QS<G> Q;
  const int wid = X.wid, p = X.pa, quad = X.quad;
  int lane = lane_id();
  const int rw = wid & 3, h = lane >> 5;
  const rsrc_t rsw = MKRS(P.wqkv, 576 * 3456), rsws = MKRS(P.ws, WS_END);
  const brs_t ws = __builtin_amdgcn_make_buffer_rsrc(P.ws, 0, WS_END, 0x00020000);
  unsigned voff[3];
  q_voff<G>(voff, rw, lane);
  auto dma = [&](auto S_, auto I_) {
    constexpr int S = decltype(S_)::value, I = decltype(I_)::value;
    if constexpr (S == 9) {
      const unsigned c = voff[I] % 3456u;
      voff[I] -= c >= 1408u ? 1152u : 0u; PIN(voff[I]);
    }
    BLDS(rsw, voff[I], quad * 288 + (S % 9) * 64 * 3456 + ((S / 9 + 1) % 3) * 1152,
         smem + (S & 3) * AQ_SLOT + (G ? 12288 + rw * 2048 : rw * 3072) + I * 1024);
  };
  LAS char* rd = smem + (8 * h + ((lane >> 2) & 3)) * 320 + ((lane >> 4) & 1) * 32 + (lane & 3) * 8;
  const bool isv = (lane >> 4) & 1;
  LAS char* wimg = smem + AQ_PK + ((lane >> 4) & 1) * 4096 + rw * 1024 + h * 512 + (lane & 15) * 16;
  LAS char* rimg = smem;
  f32x16 acc[3];
  f32x4 dd;
  unsigned prev[3][8], fpoll = 0, l16 = 0;
  bf16x8 wf[5], ka[4], va[4];
  float pa[2], pe[2], pt[2], pm[2], pl = 0.f;
  auto rdw = [&](auto E_) {
    constexpr int E = decltype(E_)::value, o = (Q::stage(E) & 3) * AQ_SLOT + (Q::ks(E) & 3) * 5120 + (Q::t0(Q::round(E)) + Q::tile(E)) * 64;
    wf[E % 5] = tr_frag(rd + o, rd + o + 1280);
  };
  auto topk = [&](auto T_) {
    constexpr int t = decltype(T_)::value;
#pragma unroll
    for (int i = 0; i < 8; ++i) { prev[t][i] = cvt_pk(acc[t][2 * i], acc[t][2 * i + 1]); PIN(prev[t][i]); }
  };
  FINE_DECL();
  FINE(0);
  sfor<0, 4>(rdw);
  sfor<0, Q::NE>([&](auto E_) {
    constexpr int E = decltype(E_)::value, R = Q::round(E), ks = Q::ks(E), t = Q::tile(E), EN = E + 4, er = E - Q::base(R);
    if constexpr (EN < Q::NE) {
      if constexpr (Q::pos(EN) == 0) {
        wait_v<Q::younger(E)>();
        if (Q::stage(EN) == 20) asm volatile("s_waitcnt lgkmcnt(2)" ::: "memory");
        if (Q::stage(EN) == 1) WAIT_L0();
        BAR(); FINE(Q::stage(EN));
        if constexpr (G == 0 && Q::stage(EN) == 26) {
          const int ln = lane_id();
          ST4(ws, (wid == 0 && ln < 4) ? (quad * 4 + ln) * 128 + p * 4 : 0x7fffff00u, WS_F2K, X.tag);
        }
      }
      rdw(IC<EN>{});
      if constexpr (E < 20) xf[16 + E] = XRD(16 + E);
      if constexpr (Q::dma(E)) dma(IC<Q::stage(EN) + 2>{}, IC<Q::dma(E) - 1>{});
    }
    if constexpr (R >= 1 && er < Q::nt(R - 1)) topk(IC<er>{});
    const f32x16 zz = {};
    if constexpr (R == 2) acc[t] = MFMA32(wf[E % 5], xf[ks], ks ? acc[t] : zz);
    else acc[t] = MFMA32(xf[ks], wf[E % 5], ks ? acc[t] : zz);
    PIN(acc[t]);
    if constexpr (R == 1) {
      sfor<0, 16 * Q::nt(0)>([&](auto L_) {
        constexpr int el = decltype(L_)::value, tl = el / 16, w = (el & 15) / 2, b = el & 1;
        if constexpr (E == Q::ent(el) + 2) {
          const float r = pa[b] + pe[b];
          constexpr bool mixed = Q::t0(0) + tl == 4;
          if constexpr (b == 0) { pl = r; PIN(pl); }
          else { const unsigned o = cvt_pk(pl, r); prev[tl][w] = (mixed && isv) ? prev[tl][w] : o; PIN(prev[tl][w]); }
          if constexpr ((el & 7) == 7)
            *(LAS u32x4*)(wimg + (mixed && isv ? AQ_V - AQ_PK - 4096 : 0) + (Q::t0(0) + tl) * 8192 + (w >> 2) * 256) =
                (u32x4){prev[tl][w - 3], prev[tl][w - 2], prev[tl][w - 1], prev[tl][w]};
        }
        if constexpr (E == Q::ent(el) + 1) {
          pe[b] = __builtin_amdgcn_fmed3f(__builtin_amdgcn_exp2f(pm[b]), 0.f, 1.f); pa[b] = __builtin_fmaxf(pt[b], 1e-6f); PIN(pe[b]); PIN(pa[b]);
        }
        if constexpr (E == Q::ent(el)) { pt[b] = b ? bf_hi(prev[tl][w]) : bf_lo(prev[tl][w]); pm[b] = pt[b] * 1.4426950409f; PIN(pt[b]); PIN(pm[b]); }
      });
    }
    if constexpr (R == 2 && er >= 3 && er < 3 + 2 * Q::nt(1)) {
      constexpr int tl = (er - 3) / 2, cc = (er - 3) & 1;
      *(LAS u32x4*)(wimg + (AQ_V - AQ_PK) + (Q::t0(1) + tl) * 8192 + cc * 256) =
          (u32x4){prev[tl][4 * cc], prev[tl][4 * cc + 1], prev[tl][4 * cc + 2], prev[tl][4 * cc + 3]};
    }
    if constexpr (E >= Q::KV0 && E < Q::KV0 + Q::NU * Q::UL) {
      constexpr int un = (E - Q::KV0) / Q::UL, ph = (E - Q::KV0) % Q::UL, it = un / 4, dj = un % 4;
      const int i = it ? 8 : wid;
      if constexpr (ph == 0) {
        if constexpr (un == 0) { l16 = lane_id() * 16; rimg = smem + AQ_PK + l16; }
        if constexpr (dj == 0) {
#pragma unroll
          for (int k = 0; k < 4; ++k) ka[k] = *(LAS bf16x8*)(rimg + (i * 4 + k) * 1024);
        } else {
          const int jv = i + dj - 2 < 0 ? 0 : i + dj - 2;
#pragma unroll
          for (int k = 0; k < 4; ++k) va[k] = *(LAS bf16x8*)(rimg + (AQ_V - AQ_PK) + (jv * 4 + k) * 1024);
        }
      }
      if constexpr (ph == Q::UL - 2) {
        const bf16x8 ones = {0x3f80, 0x3f80, 0x3f80, 0x3f80, 0x3f80, 0x3f80, 0x3f80, 0x3f80};
        dd = (f32x4){0, 0, 0, 0};
#pragma unroll
        for (int k = 0; k < 4; ++k) dd = MFMA(ka[k], dj ? va[k] : ones, dd);
        PIN(dd);
      }
      if constexpr (ph == Q::UL - 1)
        ST16(ws, l16 + dj * 1024, (it && wid != 4) ? 0x7ff00000u : WS_KVP + (quad * 16 + p) * 36864 + i * 4096, __builtin_bit_cast(u32x4, dd));
    }
    if constexpr (E == Q::WARM) warm_b(P, X, smem);
    if constexpr (EN < Q::NE && Q::stage(EN) == 26 && Q::pos(EN) == 1) { ald4(fpoll, rsws, f1_off(X), 0); first_w(P, X, smem); }
    SB0();
  });
  FINE(27); FINE_OUT(28);
  STAMP(0, 9);
  wait_v<4>(); PIN(fpoll);
  if (!all_eq(fpoll, X.tag)) spin_flag(P.ws, f1_off(X), X.tag);
  first_z(P, X, smem);
  lane = lane_id();
  const unsigned qo = ((lane >> 4) & 1) * 18432 + (lane >> 5) * 128 + (lane & 15) * 8;
  int nst = 0;
#pragma unroll
  for (int t = 0; t < Q::nt(2); ++t)
#pragma unroll
    for (int a = 0; a < 4; ++a)
      if ((Q::t0(2) + t) * 8 + 2 * a < 36)
        ST8(ws, qo + ((Q::t0(2) + t) * 8 + 2 * a) * 128, WS_PQ + (p * 8 + rw * 2) * 18432 + quad * 4608,
            ((u32x2){cvt_pk(acc[t][4 * a], acc[t][4 * a + 1]), cvt_pk(acc[t][4 * a + 2], acc[t][4 * a + 3])}));
  wait_v<(G ? 6 : 12)>();
}

template <int ROLE>
DI void bc_loader(const Params& P, const Ctx& X, LAS char* smem) {
  constexpr int CNT = ROLE ? 16 : 18;
  __builtin_amdgcn_s_setprio(3);
  Loader<ROLE> L; L.init(P, X, smem);
  const int lane = lane_id(), p = X.p, c = X.c;
  BAR(); L.issue_b(2);
  BAR(); L.issue_b(3);
#pragma unroll 1
  for (int n = 2; n < 30; ++n) { wait_v<CNT>(); BAR(); L.issue_b(n + 2); }
  wait_v<CNT>(); BAR(); wait_v<0>(); BAR();
  if (ROLE == 0) spin_flag(P.ws, WS_F2Z + ((c & 7) * 2 + (lane >> 5)) * 128 + (16 + ((lane & 31) < 12 ? lane & 31 : 0)) * 4, X.tag);
  else spin_flag(P.ws, WS_F3O + p * 128 + (lane & 15) * 4, X.tag);
  L.template issue_c<2>(32); L.template issue_c<4>(33); L.template issue_c<4>(34);
  int n = 32;
  if (ROLE == 1) {
    wait_v<2 * CNT>(); BAR(); L.template issue_c<4>(35);
    wait_v<2 * CNT>(); BAR(); L.template issue_c<4>(36);
    wait_v<2 * CNT>(); BAR(); wait_v<CNT>(); BAR(); wait_v<0>(); BAR();
    STAMP(2, 5);
    spin_flag(P.ws, WS_F4 + (2 * p + (c >> 3)) * 128 + (lane & 31) * 4, X.tag);
    STAMP(2, 6);
    L.template issue_c<4>(37); L.template issue_c<4>(38); L.template issue_c<4>(39);
    n = 37;
  }
#pragma unroll 1
  for (; n < 52; ++n) { wait_v<2 * CNT>(); BAR(); L.template issue_c<4>(n + 3); }
  wait_v<2 * CNT>(); BAR(); wait_v<CNT>(); BAR(); wait_v<0>(); BAR();
}

DI void bc_side(const Params& P, const Ctx& X, LAS char* smem) {
  __builtin_amdgcn_s_setprio(2);
  const brs_t ws = __builtin_amdgcn_make_buffer_rsrc(P.ws, 0, WS_END, 0x00020000);
  const int lane = lane_id();
  const int sw = X.wid - 6, lt = sw * 64 + lane, p = X.p, c = X.c, kq = lane >> 4, r16 = lane & 15;
  u32x4 part[3]; u32x2 qv[4][2], qx; int dst[6], src[6]; bf16x8 kvf[4][2]; float mine = 0.f;
  const unsigned oob = 0x7fffff00u;
  const unsigned f2k = WS_F2K + c * 128 + r16 * 4, f2z = WS_F2Z + p * 128 + (kq < 3 ? f1_src(c, kq) : 12 + (c >> 2)) * 4;
  unsigned fl = 0;
  auto kvf_ld = [&]() {
#pragma unroll
    for (int i = 0; i < 3; ++i) part[i] = LD16(ws, (lt + 128 * i < 352 ? lt + 128 * i : 351) * 16, WS_KVF + c * 5632);
  };
  auto phi2 = [&](u32x2& v) {
    v = (u32x2){cvt_pk(phi_f(bf_lo(v.x)), phi_f(bf_hi(v.x))), cvt_pk(phi_f(bf_lo(v.y)), phi_f(bf_hi(v.y)))};
    PIN(v);
  };
  sfor<0, 32>([&](auto N_) {
    constexpr int N = decltype(N_)::value;
    if constexpr (N == 4) wait_v<6>();
    if constexpr (N == 26) wait_v<0>();
    if constexpr (N == 9 || N == 15) WAIT_L0();
    BAR();
    if constexpr (N == 22) { if (sw == 1) warm_code(X, smem); }
    if constexpr (N == 1) fl = flag_ld(P.ws, f2k);
    if constexpr (N < 3) {
#pragma unroll
      for (int i = 2 * N; i < 2 * N + 2; ++i) {
        const int l = (lane & 3) + 4 * i, id0 = p * 44 + sw * 22 + (l < 22 ? l : 21), id = id0 < 684 ? id0 : 683;
        const int hh = id >= 342, rem = id - hh * 342, e = (rem * 3641) >> 16, d = rem - e * 18;
        const int kc = 36 * (c & 3) + 18 * hh + d, vc = kc - d + e;
        src[i] = (lane >> 2) * 36864 + (kc >> 4) * 4096 + (e == 18 ? 0 : ((vc >> 4) - (kc >> 4) + 2) * 1024 + (vc & 15) * 16) + ((kc >> 2) & 3) * 256 + (kc & 3) * 4;
        PIN(src[i]);
      }
    }
    if constexpr (N == 4 || N == 5) {
      const int r = sw * 2 + N - 4, kb = ((X.pa & 1) * 12 + X.quad * 3 + X.jj) * 4 + r;
      {
        LAS char* a = smem + B_KV + r * 5120 + (8 * kq + ((lane >> 2) & 3)) * 144;
        const unsigned so = (X.jj < 3 && kb < 90) ? WS_WX + ((X.pa >> 1) * 90 + kb) * 4608 : 0x7ff00000u;
#pragma unroll
        for (int dt = 0; dt < 4; ++dt) {
          LAS char* b = a + (dt >> 1) * 64 + (lane & 3) * 16 + (dt & 1) * 8;
          ST16(ws, lane * 16 + dt * 1024, so, __builtin_bit_cast(u32x4, tr_frag(b, b + 576)));
        }
        LAS char* b = a + 128 + (lane & 1) * 8;
        ST16(ws, r16 < 8 ? 4096 + kq * 128 + r16 * 16 : 0x7ff00000u, so, __builtin_bit_cast(u32x4, tr_frag(b, b + 576)));
      }
    }
    if constexpr (N == 8) {
#pragma unroll
      for (int i = 0; i < 4; ++i) *(LAS u32x4*)(smem + B_KV + i * 2048 + lt * 16) = (u32x4){0, 0, 0, 0};
    }
    if constexpr (N == 7 || N == 8 || N == 11) {
      constexpr int e0 = N == 11 ? 4 : 2 * (N - 7);
#pragma unroll
      for (int e6 = e0; e6 < e0 + 2; ++e6) {
        const int id = (lt + 128 * (e6 >> 1)) * 2 + (e6 & 1), hh = id >= 342, rem = id - hh * 342, e = (rem * 3641) >> 16, d = rem - e * 18;
        const int k = 18 * hh + d, n = 18 * hh + e;
        const int kof = (k >> 5) * 1024 + ((k >> 3) & 3) * 256 + (k & 7) * 2;
        dst[e6] = id >= 684 ? 8192 + lane * 2 : (e < 18 ? (n >> 4) * 2048 + (n & 15) * 16 : 6144 + hh * 16) + kof;
        PIN(dst[e6]);
      }
    }
    if constexpr (N == 4) {
      ST4(ws, lt == 0 ? 0u : oob, WS_F2Z + X.pa * 128 + (X.jj < 3 ? X.quad * 3 + X.jj : 12 + X.quad) * 4, X.tag);
    }
    if constexpr (N == 7) fl = flag_ld(P.ws, f2z);
    if constexpr (N == 3) {
      if (!all_eq(fl, X.tag)) spin_flag(P.ws, f2k, X.tag);
#pragma unroll
      for (int i = 0; i < 6; ++i) src[i] = __builtin_amdgcn_raw_buffer_load_b32(ws, src[i], WS_KVP + (c >> 2) * 16 * 36864, 16);
    }
    if constexpr (N == 6) {
#pragma unroll
      for (int i = 0; i < 6; ++i) {
        float v = __int_as_float(src[i]);
        v += dpp_get<0x124>(v); v += dpp_get<0x128>(v);
        auto a = __builtin_amdgcn_permlane16_swap(__float_as_uint(v), __float_as_uint(v), false, false);
        v = __uint_as_float(a[0]) + __uint_as_float(a[1]);
        auto b = __builtin_amdgcn_permlane32_swap(__float_as_uint(v), __float_as_uint(v), false, false);
        v = __uint_as_float(b[0]) + __uint_as_float(b[1]);
        mine = (lane >> 2) == i ? v : mine;
      }
      ST8(ws, lane < 22 ? (p * 44 + sw * 22 + lane) * 8 : oob, WS_KVF + c * 5632, ((u32x2){__float_as_uint(mine), X.tag}));
    }
    if constexpr (N == 9) {
      if (!all_eq(fl, X.tag)) spin_flag(P.ws, f2z, X.tag);
      qx = LD8(ws, kq * 18432 + 1024 + r16 * 8, WS_PQ + (p * 8 + sw * 4) * 18432 + c * 1152);
    }
    if constexpr (N == 9 || N == 10) {
#pragma unroll
      for (int tt = 2 * (N - 9); tt < 2 * (N - 8); ++tt) {
        const unsigned so = WS_PQ + (p * 8 + sw * 4 + tt) * 18432 + c * 1152;
        qv[tt][0] = LD8(ws, kq * 256 + r16 * 8, so); qv[tt][1] = LD8(ws, kq * 256 + 128 + r16 * 8, so);
      }
    }
    if constexpr (N == 11) kvf_ld();
    if constexpr (N == 12) { phi2(qx); phi2(qv[0][0]); }
    if constexpr (N == 13) { phi2(qv[0][1]); phi2(qv[1][0]); }
    if constexpr (N == 15) phi2(qv[1][1]);
    if constexpr (N == 16 || N == 17) { phi2(qv[N - 14][0]); phi2(qv[N - 14][1]); }
    if constexpr (N == 14) {
#pragma unroll 1
      for (unsigned spins = 0; spins < SPIN_MAX; ++spins) {
        bool ok = true;
#pragma unroll
        for (int i = 0; i < 3; ++i) ok &= part[i].y == X.tag && part[i].w == X.tag;
        if (__builtin_amdgcn_ballot_w64(!ok) == 0) break;
        __builtin_amdgcn_s_sleep(1);
        asm volatile("" ::: "memory");
        kvf_ld();
      }
#pragma unroll
      for (int e6 = 0; e6 < 6; ++e6) {
        const unsigned u = part[e6 >> 1][(e6 & 1) * 2];
        *(LAS u16*)(smem + B_KV + dst[e6]) = (u16)(cvt_pk(__uint_as_float(u), 0.f) & 0xffffu);
      }
    }
    if constexpr (N == 15) {
#pragma unroll
      for (int nt = 0; nt < 4; ++nt) {
        LAS char* kb = smem + B_KV + nt * 2048 + (nt < 3 ? r16 : r16 & 1) * 16;
        kvf[nt][0] = *(LAS bf16x8*)(kb + kq * 256); kvf[nt][1] = *(LAS bf16x8*)(kb + 1024);
      }
    }
    if constexpr (N >= 18 && N <= 21) {
      constexpr int tt = N - 18;
      const bf16x8 q0 = __builtin_bit_cast(bf16x8, ((u32x4){qv[tt][0].x, qv[tt][0].y, qv[tt][1].x, qv[tt][1].y}));
      const bf16x8 q1 = __builtin_bit_cast(bf16x8, ((u32x4){kq == tt ? qx.x : 0u, kq == tt ? qx.y : 0u, 0u, 0u}));
      const f32x4 z = {0, 0, 0, 0};
      const f32x4 den = MFMA(kvf[3][1], q1, MFMA(kvf[3][0], q0, z));
      f32x4 num[3];
#pragma unroll
      for (int nt = 0; nt < 3; ++nt) num[nt] = MFMA(kvf[nt][1], q1, MFMA(kvf[nt][0], q0, z));
      const float r0 = __builtin_amdgcn_rcpf(den[0] + 1e-6f), r1 = __builtin_amdgcn_rcpf(den[1] + 1e-6f), rm = kq == 0 ? r0 : r1;
      const unsigned so = WS_HX + (p * 8 + sw * 4 + tt) * (90 * 1024);
      ST16(ws, lane * 16, so + (72 + c) * 1024, ((u32x4){cvt_pk(num[0][0] * r0, num[0][1] * r0), cvt_pk(num[0][2] * r0, num[0][3] * r0),
                                                         cvt_pk(num[1][0] * rm, num[1][1] * rm), cvt_pk(num[1][2] * r1, num[1][3] * r1)}));
      ST8(ws, kq == 0 ? r16 * 16 : oob, so + (88 + (c >> 3)) * 1024 + ((c & 7) >> 1) * 256 + (c & 1) * 8,
          ((u32x2){cvt_pk(num[2][0] * r1, num[2][1] * r1), cvt_pk(num[2][2] * r1, num[2][3] * r1)}));
    }
    if constexpr (N == 26) {
      if (lt == 0) flag_st(P.ws, WS_F3O + p * 128 + c * 4, X.tag);
      if (lt == 64 && X.jj < 3) flag_st(P.ws, WS_F2Z + X.pa * 128 + (16 + X.quad * 3 + X.jj) * 4, X.tag);
    }
    SB0();
  });
  if (X.bid == 0 && sw == 0) {
#pragma unroll 1
    for (int n = 32; n < 42; ++n) BAR();
    unsigned f4[4];
#pragma unroll
    for (int i = 0; i < 4; ++i) f4[i] = flag_ld(P.ws, WS_F3O + (i * 4 + kq) * 128 + r16 * 4);
    BAR(); BAR(); BAR();
    if (!(all_eq(f4[0], X.tag) && all_eq(f4[1], X.tag) && all_eq(f4[2], X.tag) && all_eq(f4[3], X.tag))) {
#pragma unroll 1
      for (int i = 0; i < 4; ++i) spin_flag(P.ws, WS_F3O + (i * 4 + kq) * 128 + r16 * 4, X.tag);
    }
    if (lane == 0) flag_st(P.ws, WS_EPOCH, X.tag);
  }
}

template <int DH>
DI void c_compute(const Params& P, const Ctx& X, LAS char* smem) {
  constexpr int ND = DH ? 2 : 3;
  int lane = lane_id();
  const int r16 = lane & 15, kq = lane >> 4, th = X.wid & 1;
  const int rg = 2 * X.p + (X.c >> 3), cg = X.c & 7;
  f32x4 acc[ND][2];
#pragma unroll
  for (int a = 0; a < ND; ++a) { acc[a][0] = (f32x4){0, 0, 0, 0}; acc[a][1] = (f32x4){0, 0, 0, 0}; }
  const int woff = DH * 2048 + lane * 16, woff4 = 4096 + kq * 128 + (r16 & 7) * 16;
  const int hoff = 18432 + th * 8192 + lane * 16;
  bf16x8 wa[2][ND], ha[2][2], wb[2][ND], hb[2][2];
#define C_RD(W, H, slot, kp) do { LAS char* b_ = smem + SLOT(slot); \
    _Pragma("unroll") for (int k = 0; k < 2; ++k) { \
      H[k][0] = *(LAS bf16x8*)(b_ + hoff + ((kp) * 2 + k) * 1024); H[k][1] = *(LAS bf16x8*)(b_ + hoff + 4096 + ((kp) * 2 + k) * 1024); \
      W[k][0] = *(LAS bf16x8*)(b_ + woff + ((kp) * 2 + k) * 4608); W[k][1] = *(LAS bf16x8*)(b_ + woff + 1024 + ((kp) * 2 + k) * 4608); \
      if (ND == 3) W[k][ND - 1] = *(LAS bf16x8*)(b_ + woff4 + ((kp) * 2 + k) * 4608); } } while (0)
#define C_MM(W, H) do { _Pragma("unroll") for (int k = 0; k < 2; ++k) _Pragma("unroll") for (int i = 0; i < ND; ++i) { \
    acc[i][0] = MFMA(W[k][i], H[k][0], acc[i][0]); acc[i][1] = MFMA(W[k][i], H[k][1], acc[i][1]); } } while (0)
#define C_ILV() do { _Pragma("unroll") for (int i_ = 0; i_ < ND + 2; ++i_) { SGB(0x100, 2); SGB(0x8, 1); } SGB(0x8, 3 * ND - 2); } while (0)
#define C_STAGE(n) do { WAIT_L0(); BAR(); const int slot = (n) & 3; \
    C_RD(wa, ha, slot, 0); C_MM(wb, hb); C_ILV(); SB0(); \
    C_RD(wb, hb, slot, 1); C_MM(wa, ha); C_ILV(); SB0(); } while (0)
  BAR();
  C_RD(wb, hb, 0, 0);
  C_STAGE(33);
  wait_v<0>();
  if (lane == 0) flag_st(P.ws, WS_F4 + (X.p * 2 + (X.wid >> 1)) * 128 + (X.c * 2 + (X.wid & 1)) * 4, X.tag);
  STAMP(2, 1);
  u32x4 xr[2]; u32x2 xo[2]; size_t orow[2];
#pragma unroll
  for (int tt = 0; tt < 2; ++tt) {
    orow[tt] = (size_t)(rg * 64 + slot_row(th * 32 + tt * 16 + r16)) * 1152 + cg * 144;
    xr[tt] = *(const u32x4*)((const char*)P.x + orow[tt] + DH * 64 + kq * 16);
    if (DH == 0) xo[tt] = *(const u32x2*)((const char*)P.x + orow[tt] + 128 + (kq & 1) * 8);
  }
  C_STAGE(34);
#pragma unroll 1
  for (int n = 35; n < 55; n += 4) { C_STAGE(3); C_STAGE(0); C_STAGE(1); C_STAGE(2); }
  C_MM(wb, hb);
  SB0();
  STAMP(2, 2);
  const brs_t ro = __builtin_amdgcn_make_buffer_rsrc(P.out, 0, 2048 * 1152, 0x00020000);
#pragma unroll
  for (int tt = 0; tt < 2; ++tt) {
    const u32x4 x = xr[tt]; const f32x4 a0 = acc[0][tt], a1 = acc[1][tt];
    u32x4 v;
    v.x = cvt_pk(bf_lo(x.x) + a0[0], bf_hi(x.x) + a0[1]); v.y = cvt_pk(bf_lo(x.y) + a0[2], bf_hi(x.y) + a0[3]);
    v.z = cvt_pk(bf_lo(x.z) + a1[0], bf_hi(x.z) + a1[1]); v.w = cvt_pk(bf_lo(x.w) + a1[2], bf_hi(x.w) + a1[3]);
    __builtin_amdgcn_raw_buffer_store_b128(v, ro, (int)(orow[tt] + DH * 64 + kq * 16), 0, OUTAUX);
    if (DH == 0) {
      const f32x4 a2 = acc[ND - 1][tt];
      u32x2 w; w.x = cvt_pk(bf_lo(xo[tt].x) + a2[0], bf_hi(xo[tt].x) + a2[1]); w.y = cvt_pk(bf_lo(xo[tt].y) + a2[2], bf_hi(xo[tt].y) + a2[3]);
      __builtin_amdgcn_raw_buffer_store_b64(w, ro, kq < 2 ? (int)(orow[tt] + 128 + kq * 8) : 0x7fffff00, 0, OUTAUX);
    }
  }
  STAMP(2, 3);
}

DI void b_compute(const Params& P, const Ctx& X, LAS char* smem) {
  int lane = lane_id();
  const int wid = X.wid, p = X.p, c = X.c, r16 = lane & 15, kq = lane >> 4;
  const brs_t ws = __builtin_amdgcn_make_buffer_rsrc(P.ws, 0, WS_END, 0x00020000);
  f32x4 acc[9][2];
#pragma unroll
  for (int a = 0; a < 9; ++a) { acc[a][0] = (f32x4){0, 0, 0, 0}; acc[a][1] = (f32x4){0, 0, 0, 0}; }
  const int zoff = lane * 16;
  const int woff0 = 18432 + (wid * 32 + r16) * 128 + ((kq ^ (r16 >> 1)) * 16);
  const int woff1 = 18432 + (wid * 32 + r16) * 128 + (((kq + 4) ^ (r16 >> 1)) * 16);
  bf16x8 za[9], wa[2], zb[9], wb[2];
#define B_RD(Z, W, slot, kb) do { LAS char* b_ = smem + SLOT(slot); \
    W[0] = *(LAS bf16x8*)(b_ + ((kb) ? woff1 : woff0)); W[1] = *(LAS bf16x8*)(b_ + ((kb) ? woff1 : woff0) + 2048); \
    _Pragma("unroll") for (int i = 0; i < 9; ++i) Z[i] = *(LAS bf16x8*)(b_ + zoff + (i < 8 ? (kb) * 8192 + i * 1024 : 16384 + (kb) * 1024)); } while (0)
#define B_MM(Z, W) do { _Pragma("unroll") for (int i = 0; i < 9; ++i) { \
    acc[i][i & 1] = MFMA(Z[i], W[i & 1], acc[i][i & 1]); acc[i][(i & 1) ^ 1] = MFMA(Z[i], W[(i & 1) ^ 1], acc[i][(i & 1) ^ 1]); } } while (0)
#define B_ILV() do { _Pragma("unroll") for (int i_ = 0; i_ < 11; ++i_) { SGB(0x8, 1); SGB(0x100, 1); } SGB(0x8, 7); } while (0)
#define B_STAGE(n) do { WAIT_L0(); BAR(); const int slot = (n) & 3; \
    B_RD(za, wa, slot, 0); B_MM(zb, wb); B_ILV(); SB0(); \
    B_RD(zb, wb, slot, 1); B_MM(za, wa); B_ILV(); SB0(); } while (0)
  BAR();
  STAMP(1, 1);
  B_RD(za, wa, 0, 0); SB0();
  B_RD(zb, wb, 0, 1); B_MM(za, wa); B_ILV(); SB0();
  B_STAGE(1);
#pragma unroll 1
  for (int n = 2; n < 18; n += 4) {
    B_STAGE(2); B_STAGE(3);
    if (n == 2) wait_v<0>();
    B_STAGE(0); B_STAGE(1);
  }
  u32x4 z1[2][4]; u32x2 z1o[2]; u16 bsv[2];
  sfor<18, 32>([&](auto N_) {
    constexpr int N = decltype(N_)::value, tt = (N - 18) / 6, m = (N - 18) % 6;
    if constexpr (N < 30) {
      const unsigned so = WS_Z1 + (p * 8 + wid * 2 + tt) * (72 * 1024);
      if constexpr (m < 4) z1[tt][m] = LD16(ws, zoff + m * 1024, so + c * 4096);
      else if constexpr (m == 4) z1o[tt] = LD8(ws, zoff, so + (64 + (c >> 1)) * 1024 + (c & 1) * 8);
      else bsv[tt] = P.bsp[p * 128 + slot_row(wid * 32 + tt * 16 + r16)];
      SB0();
    }
    B_STAGE(N);
  });
  B_MM(zb, wb);
  SB0();
  STAMP(1, 2);
#pragma unroll
  for (int tt = 0; tt < 2; ++tt) {
    const float b = bf_lo(bsv[tt]) * (1.0f / GC);
    const unsigned so = WS_HX + (p * 8 + wid * 2 + tt) * (90 * 1024);
#pragma unroll
    for (int m = 0; m < 4; ++m) {
      const u32x4 z = z1[tt][m]; const f32x4 g0 = acc[2 * m][tt] * (1.0f / GC) + b, g1 = acc[2 * m + 1][tt] * (1.0f / GC) + b;
      u32x4 v;
      v.x = cvt_pk(bf_lo(z.x) * g0[0], bf_hi(z.x) * g0[1]); v.y = cvt_pk(bf_lo(z.y) * g0[2], bf_hi(z.y) * g0[3]);
      v.z = cvt_pk(bf_lo(z.z) * g1[0], bf_hi(z.z) * g1[1]); v.w = cvt_pk(bf_lo(z.w) * g1[2], bf_hi(z.w) * g1[3]);
      ST16(ws, lane * 16 + m * 1024, so + c * 4096, v);
    }
    const u32x2 z = z1o[tt]; const f32x4 g0 = acc[8][tt] * (1.0f / GC) + b;
    u32x2 v; v.x = cvt_pk(bf_lo(z.x) * g0[0], bf_hi(z.x) * g0[1]); v.y = cvt_pk(bf_lo(z.y) * g0[2], bf_hi(z.y) * g0[3]);
    ST8(ws, (kq >> 1) * 256 + r16 * 16 + (kq & 1) * 8, so + (64 + (c >> 1)) * 1024 + (c & 1) * 512, v);
  }
  STAMP(1, 3);
}

template <int TY>
DI void a_prologue(const Params& P, const Ctx& X, LAS char* smem, bf16x8 (&xf)[36], LAS char* (&xb)[4]) {
  const int wid = X.wid, lane = lane_id(), idx = X.bid >> 3;
  const int sl0 = ((wid + 2 * X.jj) & 7) * 16 + (lane >> 3);
  u32x4 xr[2][9];
#pragma unroll
  for (int ln = 0; ln < 9; ++ln)
#pragma unroll
    for (int hh = 0; hh < 2; ++hh)
      xr[hh][ln] = *(const u32x4*)((const char*)P.x + (size_t)(X.pa * 128 + slot_row(sl0 + hh * 8)) * 1152 + (lane & 7) * 16 + ln * 128);
  SB0();
  unsigned rlo[2], rhi[2]; float sh_[2];
  {
    const bf16x2_t one = {(__bf16)1.0f, (__bf16)1.0f};
    float s[2] = {0.f, 0.f}, q[2] = {0.f, 0.f};
#pragma unroll
    for (int ln = 0; ln < 9; ++ln)
#pragma unroll
      for (int i = 0; i < 4; ++i)
#pragma unroll
        for (int hh = 0; hh < 2; ++hh) {
          const unsigned ua = xr[hh][ln][i];
          const bf16x2_t a = __builtin_bit_cast(bf16x2_t, ua);
          s[hh] = __builtin_amdgcn_fdot2_f32_bf16(a, one, s[hh], false); q[hh] = __builtin_amdgcn_fdot2_f32_bf16(a, a, q[hh], false);
        }
#pragma unroll
    for (int hh = 0; hh < 2; ++hh) {
      const float m = oct_sum(s[hh]) * (1.0f / 576.0f);
      const float var = __builtin_fmaxf(oct_sum(q[hh]) * (1.0f / 576.0f) - m * m, 0.f);
      unsigned r = cvt_pk(__builtin_amdgcn_rsqf(var + 1e-5f) * (TY == 0 ? GC : 1.0f), 0.f);
      sh_[hh] = -m * bf_lo(r);
      unsigned r2 = r << 16;
      PIN(r); PIN(r2); PIN(sh_[hh]);
      rlo[hh] = r; rhi[hh] = r2;
    }
  }
  STAMP(0, 1);
  if (wid == 3) warm_code(X, smem);
  SB0();
#pragma unroll
  for (int i = 0; i < 4; ++i) xb[i] = smem + A_XN + ((wid & 3) * 32 + (lane & 31)) * 128 + (((2 * i + (lane >> 5)) ^ ((lane >> 1) & 7)) * 16);
#pragma unroll
  for (int ln = 0; ln < 9; ++ln) {
    if (ln == 4) {
      WAIT_L0(); BAR();
#pragma unroll
      for (int ks = 0; ks < 16; ++ks) xf[ks] = XRD(ks);
      SB0();
    }
    if (ln == 6) {
      asm volatile("s_waitcnt lgkmcnt(4)" ::: "memory"); BAR();
      if (TY == 0) {
        const rsrc_t rsw = MKRS(P.win, 576 * 9216);
        const unsigned voff = z_voff(wid, lane);
#pragma unroll
        for (int i = 0; i < 9; ++i) BLDS(rsw, voff, (2304 + (X.quad * 3 + X.jj) * 192) * 2 + i * 64 * 9216, smem + i * 8192 + wid * 1024);
      } else q_first<TY == 2>(P, X, smem);
      SB0();
    }
#pragma unroll
    for (int hh = 0; hh < 2; ++hh) {
      const u32x4 v = norm4(xr[hh][ln], rlo[hh], rhi[hh], sh_[hh]);
      const int sl = sl0 + hh * 8;
      *(LAS u32x4*)(smem + A_XN + ln * 16384 + sl * 128 + (((lane & 7) ^ ((sl >> 1) & 7)) * 16)) = v;
      SB0();
    }
  }
  STAMP(0, 2);
  {
    const int r0 = ((idx >> 2) & 7) * 72, t = wid * 64 + lane, col = (t * 911) >> 16, row = t - col * 72;
    if (TY == 0) TOUCH(MKRS(P.win, 576 * 9216), col < 5 ? row * 9216 + (col < 2 ? 4736 + col * 128 : (col - 2) * 128) : 0x7ff00000u,
                       r0 * 9216 + (X.quad * 3 + X.jj) * 384, smem + L_SCR);
    else {
      TOUCH(MKRS(P.wqkv, 576 * 3456), col < 6 ? row * 3456 + 1152 + (col < 3 ? col * 128 : 1152 + (col - 3) * 128) : 0x7ff00000u,
            r0 * 3456 + X.quad * 256, smem + L_SCR);
      TOUCH(MKRS(P.wqkv, 576 * 3456), col < 3 ? row * 3456 + col * 128 : 0x7ff00000u, r0 * 3456 + X.quad * 256, smem + L_SCR);
    }
  }
  wait_v<(TY == 0 ? 7 : TY == 1 ? 8 : 6)>();
  WAIT_L0(); BAR();
  STAMP(0, 3);
}

__global__ NOPK __launch_bounds__(512, 2) void kF(const Params P) {
  extern __shared__ __attribute__((aligned(16))) char smem_[];
  LAS char* smem = (LAS char*)smem_;
  Ctx X;
  X.wid = __builtin_amdgcn_readfirstlane(threadIdx.x >> 6); X.bid = blockIdx.x;
  const int xcd = X.bid & 7, idx = X.bid >> 3, wid = X.wid, lane = threadIdx.x & 63;
  X.pa = (xcd >> 2) * 8 + (idx >> 2); X.quad = xcd & 3; X.jj = idx & 3;
  X.p = (xcd >> 1) * 4 + (idx >> 3); X.c = (xcd & 1) * 8 + (idx & 7);
  if (lane < 18) *(volatile LAS unsigned*)(smem + 159 * 1024 + 256 + X.wid * 80 + lane * 4) = 0;
  STAMP(0, 0);
  X.tag = *(const unsigned*)(P.ws + WS_EPOCH) + 1u;
  if (X.jj < 3) { bf16x8 xf[36]; LAS char* xb[4]; a_prologue<0>(P, X, smem, xf, xb); a_zblock(P, X, smem, xf, xb); }
  else if (wid < 4) { bf16x8 xf[36]; LAS char* xb[4]; a_prologue<1>(P, X, smem, xf, xb); a_qkvblock<0>(P, X, smem, xf, xb); }
  else { bf16x8 xf[36]; LAS char* xb[4]; a_prologue<2>(P, X, smem, xf, xb); a_qkvblock<1>(P, X, smem, xf, xb); }
  STAMP(0, 11);
  if (wid == 4) { bc_loader<0>(P, X, smem); STAMP_OUT(); __builtin_amdgcn_endpgm(); }
  if (wid == 5) { bc_loader<1>(P, X, smem); STAMP_OUT(); __builtin_amdgcn_endpgm(); }
  if (wid >= 6) { bc_side(P, X, smem); STAMP_OUT(); __builtin_amdgcn_endpgm(); }
  b_compute(P, X, smem);
  if (wid < 2) { c_compute<0>(P, X, smem); STAMP_OUT(); __builtin_amdgcn_endpgm(); }
  c_compute<1>(P, X, smem);
  STAMP_OUT();
}

extern "C" __device__ __attribute__((used, noinline)) void kf_code_end() {}

extern "C" void kernel_launch(void* const* d_in, const int* in_sizes, int n_in, void* d_out, int out_size, void* d_ws,
                              size_t ws_size, hipStream_t stream) {
  Params P;
  P.x = (const u16*)d_in[0]; P.wqkv = (const u16*)d_in[1]; P.wo = (const u16*)d_in[2]; P.win = (const u16*)d_in[4];
  P.wsp = (const u16*)d_in[5]; P.bsp = (const u16*)d_in[6]; P.wout = (const u16*)d_in[7];
  P.out = (u16*)d_out; P.ws = (char*)d_ws;
  (void)hipFuncSetAttribute((const void*)kF, hipFuncAttributeMaxDynamicSharedMemorySize, LDS_BYTES);
  kF<<<256, 512, LDS_BYTES, stream>>>(P);
}
```

```cpp
#include <hip/hip_runtime.h>
#include <hip/hip_bf16.h>

#if defined(__HIP_DEVICE_COMPILE__)
#define NOPK __attribute__((target("no-packed-fp32-ops")))
#else
#define NOPK
#endif
#define LAS __attribute__((address_space(3)))
#define GAS __attribute__((address_space(1)))
typedef unsigned short u16;
typedef unsigned long long u64;
typedef short bf16x8 __attribute__((ext_vector_type(8)));
typedef short s16x4 __attribute__((ext_vector_type(4)));
typedef float f32x4 __attribute__((ext_vector_type(4)));
typedef float f32x2 __attribute__((ext_vector_type(2)));
typedef float f32x16 __attribute__((ext_vector_type(16)));
typedef unsigned u32x4 __attribute__((ext_vector_type(4)));
typedef unsigned u32x2 __attribute__((ext_vector_type(2)));
typedef __bf16 bf16x2_t __attribute__((ext_vector_type(2)));
typedef __amdgpu_buffer_rsrc_t brs_t;
typedef u32x4 rsrc_t;

#define WAIT_L0() asm volatile("s_waitcnt lgkmcnt(0)" ::: "memory")
#define BAR() do { asm volatile("" ::: "memory"); __builtin_amdgcn_s_barrier(); asm volatile("" ::: "memory"); } while (0)
#define SB0() __builtin_amdgcn_sched_barrier(0)
#define SGB(mask, n) __builtin_amdgcn_sched_group_barrier(mask, n, 0)
#define PIN(x) asm volatile("" : "+v"(x))
#define MFMA(a, b, c) __builtin_amdgcn_mfma_f32_16x16x32_bf16(a, b, c, 0, 0, 0)
#define MFMA32(a, b, c) __builtin_amdgcn_mfma_f32_32x32x16_bf16(a, b, c, 0, 0, 0)
#define DI __device__ __forceinline__
template <int N> DI void wait_v() { asm volatile("s_waitcnt vmcnt(%0)" :: "i"(N) : "memory"); }
template <int V> struct IC { static constexpr int value = V; };
template <int I, int N, class F> DI void sfor(F&& f) {
  if constexpr (I < N) { f(IC<I>{}); sfor<I + 1, N>(f); }
}

DI rsrc_t MKRS(const void* p, unsigned bytes) {
  const u64 a = (u64)p;
  return (rsrc_t){(unsigned)a, (unsigned)(a >> 32) & 0xffffu, bytes, 0x00020000u};
}
DI void BLDS(rsrc_t rs, unsigned voff, unsigned soff, LAS char* l) {
  asm volatile("s_mov_b32 m0, %3\n\ts_nop 0\n\tbuffer_load_dwordx4 %0, %1, %2 offen lds"
               :: "v"(voff), "s"(rs), "s"(soff), "s"((unsigned)(size_t)l) : "memory");
}
DI void BLDS1(rsrc_t rs, unsigned voff, unsigned soff, LAS char* l) {
  asm volatile("s_mov_b32 m0, %3\n\ts_nop 0\n\tbuffer_load_dwordx4 %0, %1, %2 offen sc1 lds"
               :: "v"(voff), "s"(rs), "s"(soff), "s"((unsigned)(size_t)l) : "memory");
}
template <int N> DI void BLDSx(rsrc_t rs, unsigned voff, unsigned soff, LAS char* l) {
  if (N == 4)
    asm volatile("s_mov_b32 m0, %3\n\ts_nop 0\n\tbuffer_load_dwordx4 %0, %1, %2 offen sc1 lds\n\tbuffer_load_dwordx4 %0, %1, %2 offen offset:1024 sc1 lds\n\t"
                 "buffer_load_dwordx4 %0, %1, %2 offen offset:2048 sc1 lds\n\tbuffer_load_dwordx4 %0, %1, %2 offen offset:3072 sc1 lds"
                 :: "v"(voff), "s"(rs), "s"(soff), "s"((unsigned)(size_t)l) : "memory");
  else if (N == 2)
    asm volatile("s_mov_b32 m0, %3\n\ts_nop 0\n\tbuffer_load_dwordx4 %0, %1, %2 offen sc1 lds\n\tbuffer_load_dwordx4 %0, %1, %2 offen offset:1024 sc1 lds"
                 :: "v"(voff), "s"(rs), "s"(soff), "s"((unsigned)(size_t)l) : "memory");
  else
    asm volatile("s_mov_b32 m0, %3\n\ts_nop 0\n\tbuffer_load_dwordx4 %0, %1, %2 offen sc1 lds"
                 :: "v"(voff), "s"(rs), "s"(soff), "s"((unsigned)(size_t)l) : "memory");
}
DI void BLDS4v(rsrc_t rs, const unsigned (&v)[4], unsigned soff, LAS char* l) {
  asm volatile("s_mov_b32 m0, %6\n\ts_nop 0\n\tbuffer_load_dwordx4 %0, %4, %5 offen lds\n\tbuffer_load_dwordx4 %1, %4, %5 offen offset:1024 lds\n\t"
               "buffer_load_dwordx4 %2, %4, %5 offen offset:2048 lds\n\tbuffer_load_dwordx4 %3, %4, %5 offen offset:3072 lds"
               :: "v"(v[0]), "v"(v[1]), "v"(v[2]), "v"(v[3]), "s"(rs), "s"(soff), "s"((unsigned)(size_t)l) : "memory");
}
DI void BLDS32(rsrc_t rs, unsigned voff, unsigned soff, LAS char* l) {
  asm volatile("s_mov_b32 m0, %3\n\ts_mov_b32 exec_hi, 0\n\tbuffer_load_dwordx4 %0, %1, %2 offen lds\n\ts_mov_b32 exec_hi, -1"
               :: "v"(voff), "s"(rs), "s"(soff), "s"((unsigned)(size_t)l) : "memory");
}
DI void BLDS3v(rsrc_t rs, const unsigned (&v)[3], unsigned soff, LAS char* l) {
  asm volatile("s_mov_b32 m0, %5\n\ts_nop 0\n\tbuffer_load_dwordx4 %0, %3, %4 offen lds\n\tbuffer_load_dwordx4 %1, %3, %4 offen offset:1024 lds\n\t"
               "buffer_load_dwordx4 %2, %3, %4 offen offset:2048 lds"
               :: "v"(v[0]), "v"(v[1]), "v"(v[2]), "s"(rs), "s"(soff), "s"((unsigned)(size_t)l) : "memory");
}
DI void BLDS15v(rsrc_t rs, const unsigned (&v)[3], unsigned soff, LAS char* l) {
  asm volatile("s_mov_b32 m0, %4\n\ts_nop 0\n\tbuffer_load_dwordx4 %0, %2, %3 offen lds\n\ts_mov_b32 exec_hi, 0\n\t"
               "buffer_load_dwordx4 %1, %2, %3 offen offset:1024 lds\n\ts_mov_b32 exec_hi, -1"
               :: "v"(v[0]), "v"(v[1]), "s"(rs), "s"(soff), "s"((unsigned)(size_t)l) : "memory");
}
template <int OFF> DI void AST16(rsrc_t rs, unsigned voff, unsigned soff, u32x4 v) {
  asm volatile("buffer_store_dwordx4 %0, %1, %2, %3 offen offset:%4 sc1\n\ts_nop 1" :: "v"(v), "v"(voff), "s"(rs), "s"(soff), "i"(OFF) : "memory");
}
DI void BLDS16(rsrc_t rs, unsigned voff, unsigned soff, LAS char* l) {
  asm volatile("s_mov_b32 m0, %3\n\ts_mov_b64 exec, 0xffff\n\tbuffer_load_dwordx4 %0, %1, %2 offen lds\n\ts_mov_b64 exec, -1"
               :: "v"(voff), "s"(rs), "s"(soff), "s"((unsigned)(size_t)l) : "memory");
}
DI void TOUCH(rsrc_t rs, unsigned voff, unsigned soff, LAS char* l) {
  asm volatile("s_mov_b32 m0, %3\n\ts_nop 0\n\tbuffer_load_dword %0, %1, %2 offen lds"
               :: "v"(voff), "s"(rs), "s"(soff), "s"((unsigned)(size_t)l) : "memory");
}
DI void ald16(u32x4& d, rsrc_t rs, unsigned voff, unsigned soff) {
  asm volatile("buffer_load_dwordx4 %0, %1, %2, %3 offen sc1" : "=v"(d) : "v"(voff), "s"(rs), "s"(soff) : "memory");
}
DI void ald8(u32x2& d, rsrc_t rs, unsigned voff, unsigned soff) {
  asm volatile("buffer_load_dwordx2 %0, %1, %2, %3 offen sc1" : "=v"(d) : "v"(voff), "s"(rs), "s"(soff) : "memory");
}
DI void ald4(unsigned& d, rsrc_t rs, unsigned voff, unsigned soff) {
  asm volatile("buffer_load_dword %0, %1, %2, %3 offen sc1" : "=v"(d) : "v"(voff), "s"(rs), "s"(soff) : "memory");
}
DI void gld8(u32x2& d, const void* p) { asm volatile("global_load_dwordx2 %0, %1, off" : "=v"(d) : "v"(p) : "memory"); }
#define ST16(rs, voff, soff, v) __builtin_amdgcn_raw_buffer_store_b128(v, rs, (int)((voff) + (soff)), 0, 16)
#define ST8(rs, voff, soff, v) __builtin_amdgcn_raw_buffer_store_b64(v, rs, (int)(voff), (int)(soff), 16)
#define ST4(rs, voff, soff, v) __builtin_amdgcn_raw_buffer_store_b32(v, rs, (int)(voff), (int)(soff), 16)
#define LD16(rs, voff, soff) __builtin_amdgcn_raw_buffer_load_b128(rs, (int)(voff), (int)(soff), 16)
#define LD8(rs, voff, soff) __builtin_amdgcn_raw_buffer_load_b64(rs, (int)(voff), (int)(soff), 16)

constexpr unsigned WS_EPOCH = 0;
constexpr unsigned WS_F1 = 1024;
constexpr unsigned WS_F2K = WS_F1 + 4096;
constexpr unsigned WS_F2Z = WS_F2K + 2048;
constexpr unsigned WS_F3O = WS_F2Z + 2048;
constexpr unsigned WS_F4 = WS_F3O + 2048;
constexpr unsigned WS_STAT = 20480;
constexpr unsigned WS_KVP = WS_STAT + 16 * 12 * 256 * 8;
constexpr unsigned WS_KVF = WS_KVP + 64 * 36864;
constexpr unsigned WS_PQ = WS_KVF + 16 * 5632;
constexpr unsigned WS_WX = WS_PQ + 128 * 16 * 1152;
constexpr unsigned WS_Z2N = WS_WX + 8 * 90 * 4608;
constexpr unsigned WS_Z2O = WS_Z2N + 16 * 64 * 8 * 1024;
constexpr unsigned WS_Z1 = WS_Z2O + 64 * 16 * 1024;
constexpr unsigned WS_HX = WS_Z1 + 128 * 72 * 1024;
constexpr unsigned WS_END = WS_HX + 128 * 90 * 1024;
constexpr unsigned SPIN_MAX = 1u << 15;

#define STAMP(K, i) do {} while (0)
#define STAMP_OUT() do {} while (0)
#define FINE_DECL() do {} while (0)
#define FINE(n) do {} while (0)
#define FINE_OUT(n) do {} while (0)

struct Params {
  const u16* x; const u16* wqkv; const u16* wo; const u16* win; const u16* wsp; const u16* bsp; const u16* wout;
  u16* out; char* ws;
};

DI int lane_id() {
  int l; asm volatile("v_mbcnt_lo_u32_b32 %0, -1, 0\n\tv_mbcnt_hi_u32_b32 %0, -1, %0" : "=v"(l)); return l;
}
DI unsigned cvt_pk(float lo, float hi) {
  f32x2 v = {lo, hi}; bf16x2_t b = __builtin_convertvector(v, bf16x2_t); return __builtin_bit_cast(unsigned, b);
}
DI u32x4 norm4(u32x4 v, unsigned rlo, unsigned rhi, float sh) {
  unsigned a = v.x, b = v.y, c = v.z, d = v.w; float t0, t1, t2, t3;
  asm("v_dot2_f32_bf16 %4, %0, %8, %10\n\tv_dot2_f32_bf16 %0, %0, %9, %10\n\t"
      "v_dot2_f32_bf16 %5, %1, %8, %10\n\tv_dot2_f32_bf16 %1, %1, %9, %10\n\t"
      "v_dot2_f32_bf16 %6, %2, %8, %10\n\tv_dot2_f32_bf16 %2, %2, %9, %10\n\t"
      "v_dot2_f32_bf16 %7, %3, %8, %10\n\tv_dot2_f32_bf16 %3, %3, %9, %10\n\t"
      "v_cvt_pk_bf16_f32 %0, %4, %0\n\tv_cvt_pk_bf16_f32 %1, %5, %1\n\tv_cvt_pk_bf16_f32 %2, %6, %2\n\tv_cvt_pk_bf16_f32 %3, %7, %3"
      : "+v"(a), "+v"(b), "+v"(c), "+v"(d), "=&v"(t0), "=&v"(t1), "=&v"(t2), "=&v"(t3) : "v"(rlo), "v"(rhi), "v"(sh));
  return (u32x4){a, b, c, d};
}
DI float bf_lo(unsigned v) { return __uint_as_float(v << 16); }
DI float bf_hi(unsigned v) { return __uint_as_float(v & 0xffff0000u); }
constexpr float GC = 0.4686686931f, GKK = 4.912229539f;
DI float gelu_s(float x) {
  const float e = __builtin_amdgcn_exp2f(-x * __builtin_fmaf(x, x, GKK));
  return x * __builtin_amdgcn_rcpf(1.0f + e);
}
DI float phi_f(float t) {
  return __builtin_fmaxf(t, 1e-6f) + __builtin_amdgcn_fmed3f(__builtin_amdgcn_exp2f(t * 1.4426950409f), 0.f, 1.f);
}
DI bf16x8 tr_frag(LAS char* a0, LAS char* a1) {
  const s16x4 lo = __builtin_amdgcn_ds_read_tr16_b64_v4i16((LAS s16x4*)a0);
  const s16x4 hi = __builtin_amdgcn_ds_read_tr16_b64_v4i16((LAS s16x4*)a1);
  return __builtin_shufflevector(lo, hi, 0, 1, 2, 3, 4, 5, 6, 7);
}
template <int CTRL> DI float dpp_get(float v) {
  return __builtin_bit_cast(float, __builtin_amdgcn_update_dpp(0, __builtin_bit_cast(int, v), CTRL, 0xf, 0xf, true));
}
DI float oct_sum(float v) {
  v += dpp_get<0xB1>(v); v += dpp_get<0x4E>(v); v += dpp_get<0x141>(v); return v;
}
DI int slot_row(int rs) { return (rs & ~31) + 16 * ((rs >> 2) & 1) + 4 * ((rs >> 3) & 3) + (rs & 3); }
DI unsigned flag_ld(const char* ws, unsigned off) {
  return __hip_atomic_load((GAS unsigned*)(ws + off), __ATOMIC_RELAXED, __HIP_MEMORY_SCOPE_AGENT);
}
DI void flag_st(char* ws, unsigned off, unsigned v) {
  __hip_atomic_store((GAS unsigned*)(ws + off), v, __ATOMIC_RELAXED, __HIP_MEMORY_SCOPE_AGENT);
}
DI bool all_eq(unsigned v, unsigned tag) { return __builtin_amdgcn_ballot_w64(v != tag) == 0; }
DI void spin_flag(const char* ws, unsigned off, unsigned tag) {
  const rsrc_t rs = MKRS(ws, WS_END);
#pragma unroll 1
  for (unsigned i = 0; i < SPIN_MAX; ++i) {
    unsigned v;
    asm volatile("buffer_load_dword %0, %1, %2, 0 offen sc1\n\ts_waitcnt vmcnt(0)" : "=&v"(v) : "v"(off), "s"(rs) : "memory");
    if (all_eq(v, tag)) break;
    __builtin_amdgcn_s_sleep(1);
  }
}

constexpr int LDS_BYTES = 160 * 1024;
constexpr int L_SCR = 159 * 1024;
constexpr int A_XN = 15 * 1024;
constexpr int AZ_SLOT = 24 * 1024, AZ_PARK = 96 * 1024, AZ_ST = 144 * 1024, AZ_ST2 = 147 * 1024, AZ_TAB = 151 * 1024;
constexpr int AQ_SLOT = 20 * 1024, AQ_PK = 80 * 1024, AQ_V = 116 * 1024;
constexpr int STG = 34 * 1024, B_KV = 4 * STG;
#define TMODE 1
#define OUTAUX 0
#define XRD(ks) (*(LAS bf16x8*)(xb[(ks) & 3] + ((ks) >> 2) * 16384))
#define SLOT(n) ((((n) + 2) & 3) * STG)
#define ROTB 3
#define ROTC 3
#define ROT0 0
#define ROT(n, r, m) ((n) < ROT0 ? (n) : (((n) & ~(m)) | (((n) + (r)) & (m))))

struct Ctx {
  int pa, quad, jj, p, c, wid, bid;
  unsigned tag;
};

template <int ROLE> struct Loader {
  rsrc_t rsws, rsw; unsigned voff[4]; unsigned l16; int p, c; LAS char* smem;
  DI void init(const Params& P, const Ctx& X, LAS char* sm) {
    smem = sm; p = X.p; c = X.c;
    const int ln = lane_id();
    l16 = ln * 16;
    rsws = MKRS(P.ws, WS_END);
    if (ROLE == 1) {
      rsw = MKRS((const char*)P.wsp + (size_t)p * 128 * 4096, 128 * 4096);
#pragma unroll
      for (int i = 0; i < 4; ++i) {
        const int rs = i * 8 + (ln >> 3);
        voff[i] = (unsigned)(slot_row(rs) * 4096 + (((ln & 7) ^ ((rs & 15) >> 1)) * 16) - i * 1024);
      }
    }
  }
  DI void issue_b(int N) {
    LAS char* d = smem + SLOT(N);
    const int n = ROT(N, p + c, ROTB);
    if (ROLE == 0) {
      const unsigned so = WS_Z2N + c * (64 * 8 * 1024) + n * 16384;
#pragma unroll
      for (int i = 0; i < 4; ++i) BLDSx<4>(rsws, l16, so + i * 4096, d + i * 4096);
      BLDSx<1>(rsws, l16, WS_Z2O + (n * 32 + c) * 1024, d + 16384);
      BLDSx<1>(rsws, l16, WS_Z2O + (n * 32 + 16 + c) * 1024, d + 17408);
    } else {
#pragma unroll
      for (int i = 0; i < 4; ++i) BLDS4v(rsw, voff, n * 128 + i * (32 * 4096), d + 18432 + i * 4096);
    }
  }
  template <int NK> DI void issue_c(int N) {
    LAS char* d = smem + SLOT(N);
    const int M = N - 32, rg = 2 * p + (c >> 3), cg = c & 7;
    const int kb = M == 0 ? 88 : M < 5 ? 72 + 4 * (M - 1) : 4 * (M < 21 ? ROT(M - 5, p + c, ROTC) : M - 5);
    if (ROLE == 0) {
      const unsigned so = WS_WX + (cg * 90 + kb) * 4608;
#pragma unroll
      for (int i = 0; i < NK; ++i) BLDSx<4>(rsws, l16, so + i * 4096, d + i * 4096);
      BLDSx<NK / 2>(rsws, l16, so + NK * 4096, d + NK * 4096);
    } else {
#pragma unroll
      for (int i = 0; i < 4; ++i) BLDSx<NK>(rsws, l16, WS_HX + ((rg * 4 + i) * 90 + kb) * 1024, d + 18432 + i * 4096);
    }
  }
};
DI void first_w(const Params& P, const Ctx& X, LAS char* smem) {
  const int ln = lane_id(), st = X.wid >> 2, g = X.wid & 3;
  unsigned voff[4];
#pragma unroll
  for (int i = 0; i < 4; ++i) {
    const int rs = i * 8 + (ln >> 3);
    voff[i] = (unsigned)(slot_row(rs) * 4096 + (((ln & 7) ^ ((rs & 15) >> 1)) * 16) - i * 1024);
  }
  BLDS4v(MKRS((const char*)P.wsp + (size_t)X.p * 128 * 4096, 128 * 4096), voff, ROT(st, X.p + X.c, ROTB) * 128 + g * (32 * 4096),
         smem + SLOT(st) + 18432 + g * 4096);
}
DI void first_z(const Params& P, const Ctx& X, LAS char* smem) {
  const int st = X.wid >> 2, g = X.wid & 3, n = ROT(st, X.p + X.c, ROTB);
  const unsigned l16 = lane_id() * 16;
  const rsrc_t rs = MKRS(P.ws, WS_END);
  BLDSx<4>(rs, l16, WS_Z2N + X.c * (64 * 8 * 1024) + n * 16384 + g * 4096, smem + SLOT(st) + g * 4096);
  BLDSx<1>(rs, l16, WS_Z2O + (n * 32 + (g & 1) * 16 + X.c) * 1024, smem + SLOT(st) + 16384 + (g & 1) * 1024);
}
DI void warm_code(const Ctx& X, LAS char* smem) {
  u64 pc; unsigned len;
  asm volatile("s_getpc_b64 %0\n1:\n\ts_mov_b32 %1, 1b-_Z2kF6Params" : "=s"(pc), "=s"(len));
  pc -= len;
  asm volatile("s_mov_b32 %0, kf_code_end-_Z2kF6Params" : "=s"(len));
  if ((int)len < 0) len = 0;
  TOUCH(MKRS((const void*)pc, len), lane_id() * 128, (X.bid >> 3) * 8192, smem + L_SCR);
}
DI void warm_b(const Params& P, const Ctx& X, LAS char* smem) {
  u64 pc; unsigned len;
  asm volatile("s_getpc_b64 %0\n1:\n\ts_mov_b32 %1, 1b-_Z2kF6Params" : "=s"(pc), "=s"(len));
  pc -= len;
  asm volatile("s_mov_b32 %0, kf_code_end-_Z2kF6Params" : "=s"(len));
  if ((int)len < 0) len = 0;
  const bool code = X.wid == 7;
  const int l = lane_id();
  TOUCH(MKRS(code ? (const void*)pc : (const void*)P.wsp, code ? len : X.wid == 6 ? 2048u * 4096u : 0u), code ? l * 128 : (l >> 2) * 4096 + (l & 3) * 128,
        code ? (X.bid >> 3) * 8192 : (X.p * 128 + (X.c & 7) * 16) * 4096, smem + L_SCR);
}
DI unsigned f1_off(const Ctx& X) { const int l = lane_id(); return WS_F1 + X.c * 256 + (l < 48 ? l : 47) * 4; }
DI int f1_src(int c, int w) { return (w == 0 ? 8 * c : w == 1 ? 8 * c + 7 : 128 + c) / 12; }

#define Z_XB1 140
#define Z_GAT 151
#define Z_XB2 164
#define Z_F1 200
#define Z_FW 211
#define Z_POLL 210
struct ZS {
  static constexpr int XB1 = Z_XB1, PUB = Z_XB1 + 2, GAT = Z_GAT, XB2 = Z_XB2, F1 = Z_F1, FW = Z_FW, POLL = Z_POLL, WARM = 135;
  static constexpr bool sbar(int E) { return E % 12 == 8 && E < 212; }
  static constexpr bool wsp(int E) { return !(TMODE & 4) && (E == 45 || E == 57 || E == 69); }
  static constexpr int nrm(int ch) { return XB2 + 6 + 2 * ch + ch / 3; }
  static constexpr bool dma(int E) { return E >= 12 && (E + 4) / 12 + 2 < 18 && E % 12 < 3; }
  static constexpr int other(int E) {
    int n = (E == PUB) + (E >= GAT && E < GAT + 3) + (E == F1) + (E == POLL) + (E == WARM) + wsp(E) + (E == 155 || E == 164 || E == 201);
    for (int ch = 0; ch < 6; ++ch) n += E == nrm(ch) + 1;
    return n;
  }
  static constexpr int younger(int SN) {
    const int last = 12 * SN - 22, bar = 12 * SN - 4;
    int n = (SN == 1 ? 3 : 0) + ((TMODE & 1) && SN <= 2 ? 1 : 0);
    for (int E = last < 0 ? 0 : last; E < bar; ++E) n += other(E) + ((E > last && dma(E)) ? 1 : 0);
    return n;
  }
  static constexpr int after_gather() {
    int n = 0;
    for (int E = GAT + 3; E < XB2 - 3; ++E) n += other(E) + (dma(E) ? 1 : 0);
    return n;
  }
  static constexpr int after_z2n() {
    int n = 0;
    for (int E = nrm(5) + 2; E < F1; ++E) n += other(E) + (dma(E) ? 1 : 0);
    return n;
  }
};
DI unsigned z_voff(int wid, int lane) {
  const int row = wid * 8 + (lane >> 3);
  return (unsigned)(row * 9216 + (((lane & 7) ^ (((row >> 1) & 1) << 2)) * 16));
}
DI void a_zblock(const Params& P, const Ctx& X, LAS char* smem, bf16x8 (&xf)[36], LAS char* (&xb)[4]) {
  const int wid = X.wid, p = X.pa, j = X.quad * 3 + X.jj;
  int lane = lane_id();
  const int rw = wid & 3, G = wid >> 2;
  const int h = lane >> 5;
  const rsrc_t rsw = MKRS(P.win, 576 * 9216), rsws = MKRS(P.ws, WS_END);
  const brs_t ws = __builtin_amdgcn_make_buffer_rsrc(P.ws, 0, WS_END, 0x00020000);
  const unsigned voff = z_voff(wid, lane);
  const int cz2 = (2304 + 3 * j * 64) * 2, cz1 = 3 * j * 64 * 2;
  auto dma = [&](auto S_, auto I_) {
    constexpr int S = decltype(S_)::value, I = decltype(I_)::value, R = S / 3;
    BLDS(rsw, voff, (R < 3 ? cz2 + R * 128 : cz1 + (R - 3) * 128) + ((S % 3) * 192 + I * 64) * 9216,
         smem + (S & 3) * AZ_SLOT + I * 8192 + wid * 1024);
  };
  const int rq = (lane >> 2) & 3;
  LAS char* rd = smem + (8 * h + rq) * 128 + ((G ^ (rq >> 1)) * 64) + ((lane >> 4) & 1) * 32 + (lane & 3) * 8;
  LAS char* park = smem + AZ_PARK + wid * 2048 + h * 1024 + (lane & 31) * 16;
  LAS char* strd = smem;
  const unsigned zo = WS_Z1 + ((p * 8 + rw * 2) * 72 + 6 * j + G) * 1024;
  unsigned zv = 0, nvo = 0, fpoll = 0, f1v = 0;
  const bf16x8 ones = {0x3f80, 0x3f80, 0x3f80, 0x3f80, 0x3f80, 0x3f80, 0x3f80, 0x3f80};
  f32x16 acc[2];
  bf16x8 wf[5], pf[2];
  f32x4 ssum[2], ssq[2];
  unsigned pk[4];
  float gt[4] = {0.f, 0.f, 0.f, 0.f}, gb = 0.f, xt[4];
  u32x4 gv[3];
  f32x4 tab[2][4];
  u32x4 nv[2];
  auto rdw = [&](auto E_) {
    constexpr int E = decltype(E_)::value, S = E / 12, o = (S & 3) * AZ_SLOT + (E % 12) * 2048;
    wf[E % 5] = tr_frag(rd + o, rd + o + 512);
  };
  auto stats_rd = [&](int rr) {
    if (rr == 0) {
      const int ln = lane_id();
      strd = smem + AZ_PARK + wid * 2048 + ((ln >> 1) & 1) * 512 + (8 * (ln >> 4) + ((ln >> 2) & 3)) * 16 + (ln & 1) * 8;
    }
#pragma unroll
    for (int u = 0; u < 2; ++u) pf[u] = tr_frag(strd + rr * 16384 + u * 1024, strd + rr * 16384 + u * 1024 + 64);
  };
  auto stats_mm = [&](auto RR_) {
    constexpr int RR = decltype(RR_)::value;
#pragma unroll
    for (int u = 0; u < 2; ++u) {
      const f32x4 zz = {0, 0, 0, 0};
      ssum[u] = MFMA(ones, pf[u], RR ? ssum[u] : zz);
      ssq[u] = MFMA(pf[u], pf[u], RR ? ssq[u] : zz);
      PIN(ssum[u]); PIN(ssq[u]);
    }
  };
  auto gelu_st = [&](auto RR_, auto EL_, auto ST_) {
    constexpr int RR = decltype(RR_)::value, EL = decltype(EL_)::value, ST = decltype(ST_)::value;
    if constexpr (EL >= 0 && EL < 16) {
      constexpr int reg = RR < 3 ? EL : (EL & 3) + ((EL >> 2) & 1) * 8 + (EL >> 3) * 4;
      const float x = acc[RR & 1][reg];
      float& g = gt[EL & 3];
      if (ST == 0) { g = __builtin_fmaf(x, x, GKK); PIN(g); }
      if (ST == 1) { g = __builtin_amdgcn_exp2f(-x * g); PIN(g); }
      if (ST == 2) { g = __builtin_amdgcn_rcpf(1.0f + g); PIN(g); }
      if (ST == 3) {
        const float y = x * g;
        if (EL & 1) { pk[(EL >> 1) & 3] = cvt_pk(gb, y); PIN(pk[(EL >> 1) & 3]); } else { gb = y; PIN(gb); }
      }
    }
  };
  auto put = [&](auto RR_, auto CC_) {
    constexpr int RR = decltype(RR_)::value, CC = decltype(CC_)::value;
    const u32x4 w = {pk[0], pk[1], pk[2], pk[3]};
    if constexpr (RR < 3) *(LAS u32x4*)(park + RR * 16384 + CC * 512) = w;
    else AST16<(RR - 3) * 2048 + CC * 512>(rsws, zv, zo, w);
  };
  auto gather = [&](int i) {
    const int t = wid * 64 + lane_id();
    ald16(gv[i], rsws, (t & 127) * 16 + ((t >> 7) * 3 + i) * 2048, WS_STAT + p * 12 * 2048);
  };
  FINE_DECL();
  FINE(0);
  sfor<0, 4>(rdw);
  sfor<0, 216>([&](auto E_) {
    constexpr int E = decltype(E_)::value, R = E / 36, ks = E % 36, EN = E + 4, SN = EN / 12;
    if constexpr (E == ZS::XB1 || E == ZS::XB2) asm volatile("s_waitcnt lgkmcnt(4)" ::: "memory");
    if constexpr (E == 8) asm volatile("s_waitcnt lgkmcnt(8)" ::: "memory");
    if constexpr (E == ZS::F1) wait_v<ZS::after_z2n()>();
    if constexpr (ZS::sbar(E)) wait_v<ZS::younger(SN)>();
    if constexpr (ZS::sbar(E) || E == ZS::XB1 || E == ZS::XB2 || E == ZS::F1) BAR();
    if constexpr (ZS::sbar(E)) FINE(SN);
    if constexpr (E == ZS::F1) ST4(ws, f1v, WS_F1, X.tag);
    if constexpr (E == ZS::F1 - 10) {
      const int ln = lane_id();
      f1v = (wid == 0 && ln < 48 && f1_src(ln & 15, ln >> 4) == j) ? (ln & 15) * 256 + ((ln >> 4) * 16 + p) * 4 : 0x7fffff00u; PIN(f1v);
    }
    if constexpr (EN < 216) {
      rdw(IC<EN>{});
      if constexpr (E < 10) { xf[16 + 2 * E] = XRD(16 + 2 * E); xf[17 + 2 * E] = XRD(17 + 2 * E); }
      if constexpr (ZS::dma(E)) dma(IC<SN + 2>{}, IC<EN % 12 - 4>{});
    }
    const f32x16 zz = {};
    if constexpr (R < 3) acc[R & 1] = MFMA32(xf[ks], wf[E % 5], ks ? acc[R & 1] : zz);
    else acc[R & 1] = MFMA32(wf[E % 5], xf[ks], ks ? acc[R & 1] : zz);
    PIN(acc[R & 1]);
    if constexpr ((R == 1 || R == 2 || R == 5) && ks >= 1) {
      gelu_st(IC<R - 1>{}, IC<(ks - 1) / 2>{}, IC<(ks - 1) & 1>{});
      gelu_st(IC<R - 1>{}, IC<(ks - 1) / 2 - 1>{}, IC<((ks - 1) & 1) + 2>{});
      if constexpr (ks == 21) put(IC<R - 1>{}, IC<0>{});
      if constexpr (ks == 34 && R < 5) put(IC<R - 1>{}, IC<1>{});
    }
    if constexpr (R == 3 || R == 4) {
      sfor<0, 4>([&](auto ST_) { gelu_st(IC<R - 1>{}, IC<ks - 1 - decltype(ST_)::value>{}, ST_); });
      if constexpr (ks == 11) put(IC<R - 1>{}, IC<0>{});
      if constexpr (ks == 20) put(IC<R - 1>{}, IC<1>{});
    }
    if constexpr (R == 3) {
      if constexpr (ks == 17 || ks == 20 || ks == 23) stats_mm(IC<(ks - 17) / 3>{});
      if constexpr (ks == 14 || ks == 17 || ks == 20) stats_rd((ks - 14) / 3);
      if constexpr (ks == 33) {
        const int ln = lane_id();
        zv = ((ln >> 4) & 1) * (72 * 1024) + (ln >> 5) * 256 + (ln & 15) * 16; PIN(zv);
        nvo = ((ln >> 4) & 1) * 1024 + (ln >> 5) * 512 + (ln & 15) * 16; PIN(nvo);
      }
    }
    if constexpr (E == ZS::XB1 - 3) {
      const int ln = lane_id();
      const int dst = (ln >> 4) == ((ln >> 2) & 3) ? G * 1024 + (rw * 32 + (ln & 15)) * 8 : 2048 + (ln & 15) * 8;
#pragma unroll
      for (int u = 0; u < 2; ++u) {
        const float q = (ln & 2) ? ((ln & 1) ? ssq[u][3] : ssq[u][2]) : ((ln & 1) ? ssq[u][1] : ssq[u][0]);
        *(LAS f32x2*)(smem + AZ_ST + dst + u * 128) = (f32x2){ssum[u][0], q};
      }
    }
    if constexpr (E == ZS::XB1) {
      const int t8 = rw * 64 + lane_id();
      xt[0] = *(LAS float*)(smem + AZ_ST + t8 * 4); xt[1] = *(LAS float*)(smem + AZ_ST + 1024 + t8 * 4);
    }
    if constexpr (E == ZS::PUB) {
      const int t8 = rw * 64 + lane_id();
      ST8(ws, t8 * 8, G ? 0x7fffff00u : WS_STAT + (p * 12 + j) * 2048, ((u32x2){__float_as_uint(xt[0] + xt[1]), X.tag}));
    }
    if constexpr (E >= ZS::GAT && E < ZS::GAT + 3) { asm volatile("" ::: "memory"); gather(E - ZS::GAT); asm volatile("" ::: "memory"); }
    if constexpr (E == ZS::XB2 - 3) {
      wait_v<ZS::after_gather()>();
#pragma unroll 1
      for (unsigned spins = 0; spins < SPIN_MAX; ++spins) {
        PIN(gv[0]); PIN(gv[1]); PIN(gv[2]);
        bool ok = true;
#pragma unroll
        for (int i = 0; i < 3; ++i) ok &= gv[i].y == X.tag && gv[i].w == X.tag;
        if (__builtin_amdgcn_ballot_w64(!ok) == 0) break;
        __builtin_amdgcn_s_sleep(1);
        asm volatile("" ::: "memory");
#pragma unroll
        for (int i = 0; i < 3; ++i) gather(i);
        wait_v<0>();
      }
      *(LAS f32x2*)(smem + AZ_ST2 + (wid * 64 + lane_id()) * 8) =
          (f32x2){__uint_as_float(gv[0].x) + __uint_as_float(gv[1].x) + __uint_as_float(gv[2].x),
                  __uint_as_float(gv[0].z) + __uint_as_float(gv[1].z) + __uint_as_float(gv[2].z)};
    }
    if constexpr (E == ZS::XB2) {
      LAS float* a = (LAS float*)(smem + AZ_ST2 + rw * 256 + lane_id() * 4);
#pragma unroll
      for (int i = 0; i < 4; ++i) xt[i] = a[i * 256];
    }
    if constexpr (E == ZS::XB2 + 3) {
      const int ln = lane_id();
      const float v = ((xt[0] + xt[1]) + (xt[2] + xt[3])) * (1.0f / 2304.0f), o = dpp_get<0xB1>(v);
      const float m = (ln & 1) ? o : v, q = (ln & 1) ? v : o;
      const float sc = __builtin_amdgcn_rsqf(__builtin_fmaxf(q - m * m, 0.f) + 1e-5f * GC * GC);
      *(LAS float*)(smem + AZ_TAB + wid * 256 + ln * 4) = (ln & 1) ? -m * sc : sc;
    }
    sfor<0, 6>([&](auto CH_) {
      constexpr int ch = decltype(CH_)::value, cc = ch / 3, rr = ch % 3, q0 = ZS::nrm(ch);
      if constexpr (E == q0 || E == q0 + 1) {
        constexpr int hf = E - q0;
#pragma unroll
        for (int i = 2 * hf; i < 2 * hf + 2; ++i) {
          unsigned t = nv[ch & 1][i];
          t = cvt_pk(__builtin_fmaf(bf_lo(t), tab[cc][i][0], tab[cc][i][1]), __builtin_fmaf(bf_hi(t), tab[cc][i][2], tab[cc][i][3]));
          PIN(t); nv[ch & 1][i] = t;
        }
        if constexpr (hf == 1) {
          const int tg = (3 * j + rr) * 4 + 2 * G, sb = p * 4 + rw;
          AST16<cc * 256>(rsws, nvo, tg < 128 ? WS_Z2N + (((tg >> 3) * 64 + sb) * 8 + (tg & 7)) * 1024 : WS_Z2O + (sb * 16 + tg - 128) * 1024, nv[ch & 1]);
        }
      }
      if constexpr (rr == 0 && E == q0 - 2) {
        const int hb = (lane_id() >> 5) * 128;
#pragma unroll
        for (int i = 0; i < 4; ++i) tab[cc][i] = *(LAS f32x4*)(smem + AZ_TAB + wid * 256 + hb + cc * 64 + i * 16);
      }
      if constexpr (E == q0 - 2) nv[ch & 1] = *(LAS u32x4*)(park + rr * 16384 + cc * 512);
    });
    if constexpr (E == ZS::POLL) {
      asm volatile("" ::: "memory");
      ald4(fpoll, rsws, f1_off(X), 0);
    }
    if constexpr (E == ZS::FW) first_w(P, X, smem);
    if constexpr (E == ZS::WARM) warm_b(P, X, smem);
    if constexpr (ZS::wsp(E)) {
      constexpr int ev = (E - 45) / 12;
      TOUCH(MKRS(P.wsp, (wid >> 1) == ev ? 2048u * 4096u : 0u), lane_id() * 128, ((p * 12 + j) * 6 + wid) * 8192, smem + L_SCR);
    }
    SB0();
  });
  FINE(18); FINE_OUT(19);
  STAMP(0, 9);
  wait_v<4>(); PIN(fpoll);
  const bool rdy = all_eq(fpoll, X.tag);
  if (rdy) first_z(P, X, smem);
  STAMP(0, 10);
  u32x4 w[2];
#pragma unroll
  for (int cc = 0; cc < 2; ++cc) {
#pragma unroll
    for (int i = 0; i < 4; ++i) {
      const int reg = ((2 * i) & 3) + (i >> 1) * 8 + cc * 4;
      w[cc][i] = cvt_pk(gelu_s(acc[1][reg]), gelu_s(acc[1][reg + 1]));
    }
    PIN(w[cc]);
    if (cc == 0 && !rdy) { spin_flag(P.ws, f1_off(X), X.tag); STAMP(0, 13); first_z(P, X, smem); }
  }
  put(IC<4>{}, IC<1>{});
  AST16<0>(rsws, zv, zo + 4096, w[0]); AST16<512>(rsws, zv, zo + 4096, w[1]);
  if (wid < 4) {
    lane = lane_id();
    const int kb0 = ((p & 1) * 12 + j) * 4 + rw, kb = kb0 < 90 ? kb0 : 0;
    const rsrc_t rs = kb < 72 ? MKRS(P.wout, 2304 * 1152) : MKRS(P.wo, 576 * 1152);
    const int r0 = kb < 72 ? 32 * kb : kb < 88 ? 36 * (kb - 72) : 288 * (kb - 88) + 32;
    const int mq = kb < 64 ? 4 : kb < 72 ? 8 : kb < 88 ? 4 : 72, mu = kb < 64 ? 16 : kb < 72 ? 4 : kb < 88 ? 16 : 36;
#pragma unroll
    for (int i = 0; i < 5; ++i) {
      const int pc = i < 4 ? i * 64 + lane : 256 + (lane & 31), k = (pc * 57) >> 9, ch = pc - k * 9;
      BLDS(rs, ((k >> 3) * mq + ((k >> 2) & 1) * mu + (k & 3)) * 1152 + ch * 16, r0 * 1152 + (p >> 1) * 144, smem + B_KV + rw * 5120 + i * 1024);
    }
    wait_v<8>();
  } else {
    wait_v<3>();
  }
}

template <int G> struct QS {
  static constexpr int nt(int R) { return R == (G ? 0 : 2) ? 3 : 2; }
  static constexpr int t0(int R) { return G ? (R == 1 ? 4 : 5) - nt(R) : 0; }
  static constexpr int base(int R) { return R == 0 ? 0 : base(R - 1) + 36 * nt(R - 1); }
  static constexpr int NE = base(3);
  static constexpr int round(int E) { return E < base(1) ? 0 : E < base(2) ? 1 : 2; }
  static constexpr int ks(int E) { return (E - base(round(E))) / nt(round(E)); }
  static constexpr int tile(int E) { return (E - base(round(E))) % nt(round(E)); }
  static constexpr int stage(int E) { return 9 * round(E) + ks(E) / 4; }
  static constexpr int pos(int E) { return (E - base(round(E))) % (4 * nt(round(E))); }
  static constexpr int ent(int el) { return base(1) + 4 + (G ? el + el / 3 : 2 * el); }
  static constexpr int KV0 = base(2) + 8 * nt(2) - 4;
  static constexpr int NU = G ? 7 : 4, UL = G ? 4 : 6;
  static constexpr int ND = G ? 2 : 3, PRE = 3;
  static constexpr int dma(int E) {
    return (E + 4 < NE && pos(E + 4) >= 1 && pos(E + 4) <= 2 * ND && (pos(E + 4) & 1) && stage(E + 4) + 2 < 27 && stage(E + 4) + 2 >= PRE) ? 1 + pos(E + 4) / 2 : 0;
  }
  static constexpr int WARM = base(2) - 10;
  static constexpr int other(int E) { return ((E >= KV0 && E < KV0 + NU * UL && (E - KV0) % UL == UL - 1) ? 1 : 0) + (E == WARM); }
  static constexpr int younger(int Eb) {
    const int SN = stage(Eb + 4);
    int n = 0;
    for (int E = Eb - 1; E >= 0; --E) {
      n += other(E);
      if (dma(E)) { if (stage(E + 4) + 2 == SN) return n; n += 1; }
    }
    return n + (PRE - 1 - SN) * ND + ((TMODE & 1) ? 2 : 0);
  }
};
template <int G> DI void q_voff(unsigned (&voff)[3], int rw, int lane) {
#pragma unroll
  for (int i = 0; i < QS<G>::ND; ++i) {
    const int pc = (G ? 768 + rw * 128 : rw * 192) + i * 64 + lane, sr = (pc * 3277) >> 16, ch = pc - sr * 20;
    voff[i] = (unsigned)(sr * 3456 + ch * 16 + (ch >= 18 ? 1120 : 0));
  }
}
template <int G> DI void q_first(const Params& P, const Ctx& X, LAS char* smem) {
  const rsrc_t rsw = MKRS(P.wqkv, 576 * 3456);
  const int rw = X.wid & 3;
  unsigned voff[3];
  q_voff<G>(voff, rw, lane_id());
#pragma unroll
  for (int S = 0; S < QS<G>::PRE; ++S)
#pragma unroll
    for (int i = 0; i < QS<G>::ND; ++i)
      BLDS(rsw, voff[i], X.quad * 288 + S * 64 * 3456 + 1152, smem + S * AQ_SLOT + (G ? 12288 + rw * 2048 : rw * 3072) + i * 1024);
}
template <int G>
DI void a_qkvblock(const Params& P, const Ctx& X, LAS char* smem, bf16x8 (&xf)[36], LAS char* (&xb)[4]) {
  typedef QS<G> Q;
  const int wid = X.wid, p = X.pa, quad = X.quad;
  int lane = lane_id();
  const int rw = wid & 3, h = lane >> 5;
  const rsrc_t rsw = MKRS(P.wqkv, 576 * 3456), rsws = MKRS(P.ws, WS_END);
  const brs_t ws = __builtin_amdgcn_make_buffer_rsrc(P.ws, 0, WS_END, 0x00020000);
  unsigned voff[3];
  q_voff<G>(voff, rw, lane);
  auto dma = [&](auto S_, auto I_) {
    constexpr int S = decltype(S_)::value, I = decltype(I_)::value;
    if constexpr (S == 9) {
      const unsigned c = voff[I] % 3456u;
      voff[I] -= c >= 1408u ? 1152u : 0u; PIN(voff[I]);
    }
    BLDS(rsw, voff[I], quad * 288 + (S % 9) * 64 * 3456 + ((S / 9 + 1) % 3) * 1152,
         smem + (S & 3) * AQ_SLOT + (G ? 12288 + rw * 2048 : rw * 3072) + I * 1024);
  };
  LAS char* rd = smem + (8 * h + ((lane >> 2) & 3)) * 320 + ((lane >> 4) & 1) * 32 + (lane & 3) * 8;
  const bool isv = (lane >> 4) & 1;
  LAS char* wimg = smem + AQ_PK + ((lane >> 4) & 1) * 4096 + rw * 1024 + h * 512 + (lane & 15) * 16;
  LAS char* rimg = smem;
  f32x16 acc[3];
  f32x4 dd;
  unsigned prev[3][8], fpoll = 0, l16 = 0;
  bf16x8 wf[5], ka[4], va[4];
  float pa[2], pe[2], pt[2], pm[2], pl = 0.f;
  auto rdw = [&](auto E_) {
    constexpr int E = decltype(E_)::value, o = (Q::stage(E) & 3) * AQ_SLOT + (Q::ks(E) & 3) * 5120 + (Q::t0(Q::round(E)) + Q::tile(E)) * 64;
    wf[E % 5] = tr_frag(rd + o, rd + o + 1280);
  };
  auto topk = [&](auto T_) {
    constexpr int t = decltype(T_)::value;
#pragma unroll
    for (int i = 0; i < 8; ++i) { prev[t][i] = cvt_pk(acc[t][2 * i], acc[t][2 * i + 1]); PIN(prev[t][i]); }
  };
  FINE_DECL();
  FINE(0);
  sfor<0, 4>(rdw);
  sfor<0, Q::NE>([&](auto E_) {
    constexpr int E = decltype(E_)::value, R = Q::round(E), ks = Q::ks(E), t = Q::tile(E), EN = E + 4, er = E - Q::base(R);
    if constexpr (EN < Q::NE) {
      if constexpr (Q::pos(EN) == 0) {
        wait_v<Q::younger(E)>();
        if (Q::stage(EN) == 20) asm volatile("s_waitcnt lgkmcnt(2)" ::: "memory");
        if (Q::stage(EN) == 1) WAIT_L0();
        BAR(); FINE(Q::stage(EN));
        if constexpr (G == 0 && Q::stage(EN) == 26) {
          const int ln = lane_id();
          ST4(ws, (wid == 0 && ln < 4) ? (quad * 4 + ln) * 128 + p * 4 : 0x7fffff00u, WS_F2K, X.tag);
        }
      }
      rdw(IC<EN>{});
      if constexpr (E < 20) xf[16 + E] = XRD(16 + E);
      if constexpr (Q::dma(E)) dma(IC<Q::stage(EN) + 2>{}, IC<Q::dma(E) - 1>{});
    }
    if constexpr (R >= 1 && er < Q::nt(R - 1)) topk(IC<er>{});
    const f32x16 zz = {};
    if constexpr (R == 2) acc[t] = MFMA32(wf[E % 5], xf[ks], ks ? acc[t] : zz);
    else acc[t] = MFMA32(xf[ks], wf[E % 5], ks ? acc[t] : zz);
    PIN(acc[t]);
    if constexpr (R == 1) {
      sfor<0, 16 * Q::nt(0)>([&](auto L_) {
        constexpr int el = decltype(L_)::value, tl = el / 16, w = (el & 15) / 2, b = el & 1;
        if constexpr (E == Q::ent(el) + 2) {
          const float r = pa[b] + pe[b];
          constexpr bool mixed = Q::t0(0) + tl == 4;
          if constexpr (b == 0) { pl = r; PIN(pl); }
          else { const unsigned o = cvt_pk(pl, r); prev[tl][w] = (mixed && isv) ? prev[tl][w] : o; PIN(prev[tl][w]); }
          if constexpr ((el & 7) == 7)
            *(LAS u32x4*)(wimg + (mixed && isv ? AQ_V - AQ_PK - 4096 : 0) + (Q::t0(0) + tl) * 8192 + (w >> 2) * 256) =
                (u32x4){prev[tl][w - 3], prev[tl][w - 2], prev[tl][w - 1], prev[tl][w]};
        }
        if constexpr (E == Q::ent(el) + 1) {
          pe[b] = __builtin_amdgcn_fmed3f(__builtin_amdgcn_exp2f(pm[b]), 0.f, 1.f); pa[b] = __builtin_fmaxf(pt[b], 1e-6f); PIN(pe[b]); PIN(pa[b]);
        }
        if constexpr (E == Q::ent(el)) { pt[b] = b ? bf_hi(prev[tl][w]) : bf_lo(prev[tl][w]); pm[b] = pt[b] * 1.4426950409f; PIN(pt[b]); PIN(pm[b]); }
      });
    }
    if constexpr (R == 2 && er >= 3 && er < 3 + 2 * Q::nt(1)) {
      constexpr int tl = (er - 3) / 2, cc = (er - 3) & 1;
      *(LAS u32x4*)(wimg + (AQ_V - AQ_PK) + (Q::t0(1) + tl) * 8192 + cc * 256) =
          (u32x4){prev[tl][4 * cc], prev[tl][4 * cc + 1], prev[tl][4 * cc + 2], prev[tl][4 * cc + 3]};
    }
    if constexpr (E >= Q::KV0 && E < Q::KV0 + Q::NU * Q::UL) {
      constexpr int un = (E - Q::KV0) / Q::UL, ph = (E - Q::KV0) % Q::UL, it = un / 4, dj = un % 4;
      const int i = it ? 8 : wid;
      if constexpr (ph == 0) {
        if constexpr (un == 0) { l16 = lane_id() * 16; rimg = smem + AQ_PK + l16; }
        if constexpr (dj == 0) {
#pragma unroll
          for (int k = 0; k < 4; ++k) ka[k] = *(LAS bf16x8*)(rimg + (i * 4 + k) * 1024);
        } else {
          const int jv = i + dj - 2 < 0 ? 0 : i + dj - 2;
#pragma unroll
          for (int k = 0; k < 4; ++k) va[k] = *(LAS bf16x8*)(rimg + (AQ_V - AQ_PK) + (jv * 4 + k) * 1024);
        }
      }
      if constexpr (ph == Q::UL - 2) {
        const bf16x8 ones = {0x3f80, 0x3f80, 0x3f80, 0x3f80, 0x3f80, 0x3f80, 0x3f80, 0x3f80};
        dd = (f32x4){0, 0, 0, 0};
#pragma unroll
        for (int k = 0; k < 4; ++k) dd = MFMA(ka[k], dj ? va[k] : ones, dd);
        PIN(dd);
      }
      if constexpr (ph == Q::UL - 1)
        ST16(ws, l16 + dj * 1024, (it && wid != 4) ? 0x7ff00000u : WS_KVP + (quad * 16 + p) * 36864 + i * 4096, __builtin_bit_cast(u32x4, dd));
    }
    if constexpr (E == Q::WARM) warm_b(P, X, smem);
    if constexpr (EN < Q::NE && Q::stage(EN) == 26 && Q::pos(EN) == 1) { ald4(fpoll, rsws, f1_off(X), 0); first_w(P, X, smem); }
    SB0();
  });
  FINE(27); FINE_OUT(28);
  STAMP(0, 9);
  wait_v<4>(); PIN(fpoll);
  if (!all_eq(fpoll, X.tag)) spin_flag(P.ws, f1_off(X), X.tag);
  first_z(P, X, smem);
  lane = lane_id();
  const unsigned qo = ((lane >> 4) & 1) * 18432 + (lane >> 5) * 128 + (lane & 15) * 8;
  int nst = 0;
#pragma unroll
  for (int t = 0; t < Q::nt(2); ++t)
#pragma unroll
    for (int a = 0; a < 4; ++a)
      if ((Q::t0(2) + t) * 8 + 2 * a < 36)
        ST8(ws, qo + ((Q::t0(2) + t) * 8 + 2 * a) * 128, WS_PQ + (p * 8 + rw * 2) * 18432 + quad * 4608,
            ((u32x2){cvt_pk(acc[t][4 * a], acc[t][4 * a + 1]), cvt_pk(acc[t][4 * a + 2], acc[t][4 * a + 3])}));
  wait_v<(G ? 6 : 12)>();
}

template <int ROLE>
DI void bc_loader(const Params& P, const Ctx& X, LAS char* smem) {
  constexpr int CNT = ROLE ? 16 : 18;
  __builtin_amdgcn_s_setprio(3);
  Loader<ROLE> L; L.init(P, X, smem);
  const int lane = lane_id(), p = X.p, c = X.c;
  BAR(); L.issue_b(2);
  BAR(); L.issue_b(3);
#pragma unroll 1
  for (int n = 2; n < 30; ++n) { wait_v<CNT>(); BAR(); L.issue_b(n + 2); }
  wait_v<CNT>(); BAR(); wait_v<0>(); BAR();
  if (ROLE == 0) spin_flag(P.ws, WS_F2Z + ((c & 7) * 2 + (lane >> 5)) * 128 + (16 + ((lane & 31) < 12 ? lane & 31 : 0)) * 4, X.tag);
  else spin_flag(P.ws, WS_F3O + p * 128 + (lane & 15) * 4, X.tag);
  L.template issue_c<2>(32); L.template issue_c<4>(33); L.template issue_c<4>(34);
  int n = 32;
  if (ROLE == 1) {
    wait_v<2 * CNT>(); BAR(); L.template issue_c<4>(35);
    wait_v<2 * CNT>(); BAR(); L.template issue_c<4>(36);
    wait_v<2 * CNT>(); BAR(); wait_v<CNT>(); BAR(); wait_v<0>(); BAR();
    STAMP(2, 5);
    spin_flag(P.ws, WS_F4 + (2 * p + (c >> 3)) * 128 + (lane & 31) * 4, X.tag);
    STAMP(2, 6);
    L.template issue_c<4>(37); L.template issue_c<4>(38); L.template issue_c<4>(39);
    n = 37;
  }
#pragma unroll 1
  for (; n < 52; ++n) { wait_v<2 * CNT>(); BAR(); L.template issue_c<4>(n + 3); }
  wait_v<2 * CNT>(); BAR(); wait_v<CNT>(); BAR(); wait_v<0>(); BAR();
}

DI void bc_side(const Params& P, const Ctx& X, LAS char* smem) {
  __builtin_amdgcn_s_setprio(2);
  const brs_t ws = __builtin_amdgcn_make_buffer_rsrc(P.ws, 0, WS_END, 0x00020000);
  const int lane = lane_id();
  const int sw = X.wid - 6, lt = sw * 64 + lane, p = X.p, c = X.c, kq = lane >> 4, r16 = lane & 15;
  u32x4 part[3]; u32x2 qv[4][2], qx; int dst[6], src[6]; bf16x8 kvf[4][2]; float mine = 0.f;
  const unsigned oob = 0x7fffff00u;
  const unsigned f2k = WS_F2K + c * 128 + r16 * 4, f2z = WS_F2Z + p * 128 + (kq < 3 ? f1_src(c, kq) : 12 + (c >> 2)) * 4;
  unsigned fl = 0;
  auto kvf_ld = [&]() {
#pragma unroll
    for (int i = 0; i < 3; ++i) part[i] = LD16(ws, (lt + 128 * i < 352 ? lt + 128 * i : 351) * 16, WS_KVF + c * 5632);
  };
  auto phi2 = [&](u32x2& v) {
    v = (u32x2){cvt_pk(phi_f(bf_lo(v.x)), phi_f(bf_hi(v.x))), cvt_pk(phi_f(bf_lo(v.y)), phi_f(bf_hi(v.y)))};
    PIN(v);
  };
  sfor<0, 32>([&](auto N_) {
    constexpr int N = decltype(N_)::value;
    if constexpr (N == 4) wait_v<6>();
    if constexpr (N == 26) wait_v<0>();
    if constexpr (N == 9 || N == 15) WAIT_L0();
    BAR();
    if constexpr (N == 22) { if (sw == 1) warm_code(X, smem); }
    if constexpr (N == 1) fl = flag_ld(P.ws, f2k);
    if constexpr (N < 3) {
#pragma unroll
      for (int i = 2 * N; i < 2 * N + 2; ++i) {
        const int l = (lane & 3) + 4 * i, id0 = p * 44 + sw * 22 + (l < 22 ? l : 21), id = id0 < 684 ? id0 : 683;
        const int hh = id >= 342, rem = id - hh * 342, e = (rem * 3641) >> 16, d = rem - e * 18;
        const int kc = 36 * (c & 3) + 18 * hh + d, vc = kc - d + e;
        src[i] = (lane >> 2) * 36864 + (kc >> 4) * 4096 + (e == 18 ? 0 : ((vc >> 4) - (kc >> 4) + 2) * 1024 + (vc & 15) * 16) + ((kc >> 2) & 3) * 256 + (kc & 3) * 4;
        PIN(src[i]);
      }
    }
    if constexpr (N == 4 || N == 5) {
      const int r = sw * 2 + N - 4, kb = ((X.pa & 1) * 12 + X.quad * 3 + X.jj) * 4 + r;
      {
        LAS char* a = smem + B_KV + r * 5120 + (8 * kq + ((lane >> 2) & 3)) * 144;
        const unsigned so = (X.jj < 3 && kb < 90) ? WS_WX + ((X.pa >> 1) * 90 + kb) * 4608 : 0x7ff00000u;
#pragma unroll
        for (int dt = 0; dt < 4; ++dt) {
          LAS char* b = a + (dt >> 1) * 64 + (lane & 3) * 16 + (dt & 1) * 8;
          ST16(ws, lane * 16 + dt * 1024, so, __builtin_bit_cast(u32x4, tr_frag(b, b + 576)));
        }
        LAS char* b = a + 128 + (lane & 1) * 8;
        ST16(ws, r16 < 8 ? 4096 + kq * 128 + r16 * 16 : 0x7ff00000u, so, __builtin_bit_cast(u32x4, tr_frag(b, b + 576)));
      }
    }
    if constexpr (N == 8) {
#pragma unroll
      for (int i = 0; i < 4; ++i) *(LAS u32x4*)(smem + B_KV + i * 2048 + lt * 16) = (u32x4){0, 0, 0, 0};
    }
    if constexpr (N == 7 || N == 8 || N == 11) {
      constexpr int e0 = N == 11 ? 4 : 2 * (N - 7);
#pragma unroll
      for (int e6 = e0; e6 < e0 + 2; ++e6) {
        const int id = (lt + 128 * (e6 >> 1)) * 2 + (e6 & 1), hh = id >= 342, rem = id - hh * 342, e = (rem * 3641) >> 16, d = rem - e * 18;
        const int k = 18 * hh + d, n = 18 * hh + e;
        const int kof = (k >> 5) * 1024 + ((k >> 3) & 3) * 256 + (k & 7) * 2;
        dst[e6] = id >= 684 ? 8192 + lane * 2 : (e < 18 ? (n >> 4) * 2048 + (n & 15) * 16 : 6144 + hh * 16) + kof;
        PIN(dst[e6]);
      }
    }
    if constexpr (N == 4) {
      ST4(ws, lt == 0 ? 0u : oob, WS_F2Z + X.pa * 128 + (X.jj < 3 ? X.quad * 3 + X.jj : 12 + X.quad) * 4, X.tag);
    }
    if constexpr (N == 7) fl = flag_ld(P.ws, f2z);
    if constexpr (N == 3) {
      if (!all_eq(fl, X.tag)) spin_flag(P.ws, f2k, X.tag);
#pragma unroll
      for (int i = 0; i < 6; ++i) src[i] = __builtin_amdgcn_raw_buffer_load_b32(ws, src[i], WS_KVP + (c >> 2) * 16 * 36864, 16);
    }
    if constexpr (N == 6) {
#pragma unroll
      for (int i = 0; i < 6; ++i) {
        float v = __int_as_float(src[i]);
        v += dpp_get<0x124>(v); v += dpp_get<0x128>(v);
        auto a = __builtin_amdgcn_permlane16_swap(__float_as_uint(v), __float_as_uint(v), false, false);
        v = __uint_as_float(a[0]) + __uint_as_float(a[1]);
        auto b = __builtin_amdgcn_permlane32_swap(__float_as_uint(v), __float_as_uint(v), false, false);
        v = __uint_as_float(b[0]) + __uint_as_float(b[1]);
        mine = (lane >> 2) == i ? v : mine;
      }
      ST8(ws, lane < 22 ? (p * 44 + sw * 22 + lane) * 8 : oob, WS_KVF + c * 5632, ((u32x2){__float_as_uint(mine), X.tag}));
    }
    if constexpr (N == 9) {
      if (!all_eq(fl, X.tag)) spin_flag(P.ws, f2z, X.tag);
      qx = LD8(ws, kq * 18432 + 1024 + r16 * 8, WS_PQ + (p * 8 + sw * 4) * 18432 + c * 1152);
    }
    if constexpr (N == 9 || N == 10) {
#pragma unroll
      for (int tt = 2 * (N - 9); tt < 2 * (N - 8); ++tt) {
        const unsigned so = WS_PQ + (p * 8 + sw * 4 + tt) * 18432 + c * 1152;
        qv[tt][0] = LD8(ws, kq * 256 + r16 * 8, so); qv[tt][1] = LD8(ws, kq * 256 + 128 + r16 * 8, so);
      }
    }
    if constexpr (N == 11) kvf_ld();
    if constexpr (N == 12) { phi2(qx); phi2(qv[0][0]); }
    if constexpr (N == 13) { phi2(qv[0][1]); phi2(qv[1][0]); }
    if constexpr (N == 15) phi2(qv[1][1]);
    if constexpr (N == 16 || N == 17) { phi2(qv[N - 14][0]); phi2(qv[N - 14][1]); }
    if constexpr (N == 14) {
#pragma unroll 1
      for (unsigned spins = 0; spins < SPIN_MAX; ++spins) {
        bool ok = true;
#pragma unroll
        for (int i = 0; i < 3; ++i) ok &= part[i].y == X.tag && part[i].w == X.tag;
        if (__builtin_amdgcn_ballot_w64(!ok) == 0) break;
        __builtin_amdgcn_s_sleep(1);
        asm volatile("" ::: "memory");
        kvf_ld();
      }
#pragma unroll
      for (int e6 = 0; e6 < 6; ++e6) {
        const unsigned u = part[e6 >> 1][(e6 & 1) * 2];
        *(LAS u16*)(smem + B_KV + dst[e6]) = (u16)(cvt_pk(__uint_as_float(u), 0.f) & 0xffffu);
      }
    }
    if constexpr (N == 15) {
#pragma unroll
      for (int nt = 0; nt < 4; ++nt) {
        LAS char* kb = smem + B_KV + nt * 2048 + (nt < 3 ? r16 : r16 & 1) * 16;
        kvf[nt][0] = *(LAS bf16x8*)(kb + kq * 256); kvf[nt][1] = *(LAS bf16x8*)(kb + 1024);
      }
    }
    if constexpr (N >= 18 && N <= 21) {
      constexpr int tt = N - 18;
      const bf16x8 q0 = __builtin_bit_cast(bf16x8, ((u32x4){qv[tt][0].x, qv[tt][0].y, qv[tt][1].x, qv[tt][1].y}));
      const bf16x8 q1 = __builtin_bit_cast(bf16x8, ((u32x4){kq == tt ? qx.x : 0u, kq == tt ? qx.y : 0u, 0u, 0u}));
      const f32x4 z = {0, 0, 0, 0};
      const f32x4 den = MFMA(kvf[3][1], q1, MFMA(kvf[3][0], q0, z));
      f32x4 num[3];
#pragma unroll
      for (int nt = 0; nt < 3; ++nt) num[nt] = MFMA(kvf[nt][1], q1, MFMA(kvf[nt][0], q0, z));
      const float r0 = __builtin_amdgcn_rcpf(den[0] + 1e-6f), r1 = __builtin_amdgcn_rcpf(den[1] + 1e-6f), rm = kq == 0 ? r0 : r1;
      const unsigned so = WS_HX + (p * 8 + sw * 4 + tt) * (90 * 1024);
      ST16(ws, lane * 16, so + (72 + c) * 1024, ((u32x4){cvt_pk(num[0][0] * r0, num[0][1] * r0), cvt_pk(num[0][2] * r0, num[0][3] * r0),
                                                         cvt_pk(num[1][0] * rm, num[1][1] * rm), cvt_pk(num[1][2] * r1, num[1][3] * r1)}));
      ST8(ws, kq == 0 ? r16 * 16 : oob, so + (88 + (c >> 3)) * 1024 + ((c & 7) >> 1) * 256 + (c & 1) * 8,
          ((u32x2){cvt_pk(num[2][0] * r1, num[2][1] * r1), cvt_pk(num[2][2] * r1, num[2][3] * r1)}));
    }
    if constexpr (N == 26) {
      if (lt == 0) flag_st(P.ws, WS_F3O + p * 128 + c * 4, X.tag);
      if (lt == 64 && X.jj < 3) flag_st(P.ws, WS_F2Z + X.pa * 128 + (16 + X.quad * 3 + X.jj) * 4, X.tag);
    }
    SB0();
  });
  if (X.bid == 0 && sw == 0) {
#pragma unroll 1
    for (int n = 32; n < 42; ++n) BAR();
    unsigned f4[4];
#pragma unroll
    for (int i = 0; i < 4; ++i) f4[i] = flag_ld(P.ws, WS_F3O + (i * 4 + kq) * 128 + r16 * 4);
    BAR(); BAR(); BAR();
    if (!(all_eq(f4[0], X.tag) && all_eq(f4[1], X.tag) && all_eq(f4[2], X.tag) && all_eq(f4[3], X.tag))) {
#pragma unroll 1
      for (int i = 0; i < 4; ++i) spin_flag(P.ws, WS_F3O + (i * 4 + kq) * 128 + r16 * 4, X.tag);
    }
    if (lane == 0) flag_st(P.ws, WS_EPOCH, X.tag);
  }
}

template <int DH>
DI void c_compute(const Params& P, const Ctx& X, LAS char* smem) {
  constexpr int ND = DH ? 2 : 3;
  int lane = lane_id();
  const int r16 = lane & 15, kq = lane >> 4, th = X.wid & 1;
  const int rg = 2 * X.p + (X.c >> 3), cg = X.c & 7;
  f32x4 acc[ND][2];
#pragma unroll
  for (int a = 0; a < ND; ++a) { acc[a][0] = (f32x4){0, 0, 0, 0}; acc[a][1] = (f32x4){0, 0, 0, 0}; }
  const int woff = DH * 2048 + lane * 16, woff4 = 4096 + kq * 128 + (r16 & 7) * 16;
  const int hoff = 18432 + th * 8192 + lane * 16;
  bf16x8 wa[2][ND], ha[2][2], wb[2][ND], hb[2][2];
#define C_RD(W, H, slot, kp) do { LAS char* b_ = smem + SLOT(slot); \
    _Pragma("unroll") for (int k = 0; k < 2; ++k) { \
      H[k][0] = *(LAS bf16x8*)(b_ + hoff + ((kp) * 2 + k) * 1024); H[k][1] = *(LAS bf16x8*)(b_ + hoff + 4096 + ((kp) * 2 + k) * 1024); \
      W[k][0] = *(LAS bf16x8*)(b_ + woff + ((kp) * 2 + k) * 4608); W[k][1] = *(LAS bf16x8*)(b_ + woff + 1024 + ((kp) * 2 + k) * 4608); \
      if (ND == 3) W[k][ND - 1] = *(LAS bf16x8*)(b_ + woff4 + ((kp) * 2 + k) * 4608); } } while (0)
#define C_MM(W, H) do { _Pragma("unroll") for (int k = 0; k < 2; ++k) _Pragma("unroll") for (int i = 0; i < ND; ++i) { \
    acc[i][0] = MFMA(W[k][i], H[k][0], acc[i][0]); acc[i][1] = MFMA(W[k][i], H[k][1], acc[i][1]); } } while (0)
#define C_ILV() do { _Pragma("unroll") for (int i_ = 0; i_ < ND + 2; ++i_) { SGB(0x100, 2); SGB(0x8, 1); } SGB(0x8, 3 * ND - 2); } while (0)
#define C_STAGE(n) do { WAIT_L0(); BAR(); const int slot = (n) & 3; \
    C_RD(wa, ha, slot, 0); C_MM(wb, hb); C_ILV(); SB0(); \
    C_RD(wb, hb, slot, 1); C_MM(wa, ha); C_ILV(); SB0(); } while (0)
  BAR();
  C_RD(wb, hb, 0, 0);
  C_STAGE(33);
  wait_v<0>();
  if (lane == 0) flag_st(P.ws, WS_F4 + (X.p * 2 + (X.wid >> 1)) * 128 + (X.c * 2 + (X.wid & 1)) * 4, X.tag);
  STAMP(2, 1);
  u32x4 xr[2]; u32x2 xo[2]; size_t orow[2];
#pragma unroll
  for (int tt = 0; tt < 2; ++tt) {
    orow[tt] = (size_t)(rg * 64 + slot_row(th * 32 + tt * 16 + r16)) * 1152 + cg * 144;
    xr[tt] = *(const u32x4*)((const char*)P.x + orow[tt] + DH * 64 + kq * 16);
    if (DH == 0) xo[tt] = *(const u32x2*)((const char*)P.x + orow[tt] + 128 + (kq & 1) * 8);
  }
  C_STAGE(34);
#pragma unroll 1
  for (int n = 35; n < 55; n += 4) { C_STAGE(3); C_STAGE(0); C_STAGE(1); C_STAGE(2); }
  C_MM(wb, hb);
  SB0();
  STAMP(2, 2);
  const brs_t ro = __builtin_amdgcn_make_buffer_rsrc(P.out, 0, 2048 * 1152, 0x00020000);
#pragma unroll
  for (int tt = 0; tt < 2; ++tt) {
    const u32x4 x = xr[tt]; const f32x4 a0 = acc[0][tt], a1 = acc[1][tt];
    u32x4 v;
    v.x = cvt_pk(bf_lo(x.x) + a0[0], bf_hi(x.x) + a0[1]); v.y = cvt_pk(bf_lo(x.y) + a0[2], bf_hi(x.y) + a0[3]);
    v.z = cvt_pk(bf_lo(x.z) + a1[0], bf_hi(x.z) + a1[1]); v.w = cvt_pk(bf_lo(x.w) + a1[2], bf_hi(x.w) + a1[3]);
    __builtin_amdgcn_raw_buffer_store_b128(v, ro, (int)(orow[tt] + DH * 64 + kq * 16), 0, OUTAUX);
    if (DH == 0) {
      const f32x4 a2 = acc[ND - 1][tt];
      u32x2 w; w.x = cvt_pk(bf_lo(xo[tt].x) + a2[0], bf_hi(xo[tt].x) + a2[1]); w.y = cvt_pk(bf_lo(xo[tt].y) + a2[2], bf_hi(xo[tt].y) + a2[3]);
      __builtin_amdgcn_raw_buffer_store_b64(w, ro, kq < 2 ? (int)(orow[tt] + 128 + kq * 8) : 0x7fffff00, 0, OUTAUX);
    }
  }
  STAMP(2, 3);
}

DI void b_compute(const Params& P, const Ctx& X, LAS char* smem) {
  int lane = lane_id();
  const int wid = X.wid, p = X.p, c = X.c, r16 = lane & 15, kq = lane >> 4;
  const brs_t ws = __builtin_amdgcn_make_buffer_rsrc(P.ws, 0, WS_END, 0x00020000);
  f32x4 acc[9][2];
#pragma unroll
  for (int a = 0; a < 9; ++a) { acc[a][0] = (f32x4){0, 0, 0, 0}; acc[a][1] = (f32x4){0, 0, 0, 0}; }
  const int zoff = lane * 16;
  const int woff0 = 18432 + (wid * 32 + r16) * 128 + ((kq ^ (r16 >> 1)) * 16);
  const int woff1 = 18432 + (wid * 32 + r16) * 128 + (((kq + 4) ^ (r16 >> 1)) * 16);
  bf16x8 za[9], wa[2], zb[9], wb[2];
#define B_RD(Z, W, slot, kb) do { LAS char* b_ = smem + SLOT(slot); \
    W[0] = *(LAS bf16x8*)(b_ + ((kb) ? woff1 : woff0)); W[1] = *(LAS bf16x8*)(b_ + ((kb) ? woff1 : woff0) + 2048); \
    _Pragma("unroll") for (int i = 0; i < 9; ++i) Z[i] = *(LAS bf16x8*)(b_ + zoff + (i < 8 ? (kb) * 8192 + i * 1024 : 16384 + (kb) * 1024)); } while (0)
#define B_MM(Z, W) do { _Pragma("unroll") for (int i = 0; i < 9; ++i) { \
    acc[i][i & 1] = MFMA(Z[i], W[i & 1], acc[i][i & 1]); acc[i][(i & 1) ^ 1] = MFMA(Z[i], W[(i & 1) ^ 1], acc[i][(i & 1) ^ 1]); } } while (0)
#define B_ILV() do { _Pragma("unroll") for (int i_ = 0; i_ < 11; ++i_) { SGB(0x8, 1); SGB(0x100, 1); } SGB(0x8, 7); } while (0)
#define B_STAGE(n) do { WAIT_L0(); BAR(); const int slot = (n) & 3; \
    B_RD(za, wa, slot, 0); B_MM(zb, wb); B_ILV(); SB0(); \
    B_RD(zb, wb, slot, 1); B_MM(za, wa); B_ILV(); SB0(); } while (0)
  BAR();
  STAMP(1, 1);
  B_RD(za, wa, 0, 0); SB0();
  B_RD(zb, wb, 0, 1); B_MM(za, wa); B_ILV(); SB0();
  B_STAGE(1);
#pragma unroll 1
  for (int n = 2; n < 18; n += 4) {
    B_STAGE(2); B_STAGE(3);
    if (n == 2) wait_v<0>();
    B_STAGE(0); B_STAGE(1);
  }
  u32x4 z1[2][4]; u32x2 z1o[2]; u16 bsv[2];
  sfor<18, 32>([&](auto N_) {
    constexpr int N = decltype(N_)::value, tt = (N - 18) / 6, m = (N - 18) % 6;
    if constexpr (N < 30) {
      const unsigned so = WS_Z1 + (p * 8 + wid * 2 + tt) * (72 * 1024);
      if constexpr (m < 4) z1[tt][m] = LD16(ws, zoff + m * 1024, so + c * 4096);
      else if constexpr (m == 4) z1o[tt] = LD8(ws, zoff, so + (64 + (c >> 1)) * 1024 + (c & 1) * 8);
      else bsv[tt] = P.bsp[p * 128 + slot_row(wid * 32 + tt * 16 + r16)];
      SB0();
    }
    B_STAGE(N);
  });
  B_MM(zb, wb);
  SB0();
  STAMP(1, 2);
#pragma unroll
  for (int tt = 0; tt < 2; ++tt) {
    const float b = bf_lo(bsv[tt]) * (1.0f / GC);
    const unsigned so = WS_HX + (p * 8 + wid * 2 + tt) * (90 * 1024);
#pragma unroll
    for (int m = 0; m < 4; ++m) {
      const u32x4 z = z1[tt][m]; const f32x4 g0 = acc[2 * m][tt] * (1.0f / GC) + b, g1 = acc[2 * m + 1][tt] * (1.0f / GC) + b;
      u32x4 v;
      v.x = cvt_pk(bf_lo(z.x) * g0[0], bf_hi(z.x) * g0[1]); v.y = cvt_pk(bf_lo(z.y) * g0[2], bf_hi(z.y) * g0[3]);
      v.z = cvt_pk(bf_lo(z.z) * g1[0], bf_hi(z.z) * g1[1]); v.w = cvt_pk(bf_lo(z.w) * g1[2], bf_hi(z.w) * g1[3]);
      ST16(ws, lane * 16 + m * 1024, so + c * 4096, v);
    }
    const u32x2 z = z1o[tt]; const f32x4 g0 = acc[8][tt] * (1.0f / GC) + b;
    u32x2 v; v.x = cvt_pk(bf_lo(z.x) * g0[0], bf_hi(z.x) * g0[1]); v.y = cvt_pk(bf_lo(z.y) * g0[2], bf_hi(z.y) * g0[3]);
    ST8(ws, (kq >> 1) * 256 + r16 * 16 + (kq & 1) * 8, so + (64 + (c >> 1)) * 1024 + (c & 1) * 512, v);
  }
  STAMP(1, 3);
}

template <int TY>
DI void a_prologue(const Params& P, const Ctx& X, LAS char* smem, bf16x8 (&xf)[36], LAS char* (&xb)[4]) {
  const int wid = X.wid, lane = lane_id(), idx = X.bid >> 3;
  const int sl0 = ((wid + 2 * X.jj) & 7) * 16 + (lane >> 3);
  u32x4 xr[2][9];
#pragma unroll
  for (int ln = 0; ln < 9; ++ln)
#pragma unroll
    for (int hh = 0; hh < 2; ++hh)
      xr[hh][ln] = *(const u32x4*)((const char*)P.x + (size_t)(X.pa * 128 + slot_row(sl0 + hh * 8)) * 1152 + (lane & 7) * 16 + ln * 128);
  SB0();
  unsigned rlo[2], rhi[2]; float sh_[2];
  {
    const bf16x2_t one = {(__bf16)1.0f, (__bf16)1.0f};
    float s[2] = {0.f, 0.f}, q[2] = {0.f, 0.f};
#pragma unroll
    for (int ln = 0; ln < 9; ++ln)
#pragma unroll
      for (int i = 0; i < 4; ++i)
#pragma unroll
        for (int hh = 0; hh < 2; ++hh) {
          const unsigned ua = xr[hh][ln][i];
          const bf16x2_t a = __builtin_bit_cast(bf16x2_t, ua);
          s[hh] = __builtin_amdgcn_fdot2_f32_bf16(a, one, s[hh], false); q[hh] = __builtin_amdgcn_fdot2_f32_bf16(a, a, q[hh], false);
        }
#pragma unroll
    for (int hh = 0; hh < 2; ++hh) {
      const float m = oct_sum(s[hh]) * (1.0f / 576.0f);
      const float var = __builtin_fmaxf(oct_sum(q[hh]) * (1.0f / 576.0f) - m * m, 0.f);
      unsigned r = cvt_pk(__builtin_amdgcn_rsqf(var + 1e-5f) * (TY == 0 ? GC : 1.0f), 0.f);
      sh_[hh] = -m * bf_lo(r);
      unsigned r2 = r << 16;
      PIN(r); PIN(r2); PIN(sh_[hh]);
      rlo[hh] = r; rhi[hh] = r2;
    }
  }
  STAMP(0, 1);
  if (wid == 3) warm_code(X, smem);
  SB0();
#pragma unroll
  for (int i = 0; i < 4; ++i) xb[i] = smem + A_XN + ((wid & 3) * 32 + (lane & 31)) * 128 + (((2 * i + (lane >> 5)) ^ ((lane >> 1) & 7)) * 16);
#pragma unroll
  for (int ln = 0; ln < 9; ++ln) {
    if (ln == 4) {
      WAIT_L0(); BAR();
#pragma unroll
      for (int ks = 0; ks < 16; ++ks) xf[ks] = XRD(ks);
      SB0();
    }
    if (ln == 6) {
      asm volatile("s_waitcnt lgkmcnt(4)" ::: "memory"); BAR();
      if (TY == 0) {
        const rsrc_t rsw = MKRS(P.win, 576 * 9216);
        const unsigned voff = z_voff(wid, lane);
#pragma unroll
        for (int i = 0; i < 9; ++i) BLDS(rsw, voff, (2304 + (X.quad * 3 + X.jj) * 192) * 2 + i * 64 * 9216, smem + i * 8192 + wid * 1024);
      } else q_first<TY == 2>(P, X, smem);
      SB0();
    }
#pragma unroll
    for (int hh = 0; hh < 2; ++hh) {
      const u32x4 v = norm4(xr[hh][ln], rlo[hh], rhi[hh], sh_[hh]);
      const int sl = sl0 + hh * 8;
      *(LAS u32x4*)(smem + A_XN + ln * 16384 + sl * 128 + (((lane & 7) ^ ((sl >> 1) & 7)) * 16)) = v;
      SB0();
    }
  }
  STAMP(0, 2);
  {
    const int r0 = ((idx >> 2) & 7) * 72, t = wid * 64 + lane, col = (t * 911) >> 16, row = t - col * 72;
    if (TY == 0) TOUCH(MKRS(P.win, 576 * 9216), col < 5 ? row * 9216 + (col < 2 ? 4736 + col * 128 : (col - 2) * 128) : 0x7ff00000u,
                       r0 * 9216 + (X.quad * 3 + X.jj) * 384, smem + L_SCR);
    else {
      TOUCH(MKRS(P.wqkv, 576 * 3456), col < 6 ? row * 3456 + 1152 + (col < 3 ? col * 128 : 1152 + (col - 3) * 128) : 0x7ff00000u,
            r0 * 3456 + X.quad * 256, smem + L_SCR);
      TOUCH(MKRS(P.wqkv, 576 * 3456), col < 3 ? row * 3456 + col * 128 : 0x7ff00000u, r0 * 3456 + X.quad * 256, smem + L_SCR);
    }
  }
  wait_v<(TY == 0 ? 7 : TY == 1 ? 8 : 6)>();
  WAIT_L0(); BAR();
  STAMP(0, 3);
}

__global__ NOPK __launch_bounds__(512, 2) void kF(const Params P) {
  extern __shared__ __attribute__((aligned(16))) char smem_[];
  LAS char* smem = (LAS char*)smem_;
  Ctx X;
  X.wid = __builtin_amdgcn_readfirstlane(threadIdx.x >> 6); X.bid = blockIdx.x;
  const int xcd = X.bid & 7, idx = X.bid >> 3, wid = X.wid, lane = threadIdx.x & 63;
  X.pa = (xcd >> 2) * 8 + (idx >> 2); X.quad = xcd & 3; X.jj = idx & 3;
  X.p = (xcd >> 1) * 4 + (idx >> 3); X.c = (xcd & 1) * 8 + (idx & 7);
  STAMP(0, 0);
  X.tag = *(const unsigned*)(P.ws + WS_EPOCH) + 1u;
  if (X.jj < 3) { bf16x8 xf[36]; LAS char* xb[4]; a_prologue<0>(P, X, smem, xf, xb); a_zblock(P, X, smem, xf, xb); }
  else if (wid < 4) { bf16x8 xf[36]; LAS char* xb[4]; a_prologue<1>(P, X, smem, xf, xb); a_qkvblock<0>(P, X, smem, xf, xb); }
  else { bf16x8 xf[36]; LAS char* xb[4]; a_prologue<2>(P, X, smem, xf, xb); a_qkvblock<1>(P, X, smem, xf, xb); }
  STAMP(0, 11);
  if (wid == 4) { bc_loader<0>(P, X, smem); STAMP_OUT(); __builtin_amdgcn_endpgm(); }
  if (wid == 5) { bc_loader<1>(P, X, smem); STAMP_OUT(); __builtin_amdgcn_endpgm(); }
  if (wid >= 6) { bc_side(P, X, smem); STAMP_OUT(); __builtin_amdgcn_endpgm(); }
  b_compute(P, X, smem);
  if (wid < 2) { c_compute<0>(P, X, smem); STAMP_OUT(); __builtin_amdgcn_endpgm(); }
  c_compute<1>(P, X, smem);
  STAMP_OUT();
}

extern "C" __device__ __attribute__((used, noinline)) void kf_code_end() {}

__global__ void k0(char* ws) { if (threadIdx.x == 0) *(volatile unsigned*)(ws + 512) = (unsigned)__builtin_amdgcn_s_memrealtime(); }
extern "C" void kernel_launch(void* const* d_in, const int* in_sizes, int n_in, void* d_out, int out_size, void* d_ws,
                              size_t ws_size, hipStream_t stream) {
  Params P;
  P.x = (const u16*)d_in[0]; P.wqkv = (const u16*)d_in[1]; P.wo = (const u16*)d_in[2]; P.win = (const u16*)d_in[4];
  P.wsp = (const u16*)d_in[5]; P.bsp = (const u16*)d_in[6]; P.wout = (const u16*)d_in[7];
  P.out = (u16*)d_out; P.ws = (char*)d_ws;
  (void)hipFuncSetAttribute((const void*)kF, hipFuncAttributeMaxDynamicSharedMemorySize, LDS_BYTES);
  k0<<<1, 64, 0, stream>>>(P.ws);
  kF<<<256, 512, LDS_BYTES, stream>>>(P);
}
```

```cpp
#include <hip/hip_runtime.h>
#include <hip/hip_bf16.h>

#if defined(__HIP_DEVICE_COMPILE__)
#define NOPK __attribute__((target("no-packed-fp32-ops")))
#else
#define NOPK
#endif
#define LAS __attribute__((address_space(3)))
#define GAS __attribute__((address_space(1)))
typedef unsigned short u16;
typedef unsigned long long u64;
typedef short bf16x8 __attribute__((ext_vector_type(8)));
typedef short s16x4 __attribute__((ext_vector_type(4)));
typedef float f32x4 __attribute__((ext_vector_type(4)));
typedef float f32x2 __attribute__((ext_vector_type(2)));
typedef float f32x16 __attribute__((ext_vector_type(16)));
typedef unsigned u32x4 __attribute__((ext_vector_type(4)));
typedef unsigned u32x2 __attribute__((ext_vector_type(2)));
typedef __bf16 bf16x2_t __attribute__((ext_vector_type(2)));
typedef __amdgpu_buffer_rsrc_t brs_t;
typedef u32x4 rsrc_t;

#define WAIT_L0() asm volatile("s_waitcnt lgkmcnt(0)" ::: "memory")
#define BAR() do { asm volatile("" ::: "memory"); __builtin_amdgcn_s_barrier(); asm volatile("" ::: "memory"); } while (0)
#define SB0() __builtin_amdgcn_sched_barrier(0)
#define SGB(mask, n) __builtin_amdgcn_sched_group_barrier(mask, n, 0)
#define PIN(x) asm volatile("" : "+v"(x))
#define MFMA(a, b, c) __builtin_amdgcn_mfma_f32_16x16x32_bf16(a, b, c, 0, 0, 0)
#define MFMA32(a, b, c) __builtin_amdgcn_mfma_f32_32x32x16_bf16(a, b, c, 0, 0, 0)
#define DI __device__ __forceinline__
template <int N> DI void wait_v() { asm volatile("s_waitcnt vmcnt(%0)" :: "i"(N) : "memory"); }
template <int V> struct IC { static constexpr int value = V; };
template <int I, int N, class F> DI void sfor(F&& f) {
  if constexpr (I < N) { f(IC<I>{}); sfor<I + 1, N>(f); }
}

DI rsrc_t MKRS(const void* p, unsigned bytes) {
  const u64 a = (u64)p;
  return (rsrc_t){(unsigned)a, (unsigned)(a >> 32) & 0xffffu, bytes, 0x00020000u};
}
DI void BLDS(rsrc_t rs, unsigned voff, unsigned soff, LAS char* l) {
  asm volatile("s_mov_b32 m0, %3\n\ts_nop 0\n\tbuffer_load_dwordx4 %0, %1, %2 offen lds"
               :: "v"(voff), "s"(rs), "s"(soff), "s"((unsigned)(size_t)l) : "memory");
}
template <int N> DI void BLDSx(rsrc_t rs, unsigned voff, unsigned soff, LAS char* l) {
  if (N == 4)
    asm volatile("s_mov_b32 m0, %3\n\ts_nop 0\n\tbuffer_load_dwordx4 %0, %1, %2 offen sc1 lds\n\tbuffer_load_dwordx4 %0, %1, %2 offen offset:1024 sc1 lds\n\t"
                 "buffer_load_dwordx4 %0, %1, %2 offen offset:2048 sc1 lds\n\tbuffer_load_dwordx4 %0, %1, %2 offen offset:3072 sc1 lds"
                 :: "v"(voff), "s"(rs), "s"(soff), "s"((unsigned)(size_t)l) : "memory");
  else if (N == 2)
    asm volatile("s_mov_b32 m0, %3\n\ts_nop 0\n\tbuffer_load_dwordx4 %0, %1, %2 offen sc1 lds\n\tbuffer_load_dwordx4 %0, %1, %2 offen offset:1024 sc1 lds"
                 :: "v"(voff), "s"(rs), "s"(soff), "s"((unsigned)(size_t)l) : "memory");
  else
    asm volatile("s_mov_b32 m0, %3\n\ts_nop 0\n\tbuffer_load_dwordx4 %0, %1, %2 offen sc1 lds"
                 :: "v"(voff), "s"(rs), "s"(soff), "s"((unsigned)(size_t)l) : "memory");
}
DI void BLDS4v(rsrc_t rs, const unsigned (&v)[4], unsigned soff, LAS char* l) {
  asm volatile("s_mov_b32 m0, %6\n\ts_nop 0\n\tbuffer_load_dwordx4 %0, %4, %5 offen lds\n\tbuffer_load_dwordx4 %1, %4, %5 offen offset:1024 lds\n\t"
               "buffer_load_dwordx4 %2, %4, %5 offen offset:2048 lds\n\tbuffer_load_dwordx4 %3, %4, %5 offen offset:3072 lds"
               :: "v"(v[0]), "v"(v[1]), "v"(v[2]), "v"(v[3]), "s"(rs), "s"(soff), "s"((unsigned)(size_t)l) : "memory");
}
template <int OFF> DI void AST16(rsrc_t rs, unsigned voff, unsigned soff, u32x4 v) {
  asm volatile("buffer_store_dwordx4 %0, %1, %2, %3 offen offset:%4 sc1\n\ts_nop 1" :: "v"(v), "v"(voff), "s"(rs), "s"(soff), "i"(OFF) : "memory");
}
DI void TOUCH(rsrc_t rs, unsigned voff, unsigned soff, LAS char* l) {
  asm volatile("s_mov_b32 m0, %3\n\ts_nop 0\n\tbuffer_load_dword %0, %1, %2 offen lds"
               :: "v"(voff), "s"(rs), "s"(soff), "s"((unsigned)(size_t)l) : "memory");
}
DI void ald16(u32x4& d, rsrc_t rs, unsigned voff, unsigned soff) {
  asm volatile("buffer_load_dwordx4 %0, %1, %2, %3 offen sc1" : "=v"(d) : "v"(voff), "s"(rs), "s"(soff) : "memory");
}
DI void ald8(u32x2& d, rsrc_t rs, unsigned voff, unsigned soff) {
  asm volatile("buffer_load_dwordx2 %0, %1, %2, %3 offen sc1" : "=v"(d) : "v"(voff), "s"(rs), "s"(soff) : "memory");
}
DI void ald4(unsigned& d, rsrc_t rs, unsigned voff, unsigned soff) {
  asm volatile("buffer_load_dword %0, %1, %2, %3 offen sc1" : "=v"(d) : "v"(voff), "s"(rs), "s"(soff) : "memory");
}
DI void ald2(unsigned& d, rsrc_t rs, unsigned voff) {
  asm volatile("buffer_load_ushort %0, %1, %2, 0 offen" : "=v"(d) : "v"(voff), "s"(rs) : "memory");
}
#define ST16(rs, voff, soff, v) __builtin_amdgcn_raw_buffer_store_b128(v, rs, (int)((voff) + (soff)), 0, 16)
#define ST8(rs, voff, soff, v) __builtin_amdgcn_raw_buffer_store_b64(v, rs, (int)(voff), (int)(soff), 16)
#define ST4(rs, voff, soff, v) __builtin_amdgcn_raw_buffer_store_b32(v, rs, (int)(voff), (int)(soff), 16)

constexpr unsigned WS_EPOCH = 0;
constexpr unsigned WS_F1 = 1024;
constexpr unsigned WS_F2K = WS_F1 + 4096;
constexpr unsigned WS_F2Z = WS_F2K + 2048;
constexpr unsigned WS_F3O = WS_F2Z + 2048;
constexpr unsigned WS_F3W = WS_F3O + 2048;
constexpr unsigned WS_F4 = WS_F3W + 2048;
constexpr unsigned WS_STAT = 20480;
constexpr unsigned WS_KVP = WS_STAT + 16 * 12 * 256 * 8;
constexpr unsigned WS_KVF = WS_KVP + 64 * 36864;
constexpr unsigned WS_PQ = WS_KVF + 16 * 6144;
constexpr unsigned WS_WX = WS_PQ + 128 * 16 * 1152;
constexpr unsigned WS_Z2N = WS_WX + 8 * 90 * 4608;
constexpr unsigned WS_Z2O = WS_Z2N + 16 * 64 * 8 * 1024;
constexpr unsigned WS_Z1 = WS_Z2O + 64 * 16 * 1024;
constexpr unsigned WS_HX = WS_Z1 + 128 * 72 * 1024;
constexpr unsigned WS_END = WS_HX + 128 * 90 * 1024;
constexpr unsigned SPIN_MAX = 1u << 16;

struct Params {
  const u16* x; const u16* wqkv; const u16* wo; const u16* win; const u16* wsp; const u16* bsp; const u16* wout;
  u16* out; char* ws;
};

DI int lane_id() {
  int l; asm volatile("v_mbcnt_lo_u32_b32 %0, -1, 0\n\tv_mbcnt_hi_u32_b32 %0, -1, %0" : "=v"(l)); return l;
}
DI unsigned cvt_pk(float lo, float hi) {
  f32x2 v = {lo, hi}; bf16x2_t b = __builtin_convertvector(v, bf16x2_t); return __builtin_bit_cast(unsigned, b);
}
DI u32x4 norm4(u32x4 v, unsigned rlo, unsigned rhi, float sh) {
  unsigned a = v.x, b = v.y, c = v.z, d = v.w; float t0, t1, t2, t3;
  asm("v_dot2_f32_bf16 %4, %0, %8, %10\n\tv_dot2_f32_bf16 %0, %0, %9, %10\n\t"
      "v_dot2_f32_bf16 %5, %1, %8, %10\n\tv_dot2_f32_bf16 %1, %1, %9, %10\n\t"
      "v_dot2_f32_bf16 %6, %2, %8, %10\n\tv_dot2_f32_bf16 %2, %2, %9, %10\n\t"
      "v_dot2_f32_bf16 %7, %3, %8, %10\n\tv_dot2_f32_bf16 %3, %3, %9, %10\n\t"
      "v_cvt_pk_bf16_f32 %0, %4, %0\n\tv_cvt_pk_bf16_f32 %1, %5, %1\n\tv_cvt_pk_bf16_f32 %2, %6, %2\n\tv_cvt_pk_bf16_f32 %3, %7, %3"
      : "+v"(a), "+v"(b), "+v"(c), "+v"(d), "=&v"(t0), "=&v"(t1), "=&v"(t2), "=&v"(t3) : "v"(rlo), "v"(rhi), "v"(sh));
  return (u32x4){a, b, c, d};
}
DI float bf_lo(unsigned v) { return __uint_as_float(v << 16); }
DI float bf_hi(unsigned v) { return __uint_as_float(v & 0xffff0000u); }
constexpr float GC = 0.4686686931f, GKK = 4.912229539f;
DI float gelu_s(float x) {
  const float e = __builtin_amdgcn_exp2f(-x * __builtin_fmaf(x, x, GKK));
  return x * __builtin_amdgcn_rcpf(1.0f + e);
}
DI float phi_f(float t) {
  return __builtin_amdgcn_fmed3f(t, 1e-6f, 3.402823466e38f) + __builtin_amdgcn_fmed3f(__builtin_amdgcn_exp2f(t * 1.4426950409f), 0.f, 1.f);
}
DI bf16x8 tr_frag(LAS char* a0, LAS char* a1) {
  const s16x4 lo = __builtin_amdgcn_ds_read_tr16_b64_v4i16((LAS s16x4*)a0);
  const s16x4 hi = __builtin_amdgcn_ds_read_tr16_b64_v4i16((LAS s16x4*)a1);
  return __builtin_shufflevector(lo, hi, 0, 1, 2, 3, 4, 5, 6, 7);
}
template <int CTRL> DI float dpp_get(float v) {
  return __builtin_bit_cast(float, __builtin_amdgcn_update_dpp(0, __builtin_bit_cast(int, v), CTRL, 0xf, 0xf, true));
}
DI float oct_sum(float v) {
  v += dpp_get<0xB1>(v); v += dpp_get<0x4E>(v); v += dpp_get<0x141>(v); return v;
}
DI int slot_row(int rs) { return (rs & ~31) + 16 * ((rs >> 2) & 1) + 4 * ((rs >> 3) & 3) + (rs & 3); }
DI void flag_st(char* ws, unsigned off, unsigned v) {
  __hip_atomic_store((GAS unsigned*)(ws + off), v, __ATOMIC_RELAXED, __HIP_MEMORY_SCOPE_AGENT);
}
DI bool all_eq(unsigned v, unsigned tag) { return __builtin_amdgcn_ballot_w64(v != tag) == 0; }
DI void spin_flag(const char* ws, unsigned off, unsigned tag) {
  const rsrc_t rs = MKRS(ws, WS_END);
#pragma unroll 1
  for (unsigned i = 0; i < SPIN_MAX; ++i) {
    unsigned v;
    asm volatile("buffer_load_dword %0, %1, %2, 0 offen sc1\n\ts_waitcnt vmcnt(0)" : "=&v"(v) : "v"(off), "s"(rs) : "memory");
    if (all_eq(v, tag)) break;
    __builtin_amdgcn_s_sleep(1);
  }
}

constexpr int LDS_BYTES = 160 * 1024;
constexpr int L_SCR = 159 * 1024;
constexpr int A_XN = 63 * 1024;
constexpr int AZ_SLOT = 24 * 1024, AZ_PARK = 96 * 1024, AZ_ST = 144 * 1024, AZ_ST2 = 147 * 1024, AZ_TAB = 151 * 1024;
constexpr int AQ_SLOT = 20 * 1024, AQ_PK = 80 * 1024, AQ_V = 116 * 1024;
constexpr int STG = 34 * 1024, B_KV = 4 * STG, B_Z1 = STG;
#define KOFF(p) (((p) >> 3) * 16)
#define BSTG(n, p, c) ((ROT(n, (p) + (c)) + KOFF(p)) & 31)
#define XLN(ln) (((ln) < 6 ? (ln) : (ln) - 3) * 16384)
#define XRD(ks) (*(LAS bf16x8*)(xb[(ks) & 3] + XLN((ks) >> 2)))
#define SLOT(n) ((((n) + 2) & 3) * STG)
#define ROT(n, r) (((n) & ~3) | (((n) + (r)) & 3))

struct Ctx {
  int pa, quad, jj, p, c, wid, bid;
  unsigned tag;
};

DI void w_voff(unsigned (&voff)[4], int ln) {
  const unsigned b = (16 * ((ln >> 5) & 1) + ((ln >> 3) & 3)) * 4096 + (((ln & 7) ^ (ln >> 4)) * 16);
#pragma unroll
  for (int i = 0; i < 4; ++i) voff[i] = (b ^ ((i & 1) * 64)) + i * (16384 - 1024);
}
template <int ROLE> struct Loader {
  rsrc_t rsws, rsw; unsigned voff[4]; unsigned l16; int p, c; LAS char* smem;
  DI void init(const Params& P, const Ctx& X, LAS char* sm) {
    smem = sm; p = X.p; c = X.c;
    const int ln = lane_id();
    l16 = ln * 16;
    rsws = MKRS(P.ws, WS_END);
    if (ROLE == 1) {
      rsw = MKRS((const char*)P.wsp + (size_t)p * 128 * 4096, 128 * 4096);
      w_voff(voff, ln);
    }
  }
  DI void issue_b(int N) {
    LAS char* d = smem + SLOT(N);
    const int n = BSTG(N, p, c);
    if (ROLE == 0) {
      const unsigned so = WS_Z2N + c * (64 * 8 * 1024) + n * 16384;
#pragma unroll
      for (int i = 0; i < 4; ++i) BLDSx<4>(rsws, l16, so + i * 4096, d + i * 4096);
      BLDSx<1>(rsws, l16, WS_Z2O + (n * 32 + c) * 1024, d + 16384);
    } else {
      BLDSx<1>(rsws, l16, WS_Z2O + (n * 32 + 16 + c) * 1024, d + 17408);
#pragma unroll
      for (int i = 0; i < 4; ++i) BLDS4v(rsw, voff, n * 128 + i * (32 * 4096), d + 18432 + i * 4096);
    }
  }
  template <int NK> DI void issue_c(int N) {
    LAS char* d = smem + SLOT(N);
    const int M = N - 32, rg = 2 * p + (c >> 3), cg = c & 7;
    const int kb = M == 0 ? 88 : M < 5 ? 72 + 4 * ((M - 1 + p + c) & 3) : 4 * (M < 21 ? ROT(M - 5, p + c) : M - 5);
    if (ROLE == 0) {
      const unsigned so = WS_WX + (cg * 90 + kb) * 4608;
#pragma unroll
      for (int i = 0; i < NK; ++i) BLDSx<4>(rsws, l16, so + i * 4096, d + i * 4096);
      BLDSx<NK / 2>(rsws, l16, so + NK * 4096, d + NK * 4096);
    } else {
#pragma unroll
      for (int i = 0; i < 4; ++i) BLDSx<NK>(rsws, l16, WS_HX + ((rg * 4 + i) * 90 + kb) * 1024, d + 18432 + i * 4096);
    }
  }
};
DI void first_w(const Params& P, const Ctx& X, LAS char* smem, const unsigned (&voff)[4]) {
  const int st = X.wid >> 2, g = X.wid & 3;
  BLDS4v(MKRS((const char*)P.wsp + (size_t)X.p * 128 * 4096, 128 * 4096), voff, BSTG(st, X.p, X.c) * 128 + g * (32 * 4096),
         smem + SLOT(st) + 18432 + g * 4096);
}
DI void first_z(const Params& P, const Ctx& X, LAS char* smem) {
  const int st = X.wid >> 2, g = X.wid & 3, n = BSTG(st, X.p, X.c);
  const unsigned l16 = lane_id() * 16;
  const rsrc_t rs = MKRS(P.ws, WS_END);
  BLDSx<4>(rs, l16, WS_Z2N + X.c * (64 * 8 * 1024) + n * 16384 + g * 4096, smem + SLOT(st) + g * 4096);
  BLDSx<1>(rs, l16, WS_Z2O + (n * 32 + (g & 1) * 16 + X.c) * 1024, smem + SLOT(st) + 16384 + (g & 1) * 1024);
}
DI rsrc_t code_rs(bool on) {
  u64 pc; unsigned len;
  asm volatile("s_getpc_b64 %0\n1:\n\ts_mov_b32 %1, 1b-_Z2kF6Params" : "=s"(pc), "=s"(len));
  pc -= len;
  asm volatile("s_mov_b32 %0, kf_code_end-_Z2kF6Params" : "=s"(len));
  return MKRS((const void*)pc, (on && (int)len > 0) ? len : 0u);
}
DI void warm_code(const Ctx& X, LAS char* smem) { TOUCH(code_rs(true), lane_id() * 128, (X.bid >> 3) * 8192, smem + L_SCR); }
DI unsigned f1_off(const Ctx& X) { const int l = lane_id(); return WS_F1 + X.c * 256 + (l < 48 ? l : 47) * 4; }
DI u64 f1_need(int win, int p) { return 0x000300030003ull << ((2 * win + KOFF(p) / 2) & 15); }
DI u64 f1_spin(const Params& P, const Ctx& X, u64 need) {
  const rsrc_t rs = MKRS(P.ws, WS_END);
  const unsigned off = f1_off(X);
  u64 m = 0;
#pragma unroll 1
  for (unsigned i = 0; i < SPIN_MAX; ++i) {
    unsigned v;
    asm volatile("buffer_load_dword %0, %1, %2, 0 offen sc1\n\ts_waitcnt vmcnt(0)" : "=&v"(v) : "v"(off), "s"(rs) : "memory");
    m = __builtin_amdgcn_ballot_w64(v == X.tag);
    if ((m & need) == need) break;
    __builtin_amdgcn_s_sleep(1);
  }
  return m;
}
DI int f1_src(int c, int w) { return (w == 0 ? 8 * c : w == 1 ? 8 * c + 7 : 128 + c) / 12; }

struct ZS {
  static constexpr int XB1 = 128, PUB = 130, WARM = 135, GAT = 149, XB2 = 164, F1 = 194, POLL = 207, FW = 208;
  static constexpr bool sbar(int E) { return E % 12 == 8 && E < 212; }
  static constexpr int wt(int E) { return E == 3 ? 1 : E == 4 ? 6 : E == 15 ? 2 : E == 16 ? 3 : E == 27 ? 4 : E == 28 ? 5 : 0; }
  static constexpr int wsp(int E) { return (E >= 51 && E < 101 && (E - 51) % 24 < 2) ? 1 + (E - 51) / 24 * 2 + (E - 51) % 24 : 0; }
  static constexpr int nrm(int ch) { return XB2 + 6 + 2 * ch + ch / 3; }
  static constexpr bool dma(int E) { return (E + 4) / 12 + 2 < 18 && E % 12 < 3; }
  static constexpr int other(int E) {
    int n = (E == PUB) + (E >= GAT && E < GAT + 3) + (E == F1) + (E == POLL) + (E == WARM) + (wsp(E) > 0) + (wt(E) > 0) + (E == 155 || E == 164);
    for (int ch = 0; ch < 6; ++ch) n += E == nrm(ch) + 1;
    return n;
  }
  static constexpr int younger(int SN) {
    const int last = 12 * SN - 22, bar = 12 * SN - 4;
    int n = 0;
    for (int E = last < 0 ? 0 : last; E < bar; ++E) n += other(E) + ((E > last && dma(E)) ? 1 : 0);
    return n;
  }
  static constexpr int after_gather() {
    int n = 0;
    for (int E = GAT + 3; E < XB2 - 3; ++E) n += other(E) + (dma(E) ? 1 : 0);
    return n;
  }
  static constexpr int after_z2n() {
    int n = 0;
    for (int E = nrm(5) + 2; E < F1; ++E) n += other(E) + (dma(E) ? 1 : 0);
    return n;
  }
};
DI unsigned z_voff(int wid, int lane) {
  const int row = wid * 8 + (lane >> 3);
  return (unsigned)(row * 9216 + (((lane & 7) ^ (((row >> 1) & 1) << 2)) * 16));
}
DI void a_zblock(const Params& P, const Ctx& X, LAS char* smem, bf16x8 (&xf)[36], LAS char* (&xb)[4]) {
  const int wid = X.wid, p = X.pa, j = X.quad * 3 + X.jj;
  int lane = lane_id();
  const int rw = wid & 3, G = wid >> 2;
  const int h = lane >> 5;
  const rsrc_t rsw = MKRS(P.win, 576 * 9216), rsws = MKRS(P.ws, WS_END);
  const brs_t ws = __builtin_amdgcn_make_buffer_rsrc(P.ws, 0, WS_END, 0x00020000);
  const unsigned voff = z_voff(wid, lane);
  const int xl = X.jj < 2 ? 8 * X.quad + 6 + X.jj : 32 + X.quad;
  const int cz2 = (2304 + 3 * j * 64) * 2, cz1 = (2 * X.c - 4) * 128;
#define ZCOL(r) ((r) < 3 ? cz2 + (r) * 128 : (r) == 3 ? xl * 128 : cz1 + (r) * 128)
  auto dma = [&](auto S_, auto I_) {
    constexpr int S = decltype(S_)::value, I = decltype(I_)::value, R = S / 3;
    BLDS(rsw, voff, ZCOL(R) + ((S % 3) * 192 + I * 64) * 9216,
         smem + (S & 3) * AZ_SLOT + I * 8192 + wid * 1024);
  };
  const int rq = (lane >> 2) & 3;
  LAS char* rd = smem + (8 * h + rq) * 128 + ((G ^ (rq >> 1)) * 64) + ((lane >> 4) & 1) * 32 + (lane & 3) * 8;
  LAS char* rdh = rd + 2 * AZ_SLOT; PIN(rdh);
  LAS char* park = smem + AZ_PARK + wid * 2048 + h * 1024 + (lane & 31) * 16; PIN(park);
  LAS char* strd = smem;
  const unsigned zo = WS_Z1 + ((p * 8 + rw * 2) * 72 + 2 * xl + G) * 1024;
  unsigned zv = 0, zv2 = 0, nvo = 0, fpoll = 0, f1v = 0, f1o = 0, gvo = 0, t8o = 0, wv[4], sn_[3];
  LAS char *stw = smem, *str = smem, *tabr = smem;
#define PINS(x) asm volatile("" : "+s"(x))
  const bf16x8 ones = {0x3f80, 0x3f80, 0x3f80, 0x3f80, 0x3f80, 0x3f80, 0x3f80, 0x3f80};
  f32x16 acc[2];
  bf16x8 wf[5], pf[2];
  f32x4 ssum[2], ssq[2];
  unsigned pk[4];
  float gt[8], gb = 0.f, xt[4];
  u32x4 gv[3];
  f32x4 tab[2][4];
  u32x4 nv[2];
  auto rdw = [&](auto E_) {
    constexpr int E = decltype(E_)::value, S = E / 12, o = (S & 1) * AZ_SLOT + (E % 12) * 2048;
    LAS char* b = (S & 2) ? rdh : rd;
    wf[E % 5] = tr_frag(b + o, b + o + 512);
  };
  auto stats_rd = [&](int rr) {
#pragma unroll
    for (int u = 0; u < 2; ++u) pf[u] = tr_frag(strd + rr * 16384 + u * 1024, strd + rr * 16384 + u * 1024 + 64);
  };
  auto stats_mm = [&](auto RR_) {
    constexpr int RR = decltype(RR_)::value;
#pragma unroll
    for (int u = 0; u < 2; ++u) {
      const f32x4 zz = {0, 0, 0, 0};
      ssum[u] = MFMA(ones, pf[u], RR ? ssum[u] : zz);
      ssq[u] = MFMA(pf[u], pf[u], RR ? ssq[u] : zz);
      PIN(ssum[u]); PIN(ssq[u]);
    }
  };
  auto gelu_st = [&](auto RR_, auto EL_, auto ST_) {
    constexpr int RR = decltype(RR_)::value, EL = decltype(EL_)::value, ST = decltype(ST_)::value;
    if constexpr (EL >= 0 && EL < 16) {
      constexpr int reg = RR < 3 ? EL : (EL & 3) + ((EL >> 2) & 1) * 8 + (EL >> 3) * 4;
      const float x = acc[RR & 1][reg];
      float& g = gt[EL & 7];
      if (ST == 0) { g = __builtin_fmaf(x, x, GKK); PIN(g); }
      if (ST == 1) { g = __builtin_amdgcn_exp2f(-x * g); PIN(g); }
      if (ST == 2) { g = __builtin_amdgcn_rcpf(1.0f + g); PIN(g); }
      if (ST == 3) {
        const float y = x * g;
        if (EL & 1) { pk[(EL >> 1) & 3] = cvt_pk(gb, y); PIN(pk[(EL >> 1) & 3]); } else { gb = y; PIN(gb); }
      }
    }
  };
  auto put = [&](auto RR_, auto CC_) {
    constexpr int RR = decltype(RR_)::value, CC = decltype(CC_)::value;
    const u32x4 w = {pk[0], pk[1], pk[2], pk[3]};
    if constexpr (RR < 3) *(LAS u32x4*)(park + RR * 16384 + CC * 512) = w;
    else if constexpr (RR == 3) AST16<CC * 512>(rsws, zv, zo, w);
    else *(LAS u32x4*)(smem + B_Z1 + 16384 + zv2 + CC * 512) = w;
  };
  auto gather = [&](int i) {
    ald16(gv[i], rsws, gvo, WS_STAT + (p * 12 + (wid >> 1) * 3 + i) * 2048);
  };
  sfor<0, 4>(rdw);
  sfor<0, 216>([&](auto E_) {
    constexpr int E = decltype(E_)::value, R = E / 36, ks = E % 36, EN = E + 4, SN = EN / 12;
    if constexpr (E == ZS::XB1 || E == ZS::XB2) asm volatile("s_waitcnt lgkmcnt(4)" ::: "memory");
    if constexpr (E == ZS::F1) wait_v<ZS::after_z2n()>();
    if constexpr (ZS::sbar(E)) wait_v<ZS::younger(SN)>();
    if constexpr (ZS::sbar(E) || E == ZS::XB1 || E == ZS::XB2 || E == ZS::F1) BAR();
    if constexpr (E == ZS::F1) ST4(ws, f1v, WS_F1, X.tag);
    if constexpr (EN < 216) {
      rdw(IC<EN>{});
      if constexpr (E >= 8 && E < 20) xf[16 + E] = XRD(16 + E);
      if constexpr (ZS::dma(E)) dma(IC<SN + 2>{}, IC<EN % 12 - 4>{});
    }
    const f32x16 zz = {};
    if constexpr (R < 3) acc[R & 1] = MFMA32(xf[ks], wf[E % 5], ks ? acc[R & 1] : zz);
    else acc[R & 1] = MFMA32(wf[E % 5], xf[ks], ks ? acc[R & 1] : zz);
    PIN(acc[R & 1]);
    if constexpr ((R == 1 || R == 2 || R == 5) && ks >= 1) {
      gelu_st(IC<R - 1>{}, IC<(ks - 1) / 2>{}, IC<(ks - 1) & 1>{});
      gelu_st(IC<R - 1>{}, IC<(ks - 1) / 2 - 1>{}, IC<((ks - 1) & 1) + 2>{});
      if constexpr (ks == 21) put(IC<R - 1>{}, IC<0>{});
      if constexpr (ks == 34) put(IC<R - 1>{}, IC<1>{});
    }
    if constexpr (R == 3) {
      sfor<0, 4>([&](auto ST_) {
        constexpr int q = ks - 1 - decltype(ST_)::value;
        gelu_st(IC<R - 1>{}, IC<2 * q>{}, ST_); gelu_st(IC<R - 1>{}, IC<(q < 0 ? -1 : 2 * q + 1)>{}, ST_);
      });
      if constexpr (ks == 7) put(IC<R - 1>{}, IC<0>{});
      if constexpr (ks == 11) put(IC<R - 1>{}, IC<1>{});
    }
    if constexpr (R == 4) {
      sfor<0, 4>([&](auto ST_) { gelu_st(IC<R - 1>{}, IC<ks - 1 - decltype(ST_)::value>{}, ST_); });
      if constexpr (ks == 11) put(IC<R - 1>{}, IC<0>{});
      if constexpr (ks == 20) put(IC<R - 1>{}, IC<1>{});
    }
    if constexpr (R == 3) {
      if constexpr (ks == 10 || ks == 12 || ks == 15) stats_mm(IC<(ks - 10) / 2>{});
      if constexpr (ks == 8 || ks == 10 || ks == 12) stats_rd((ks - 8) / 2);
    }
    if constexpr (E == 22) { strd = smem + AZ_PARK + wid * 2048 + ((lane >> 1) & 1) * 512 + (8 * (lane >> 4) + rq) * 16 + (lane & 1) * 8; PIN(strd); }
    if constexpr (E == 30) { str = smem + AZ_ST + rw * 256 + lane * 4; PIN(str); t8o = (rw * 64 + lane) * 8; PIN(t8o); }
    if constexpr (E == 34) { stw = smem + AZ_ST + ((lane >> 4) == rq ? G * 1024 + rw * 256 : 2048) + (lane & 15) * 8; PIN(stw); }
    if constexpr (E == ZS::PUB + 1) {
      zv = ((lane >> 4) & 1) * (72 * 1024) + h * 256 + (lane & 15) * 16; PIN(zv);
      nvo = ((lane >> 4) & 1) * 1024 + h * 512 + (lane & 15) * 16; PIN(nvo);
      gvo = ((wid & 1) * 64 + lane) * 16; PIN(gvo);
    }
    if constexpr (E == ZS::PUB + 2) {
      zv2 = ((rw * 2 + ((lane >> 4) & 1)) * 2 + G) * 1024 + h * 256 + (lane & 15) * 16; PIN(zv2);
      tabr = smem + AZ_TAB + wid * 256 + h * 128; PIN(tabr);
    }
    if constexpr (E == ZS::PUB + 3) { w_voff(wv, lane); PIN(wv[0]); PIN(wv[1]); PIN(wv[2]); PIN(wv[3]); }
    if constexpr (E == ZS::PUB + 4) { f1o = f1_off(X); PIN(f1o); }
    if constexpr (E == ZS::PUB + 6) {
      f1v = (wid == 0 && lane < 48 && f1_src(lane & 15, lane >> 4) == j) ? (lane & 15) * 256 + ((lane >> 4) * 16 + p) * 4 : 0x7fffff00u; PIN(f1v);
    }
    if constexpr (E > ZS::PUB && E <= ZS::PUB + 3) {
      constexpr int rr = E - ZS::PUB - 1;
      const int tg = (3 * j + rr) * 4 + 2 * G, sb = p * 4 + rw;
      sn_[rr] = tg < 128 ? WS_Z2N + (((tg >> 3) * 64 + sb) * 8 + (tg & 7)) * 1024 : WS_Z2O + (sb * 16 + tg - 128) * 1024; PINS(sn_[rr]);
    }
    if constexpr (E == ZS::XB1 - 3) {
#pragma unroll
      for (int u = 0; u < 2; ++u) {
        const float q = (lane & 2) ? ((lane & 1) ? ssq[u][3] : ssq[u][2]) : ((lane & 1) ? ssq[u][1] : ssq[u][0]);
        *(LAS f32x2*)(stw + u * 128) = (f32x2){ssum[u][0], q};
      }
    }
    if constexpr (E == ZS::XB1) { xt[0] = *(LAS float*)str; xt[1] = *(LAS float*)(str + 1024); }
    if constexpr (E == ZS::PUB)
      ST8(ws, t8o, G ? 0x7fffff00u : WS_STAT + (p * 12 + j) * 2048, ((u32x2){__float_as_uint(xt[0] + xt[1]), X.tag}));
    if constexpr (E >= ZS::GAT && E < ZS::GAT + 3) { asm volatile("" ::: "memory"); gather(E - ZS::GAT); asm volatile("" ::: "memory"); }
    if constexpr (E == ZS::XB2 - 3) {
      wait_v<ZS::after_gather()>();
#pragma unroll 1
      for (unsigned spins = 0; spins < SPIN_MAX; ++spins) {
        PIN(gv[0]); PIN(gv[1]); PIN(gv[2]);
        bool ok = true;
#pragma unroll
        for (int i = 0; i < 3; ++i) ok &= gv[i].y == X.tag && gv[i].w == X.tag;
        if (__builtin_amdgcn_ballot_w64(!ok) == 0) break;
        __builtin_amdgcn_s_sleep(1);
        asm volatile("" ::: "memory");
#pragma unroll
        for (int i = 0; i < 3; ++i) gather(i);
        wait_v<0>();
      }
      *(LAS f32x2*)(smem + AZ_ST2 + G * 2048 + t8o) =
          (f32x2){__uint_as_float(gv[0].x) + __uint_as_float(gv[1].x) + __uint_as_float(gv[2].x),
                  __uint_as_float(gv[0].z) + __uint_as_float(gv[1].z) + __uint_as_float(gv[2].z)};
    }
    if constexpr (E == ZS::XB2) {
      LAS float* a = (LAS float*)(str + (AZ_ST2 - AZ_ST));
#pragma unroll
      for (int i = 0; i < 4; ++i) xt[i] = a[i * 256];
    }
    if constexpr (E == ZS::XB2 + 3) {
      const int ln = lane;
      const float v = ((xt[0] + xt[1]) + (xt[2] + xt[3])) * (1.0f / 2304.0f), o = dpp_get<0xB1>(v);
      const float m = (ln & 1) ? o : v, q = (ln & 1) ? v : o;
      const float sc = __builtin_amdgcn_rsqf(__builtin_fmaxf(q - m * m, 0.f) + 1e-5f * GC * GC) * (1.0f / GC);
      *(LAS float*)(str + (AZ_TAB - AZ_ST) + G * 1024) = (ln & 1) ? -m * sc : sc;
    }
    sfor<0, 6>([&](auto CH_) {
      constexpr int ch = decltype(CH_)::value, cc = ch / 3, rr = ch % 3, q0 = ZS::nrm(ch);
      if constexpr (E == q0 || E == q0 + 1) {
        constexpr int hf = E - q0;
#pragma unroll
        for (int i = 2 * hf; i < 2 * hf + 2; ++i) {
          unsigned t = nv[ch & 1][i];
          t = cvt_pk(__builtin_fmaf(bf_lo(t), tab[cc][i][0], tab[cc][i][1]), __builtin_fmaf(bf_hi(t), tab[cc][i][2], tab[cc][i][3]));
          PIN(t); nv[ch & 1][i] = t;
        }
        if constexpr (hf == 1) {
          AST16<cc * 256>(rsws, nvo, sn_[rr], nv[ch & 1]);
        }
      }
      if constexpr (rr == 0 && E == q0 - 2) {
#pragma unroll
        for (int i = 0; i < 4; ++i) tab[cc][i] = *(LAS f32x4*)(tabr + cc * 64 + i * 16);
      }
      if constexpr (E == q0 - 2) nv[ch & 1] = *(LAS u32x4*)(park + rr * 16384 + cc * 512);
    });
    if constexpr (E == ZS::POLL) {
      asm volatile("" ::: "memory");
      ald4(fpoll, rsws, f1o, 0);
    }
    if constexpr (E == ZS::FW) first_w(P, X, smem, wv);
    if constexpr (E == ZS::WARM) TOUCH(code_rs(wid == 7), lane * 128, ((p & 7) * 3 + X.jj) * 8192, smem + L_SCR);
    if constexpr (ZS::wt(E)) {
      constexpr int RT = ZS::wt(E);
      const int r = RT < 6 ? RT : 1 + (p & 7) % 5;
      TOUCH(rsw, voff, ZCOL(r) + (RT < 6 ? p & 7 : 8) * 64 * 9216, smem + L_SCR);
    }
    if constexpr (ZS::wsp(E)) {
      TOUCH(MKRS(P.wsp, 2048 * 4096), voff, p * 64 * 9216 + (j * 6 + ZS::wsp(E) - 1) * 128, smem + L_SCR);
    }
    SB0();
  });
  wait_v<4>(); PIN(fpoll);
  const u64 need = f1_need(0, X.p);
  const bool rdy = (__builtin_amdgcn_ballot_w64(fpoll == X.tag) & need) == need;
  if (rdy) first_z(P, X, smem);
  BAR();
  u32x4 w[2];
#pragma unroll
  for (int cc = 0; cc < 2; ++cc) {
#pragma unroll
    for (int i = 0; i < 4; ++i) {
      const int reg = ((2 * i) & 3) + (i >> 1) * 8 + cc * 4;
      w[cc][i] = cvt_pk(gelu_s(acc[1][reg]), gelu_s(acc[1][reg + 1]));
    }
    PIN(w[cc]);
    if (cc == 0 && !rdy) { f1_spin(P, X, need); first_z(P, X, smem); }
  }
  *(LAS u32x4*)(smem + B_Z1 + zv2) = w[0]; *(LAS u32x4*)(smem + B_Z1 + zv2 + 512) = w[1];
  if (wid < 4) {
    lane = lane_id();
    const int kb0 = ((p & 1) * 12 + j) * 4 + rw, kb = kb0 < 90 ? kb0 : 0;
    const rsrc_t rs = kb < 72 ? MKRS(P.wout, 2304 * 1152) : MKRS(P.wo, 576 * 1152);
    const int r0 = kb < 72 ? 32 * kb : kb < 88 ? 36 * (kb - 72) : 288 * (kb - 88) + 32;
    const int mq = kb < 64 ? 4 : kb < 72 ? 8 : kb < 88 ? 4 : 72, mu = kb < 64 ? 16 : kb < 72 ? 4 : kb < 88 ? 16 : 36;
#pragma unroll
    for (int i = 0; i < 5; ++i) {
      const int pc = i < 4 ? i * 64 + lane : 256 + (lane & 31), k = (pc * 57) >> 9, ch = pc - k * 9;
      BLDS(rs, ((k >> 3) * mq + ((k >> 2) & 1) * mu + (k & 3)) * 1152 + ch * 16, r0 * 1152 + (p >> 1) * 144, smem + B_KV + rw * 5120 + i * 1024);
    }
    wait_v<5>();
  } else {
    wait_v<0>();
  }
  WAIT_L0();
}

template <int G> struct QS {
  static constexpr int nt(int R) { return R == (G ? 0 : 2) ? 3 : 2; }
  static constexpr int t0(int R) { return G ? (R == 1 ? 4 : 5) - nt(R) : 0; }
  static constexpr int base(int R) { return R == 0 ? 0 : base(R - 1) + 36 * nt(R - 1); }
  static constexpr int NE = base(3);
  static constexpr int round(int E) { return E < base(1) ? 0 : E < base(2) ? 1 : 2; }
  static constexpr int ks(int E) { return (E - base(round(E))) / nt(round(E)); }
  static constexpr int tile(int E) { return (E - base(round(E))) % nt(round(E)); }
  static constexpr int stage(int E) { return 9 * round(E) + ks(E) / 4; }
  static constexpr int pos(int E) { return (E - base(round(E))) % (4 * nt(round(E))); }
  static constexpr int ent(int el) { return base(1) + 4 + (G ? el + el / 3 : 2 * el); }
  static constexpr int KV0 = base(2) + 8 * nt(2) - 4;
  static constexpr int NU = G ? 7 : 4, UL = G ? 4 : 6;
  static constexpr int ND = G ? 2 : 3, PRE = 2;
  static constexpr int dma(int E) {
    if (stage(E + 4) == 0) return E < ND ? 1 + E : 0;
    return (E + 4 < NE && pos(E + 4) >= 1 && pos(E + 4) <= 2 * ND && (pos(E + 4) & 1) && stage(E + 4) + 2 < 27) ? 1 + pos(E + 4) / 2 : 0;
  }
  static constexpr int other(int E) { return ((E >= KV0 && E < KV0 + NU * UL && (E - KV0) % UL == UL - 1) ? 1 : 0) + (wt(E) > 0); }
  static constexpr int wt(int E) {
    if (E + 4 >= NE || pos(E + 4) != 7) return 0;
    const int S = stage(E + 4);
    return S < ND ? 1 + S : (S >= 3 && S < 3 + ND) ? 1 + S : (S >= 11 && S < 11 + ND) ? 7 + S - 11 : 0;
  }
  static constexpr int younger(int Eb) {
    const int SN = stage(Eb + 4);
    int n = 0;
    for (int E = Eb - 1; E >= 0; --E) {
      n += other(E);
      if (dma(E)) { if (stage(E + 4) + 2 == SN) return n; n += 1; }
    }
    return n + (PRE - 1 - SN) * ND;
  }
};
template <int G> DI void q_voff(unsigned (&voff)[3], int rw, int lane) {
#pragma unroll
  for (int i = 0; i < QS<G>::ND; ++i) {
    const int pc = (G ? 768 + rw * 128 : rw * 192) + i * 64 + lane, sr = (pc * 3277) >> 16, ch = pc - sr * 20;
    voff[i] = (unsigned)(sr * 3456 + ch * 16 + (ch >= 18 ? 1120 : 0));
  }
}
template <int G> DI void q_first(const Params& P, const Ctx& X, LAS char* smem, int S) {
  const rsrc_t rsw = MKRS(P.wqkv, 576 * 3456);
  const int rw = X.wid & 3;
  unsigned voff[3];
  q_voff<G>(voff, rw, lane_id());
#pragma unroll
  for (int i = 0; i < QS<G>::ND; ++i)
      BLDS(rsw, voff[i], X.quad * 288 + S * 64 * 3456 + 1152, smem + S * AQ_SLOT + (G ? 12288 + rw * 2048 : rw * 3072) + i * 1024);
}
template <int G>
DI void a_qkvblock(const Params& P, const Ctx& X, LAS char* smem, bf16x8 (&xf)[36], LAS char* (&xb)[4]) {
  typedef QS<G> Q;
  const int wid = X.wid, p = X.pa, quad = X.quad;
  int lane = lane_id();
  const int rw = wid & 3, h = lane >> 5;
  const rsrc_t rsw = MKRS(P.wqkv, 576 * 3456), rsws = MKRS(P.ws, WS_END);
  const brs_t ws = __builtin_amdgcn_make_buffer_rsrc(P.ws, 0, WS_END, 0x00020000);
  unsigned voff[3];
  q_voff<G>(voff, rw, lane);
  auto dma = [&](auto S_, auto I_) {
    constexpr int S = decltype(S_)::value, I = decltype(I_)::value;
    if constexpr (S == 9) {
      const unsigned c = voff[I] % 3456u;
      voff[I] -= c >= 1408u ? 1152u : 0u; PIN(voff[I]);
    }
    BLDS(rsw, voff[I], quad * 288 + (S % 9) * 64 * 3456 + ((S / 9 + 1) % 3) * 1152,
         smem + (S & 3) * AQ_SLOT + (G ? 12288 + rw * 2048 : rw * 3072) + I * 1024);
  };
  LAS char* rd = smem + (8 * h + ((lane >> 2) & 3)) * 320 + ((lane >> 4) & 1) * 32 + (lane & 3) * 8;
  LAS char* rdh = rd + 2 * AQ_SLOT; PIN(rdh);
  const bool isv = (lane >> 4) & 1;
  LAS char* wimg = smem + AQ_PK + ((lane >> 4) & 1) * 4096 + rw * 1024 + h * 512 + (lane & 15) * 16;
  LAS char* rimg = smem;
  f32x16 acc[3];
  f32x4 dd;
  unsigned prev[3][8], fpoll = 0, l16 = 0, f1o = 0, wv[4];
  bf16x8 wf[5], ka[4], va[4];
  float pa[2], pe[2], pt[2], pm[2], pl = 0.f;
  auto rdw = [&](auto E_) {
    constexpr int E = decltype(E_)::value, o = (Q::stage(E) & 1) * AQ_SLOT + (Q::ks(E) & 3) * 5120 + (Q::t0(Q::round(E)) + Q::tile(E)) * 64;
    LAS char* b = (Q::stage(E) & 2) ? rdh : rd;
    wf[E % 5] = tr_frag(b + o, b + o + 1280);
  };
  auto topk = [&](auto T_) {
    constexpr int t = decltype(T_)::value;
#pragma unroll
    for (int i = 0; i < 8; ++i) { prev[t][i] = cvt_pk(acc[t][2 * i], acc[t][2 * i + 1]); PIN(prev[t][i]); }
  };
  sfor<0, 4>(rdw);
  sfor<0, Q::NE>([&](auto E_) {
    constexpr int E = decltype(E_)::value, R = Q::round(E), ks = Q::ks(E), t = Q::tile(E), EN = E + 4, er = E - Q::base(R);
    if constexpr (EN < Q::NE) {
      if constexpr (Q::pos(EN) == 0) {
        wait_v<Q::younger(E)>();
        if (Q::stage(EN) == 20) asm volatile("s_waitcnt lgkmcnt(2)" ::: "memory");
        BAR();
        if constexpr (G == 0 && Q::stage(EN) == 26) {
          const int ln = lane_id();
          ST4(ws, (wid == 0 && ln < 4) ? (quad * 4 + ln) * 128 + p * 4 : 0x7fffff00u, WS_F2K, X.tag);
        }
      }
      rdw(IC<EN>{});
      if constexpr (E >= 8 && E < 20) xf[16 + E] = XRD(16 + E);
      if constexpr (Q::dma(E)) dma(IC<Q::stage(EN) + 2>{}, IC<Q::dma(E) - 1>{});
    }
    if constexpr (R >= 1 && er < Q::nt(R - 1)) topk(IC<er>{});
    const f32x16 zz = {};
    if constexpr (R == 2) acc[t] = MFMA32(wf[E % 5], xf[ks], ks ? acc[t] : zz);
    else acc[t] = MFMA32(xf[ks], wf[E % 5], ks ? acc[t] : zz);
    PIN(acc[t]);
    if constexpr (R == 1) {
      sfor<0, 16 * Q::nt(0)>([&](auto L_) {
        constexpr int el = decltype(L_)::value, tl = el / 16, w = (el & 15) / 2, b = el & 1;
        if constexpr (E == Q::ent(el) + 2) {
          const float r = pa[b] + pe[b];
          constexpr bool mixed = Q::t0(0) + tl == 4;
          if constexpr (b == 0) { pl = r; PIN(pl); }
          else { const unsigned o = cvt_pk(pl, r); prev[tl][w] = (mixed && isv) ? prev[tl][w] : o; PIN(prev[tl][w]); }
          if constexpr ((el & 7) == 7)
            *(LAS u32x4*)(wimg + (mixed && isv ? AQ_V - AQ_PK - 4096 : 0) + (Q::t0(0) + tl) * 8192 + (w >> 2) * 256) =
                (u32x4){prev[tl][w - 3], prev[tl][w - 2], prev[tl][w - 1], prev[tl][w]};
        }
        if constexpr (E == Q::ent(el) + 1) {
          pe[b] = __builtin_amdgcn_fmed3f(__builtin_amdgcn_exp2f(pm[b]), 0.f, 1.f); pa[b] = __builtin_amdgcn_fmed3f(pt[b], 1e-6f, 3.402823466e38f); PIN(pe[b]); PIN(pa[b]);
        }
        if constexpr (E == Q::ent(el)) { pt[b] = b ? bf_hi(prev[tl][w]) : bf_lo(prev[tl][w]); pm[b] = pt[b] * 1.4426950409f; PIN(pt[b]); PIN(pm[b]); }
      });
    }
    if constexpr (R == 2 && er >= 3 && er < 3 + 2 * Q::nt(1)) {
      constexpr int tl = (er - 3) / 2, cc = (er - 3) & 1;
      *(LAS u32x4*)(wimg + (AQ_V - AQ_PK) + (Q::t0(1) + tl) * 8192 + cc * 256) =
          (u32x4){prev[tl][4 * cc], prev[tl][4 * cc + 1], prev[tl][4 * cc + 2], prev[tl][4 * cc + 3]};
    }
    if constexpr (E >= Q::KV0 && E < Q::KV0 + Q::NU * Q::UL) {
      constexpr int un = (E - Q::KV0) / Q::UL, ph = (E - Q::KV0) % Q::UL, it = un / 4, dj = un % 4;
      const int i = it ? 8 : wid;
      if constexpr (ph == 0) {
        if constexpr (un == 0) { l16 = lane_id() * 16; rimg = smem + AQ_PK + l16; }
        if constexpr (dj == 0) {
#pragma unroll
          for (int k = 0; k < 4; ++k) ka[k] = *(LAS bf16x8*)(rimg + (i * 4 + k) * 1024);
        } else {
          const int jv = i + dj - 2 < 0 ? 0 : i + dj - 2;
#pragma unroll
          for (int k = 0; k < 4; ++k) va[k] = *(LAS bf16x8*)(rimg + (AQ_V - AQ_PK) + (jv * 4 + k) * 1024);
        }
      }
      if constexpr (ph == Q::UL - 2) {
        const bf16x8 ones = {0x3f80, 0x3f80, 0x3f80, 0x3f80, 0x3f80, 0x3f80, 0x3f80, 0x3f80};
        dd = (f32x4){0, 0, 0, 0};
#pragma unroll
        for (int k = 0; k < 4; ++k) dd = MFMA(ka[k], dj ? va[k] : ones, dd);
        PIN(dd);
      }
      if constexpr (ph == Q::UL - 1) {
        ST16(ws, l16 + dj * 1024, (it && wid != 4) ? 0x7ff00000u : WS_KVP + (quad * 16 + p) * 36864 + i * 4096, __builtin_bit_cast(u32x4, dd));
        asm volatile("s_nop 1" ::: "memory");
      }
    }
    if constexpr (Q::wt(E)) {
      constexpr int RT = (Q::wt(E) - 1) / 3, I = (Q::wt(E) - 1) % 3;
      const int st = RT ? (p & 7) : 5 + (p & 3);
      TOUCH(rsw, voff[I], quad * 288 + st * 64 * 3456 + ((RT + 1) % 3) * 1152, smem + L_SCR);
    }
    if constexpr (EN < Q::NE && Q::stage(EN) == 26 && Q::pos(EN) == 1) { ald4(fpoll, rsws, f1o, 0); first_w(P, X, smem, wv); }
    if constexpr (E == 24) { f1o = f1_off(X); PIN(f1o); }
    if constexpr (EN < Q::NE && Q::stage(EN) == 25 && Q::pos(EN) == 1) { w_voff(wv, lane_id()); PIN(wv[0]); PIN(wv[1]); PIN(wv[2]); PIN(wv[3]); }
    SB0();
  });
  wait_v<4>(); PIN(fpoll);
  const u64 need = f1_need(0, X.p);
  if ((__builtin_amdgcn_ballot_w64(fpoll == X.tag) & need) != need) f1_spin(P, X, need);
  first_z(P, X, smem);
  lane = lane_id();
  const unsigned qo = ((lane >> 4) & 1) * 18432 + (lane >> 5) * 128 + (lane & 15) * 8;
#pragma unroll
  for (int t = 0; t < Q::nt(2); ++t)
#pragma unroll
    for (int a = 0; a < 4; ++a)
      if ((Q::t0(2) + t) * 8 + 2 * a < 36)
        ST8(ws, qo + ((Q::t0(2) + t) * 8 + 2 * a) * 128, WS_PQ + (p * 8 + rw * 2) * 18432 + quad * 4608,
            ((u32x2){cvt_pk(acc[t][4 * a], acc[t][4 * a + 1]), cvt_pk(acc[t][4 * a + 2], acc[t][4 * a + 3])}));
  wait_v<(G ? 6 : 12)>();
}

template <int ROLE>
DI void bc_loader(const Params& P, const Ctx& X, LAS char* smem) {
  constexpr int CNT = ROLE ? 16 : 18;
  __builtin_amdgcn_s_setprio(3);
  Loader<ROLE> L; L.init(P, X, smem);
  const int lane = lane_id(), p = X.p, c = X.c;
  constexpr int CB = 17;
  unsigned fl = 0; u64 have = 0;
  if (ROLE == 0) ald4(fl, L.rsws, f1_off(X), 0);
  BAR();
  L.issue_b(2);
  BAR(); L.issue_b(3);
#pragma unroll 1
  for (int n = 2; n < 30; ++n) {
    wait_v<CB>();
    if (ROLE == 0 && (n & 3) == 2) {
      PIN(fl);
      have |= __builtin_amdgcn_ballot_w64(fl == X.tag);
      const u64 need = f1_need((n + 2) >> 2, p);
      if ((have & need) != need) have = f1_spin(P, X, need);
    }
    BAR();
    L.issue_b(n + 2);
  }
  const unsigned f3 = ROLE == 0 ? WS_F3W + ((c & 7) * 2 + (lane >> 5)) * 128 + ((lane & 31) < 24 ? lane & 31 : 0) * 4
                                : WS_F3O + p * 128 + ((lane & 15) * 2 + (c >> 3)) * 4;
  ald4(fl, L.rsws, f3, 0);
  wait_v<CB + 1>(); BAR();
  wait_v<0>(); PIN(fl);
  const bool early = all_eq(fl, X.tag);
  if (early) L.template issue_c<2>(32);
  BAR();
  if (!early) { spin_flag(P.ws, f3, X.tag); L.template issue_c<2>(32); }
  L.template issue_c<4>(33);
  __builtin_amdgcn_s_sleep(16);
  L.template issue_c<4>(34);
  wait_v<2 * CNT>(); BAR(); L.template issue_c<4>(35);
  int n = 33;
  if (ROLE == 1) {
    const unsigned f4 = WS_F4 + (2 * p + (c >> 3)) * 128 + (lane & 31) * 4;
    wait_v<2 * CNT>(); BAR(); L.template issue_c<4>(36);
    wait_v<2 * CNT>(); BAR();
    ald4(fl, L.rsws, f4, 0);
    wait_v<CNT + 1>(); BAR();
    wait_v<0>(); PIN(fl);
    const bool e4 = all_eq(fl, X.tag);
    if (e4) { L.template issue_c<4>(37); }
    BAR();
    if (!e4) { spin_flag(P.ws, f4, X.tag); L.template issue_c<4>(37); }
    L.template issue_c<4>(38);
#pragma unroll 1
    for (n = 37; n < 53; ++n) { wait_v<CNT>(); BAR(); L.template issue_c<4>(n + 2); }
    wait_v<CNT>(); BAR(); wait_v<0>(); BAR();
    return;
  }
#pragma unroll 1
  for (; n < 52; ++n) { wait_v<2 * CNT>(); BAR(); L.template issue_c<4>(n + 3); }
  wait_v<2 * CNT>(); BAR(); wait_v<CNT>(); BAR(); wait_v<0>(); BAR();
}

DI void bc_side(const Params& P, const Ctx& X, LAS char* smem) {
  __builtin_amdgcn_s_setprio(2);
  const brs_t ws = __builtin_amdgcn_make_buffer_rsrc(P.ws, 0, WS_END, 0x00020000);
  const rsrc_t rs = MKRS(P.ws, WS_END);
  const int lane = lane_id();
  const int sw = X.wid - 6, lt = sw * 64 + lane, p = X.p, c = X.c, kq = lane >> 4, r16 = lane & 15;
  u32x4 part[3], pf[2][5]; u32x2 qv[4][2], qx; int dst[3]; unsigned src[2]; bf16x8 kvf[4][2];
  const unsigned oob = 0x7fffff00u;
  const unsigned f2k = WS_F2K + c * 128 + r16 * 4, f2z = WS_F2Z + p * 128 + (kq < 3 ? 3 * (c >> 2) + kq : 12 + (c >> 2)) * 4;
  unsigned fl = 0;
  int n = 0;
#define NEXT() do { SB0(); if (n < nb) BAR(); ++n; } while (0)
  int nb = 30;
  auto ready = [&](unsigned off, int limit) {
#pragma unroll 1
    while (!all_eq(fl, X.tag)) {
      if (n >= limit) { spin_flag(P.ws, off, X.tag); break; }
      ald4(fl, rs, off, 0); NEXT(); NEXT(); wait_v<0>(); PIN(fl);
    }
  };
  auto phi2 = [&](u32x2& v) {
    PIN(v);
    v = (u32x2){cvt_pk(phi_f(bf_lo(v.x)), phi_f(bf_hi(v.x))), cvt_pk(phi_f(bf_lo(v.y)), phi_f(bf_hi(v.y)))};
    PIN(v);
  };
  auto mk_src = [&](int i) {
    const int l = (lane & 3) + 4 * i, u0 = p * 12 + sw * 6 + (l < 6 ? l : 5), u = u0 < 190 ? u0 : 189;
    const int hh = u >= 95, r = u - hh * 95, gi = (r * 27) >> 9, e = r - gi * 19;
    const int kc = 36 * (c & 3) + 4 * gi + 16 * hh, vc = 36 * (c & 3) + 18 * hh + e;
    src[i] = (lane >> 2) * 36864 + (kc >> 4) * 4096 + (e == 18 ? 0 : ((vc >> 4) - (kc >> 4) + 2) * 1024 + (vc & 15) * 16) + ((kc >> 2) & 3) * 256;
    PIN(src[i]);
  };
  auto mk_dst = [&](int i) {
    const int id = lt + 128 * i, u = id >> 1, hh = u >= 95, r = u - hh * 95, gi = (r * 27) >> 9, e = r - gi * 19;
    const int k = 4 * gi + 16 * hh + 2 * (id & 1), nn = 18 * hh + e;
    const int kof = (k >> 5) * 1024 + ((k >> 3) & 3) * 256 + (k & 7) * 2;
    dst[i] = (u >= 190 || (unsigned)(k - 18 * hh) >= 18u) ? 8192 + lane * 4 : (e < 18 ? (nn >> 4) * 2048 + (nn & 15) * 16 : 6144 + hh * 16) + kof;
    PIN(dst[i]);
  };
  auto pack_st = [&](auto I_) {
    constexpr int r = decltype(I_)::value / 5, dt = decltype(I_)::value % 5;
    const int kb = ((X.pa & 1) * 12 + X.quad * 3 + X.jj) * 4 + sw * 2 + r;
    const unsigned so = (X.jj < 3 && kb < 90) ? WS_WX + ((X.pa >> 1) * 90 + kb) * 4608 : 0x7ff00000u;
    if constexpr (dt < 4) AST16<dt * 1024>(rs, lane * 16, so, pf[r][dt]);
    else AST16<0>(rs, r16 < 8 ? 4096 + kq * 128 + r16 * 16 : 0x7ff00000u, so, pf[r][4]);
  };
  auto q_ld = [&](int tt) {
    const unsigned so = WS_PQ + (p * 8 + sw * 4 + tt) * 18432 + c * 1152;
    ald8(qv[tt][0], rs, kq * 256 + r16 * 8, so); ald8(qv[tt][1], rs, kq * 256 + 128 + r16 * 8, so);
  };
  auto kvf_ld = [&]() {
#pragma unroll
    for (int i = 0; i < 3; ++i) ald16(part[i], rs, lt * 16, WS_KVF + c * 6144 + i * 2048);
  };
  NEXT();
  ald4(fl, rs, f2k, 0); mk_src(0); mk_src(1); NEXT();
  if (sw == 1) warm_code(X, smem);
  mk_dst(0); mk_dst(1); NEXT();
  mk_dst(2); NEXT();
  wait_v<0>(); PIN(fl);
  NEXT();
  ST4(ws, lt == 0 ? 0u : oob, WS_F2Z + X.pa * 128 + (X.jj < 3 ? X.quad * 3 + X.jj : 12 + X.quad) * 4, X.tag);
#pragma unroll
  for (int r = 0; r < 2; ++r) {
    LAS char* a = smem + B_KV + (sw * 2 + r) * 5120 + (8 * kq + ((lane >> 2) & 3)) * 144;
#pragma unroll
    for (int dt = 0; dt < 5; ++dt) {
      LAS char* b = dt < 4 ? a + (dt >> 1) * 64 + (lane & 3) * 16 + (dt & 1) * 8 : a + 128 + (lane & 1) * 8;
      pf[r][dt] = __builtin_bit_cast(u32x4, tr_frag(b, b + 576));
    }
  }
  WAIT_L0(); NEXT();
#pragma unroll
  for (int i = 0; i < 4; ++i) *(LAS u32x4*)(smem + B_KV + i * 2048 + lt * 16) = (u32x4){0, 0, 0, 0};
  *(LAS unsigned*)(smem + B_KV + 8448) = 0u;
  WAIT_L0();
  ready(f2k, 9);
#pragma unroll
  for (int i = 0; i < 2; ++i) ald16(part[i], rs, src[i], WS_KVP + (c >> 2) * 16 * 36864);
  NEXT();
  sfor<0, 2>(pack_st); NEXT();
  ald4(fl, rs, f2z, 0); sfor<2, 4>(pack_st); NEXT();
  {
    wait_v<5>(); PIN(part[0]); PIN(part[1]);
    float mine = 0.f;
#pragma unroll
    for (int i = 0; i < 8; ++i) {
      if (i == 4) { PIN(mine); NEXT(); wait_v<2>(); PIN(fl); }
      const unsigned u = part[i >> 2][i & 3];
      float v = __uint_as_float(u);
      v += dpp_get<0x124>(v); v += dpp_get<0x128>(v);
      auto a = __builtin_amdgcn_permlane16_swap(__float_as_uint(v), __float_as_uint(v), false, false);
      v = __uint_as_float(a[0]) + __uint_as_float(a[1]);
      auto b = __builtin_amdgcn_permlane32_swap(__float_as_uint(v), __float_as_uint(v), false, false);
      v = __uint_as_float(b[0]) + __uint_as_float(b[1]);
      mine = (lane >> 2) == i ? v : mine;
    }
    const int l = (lane & 3) + 4 * kq;
    ST8(ws, l < 6 ? ((p * 12 + sw * 6 + l) * 4 + ((lane >> 2) & 3)) * 8 : oob, WS_KVF + c * 6144, ((u32x2){__float_as_uint(mine), X.tag}));
  }
  NEXT();
  ready(f2z, 17);
  ald8(qx, rs, kq * 18432 + 1024 + r16 * 8, WS_PQ + (p * 8 + sw * 4) * 18432 + c * 1152); q_ld(0); NEXT();
  q_ld(1); sfor<4, 5>(pack_st); NEXT();
  q_ld(2); sfor<5, 6>(pack_st); NEXT();
  q_ld(3); wait_v<8>(); phi2(qx); phi2(qv[0][0]); NEXT();
  kvf_ld(); phi2(qv[0][1]); NEXT();
  sfor<6, 8>(pack_st); wait_v<10>(); phi2(qv[1][0]); phi2(qv[1][1]); NEXT();
  sfor<8, 10>(pack_st); wait_v<9>(); phi2(qv[2][0]); phi2(qv[2][1]); NEXT();
  wait_v<4>();
#pragma unroll 1
  for (unsigned spins = 0; spins < SPIN_MAX; ++spins) {
    bool ok = true;
#pragma unroll
    for (int i = 0; i < 3; ++i) { PIN(part[i]); ok &= part[i].y == X.tag && part[i].w == X.tag; }
    if (__builtin_amdgcn_ballot_w64(!ok) == 0) break;
    kvf_ld(); NEXT(); NEXT(); wait_v<0>();
  }
#pragma unroll
  for (int i = 0; i < 3; ++i) *(LAS unsigned*)(smem + B_KV + dst[i]) = cvt_pk(__uint_as_float(part[i].x), __uint_as_float(part[i].z));
  asm volatile("" ::: "memory");
  if (lane == 0) __hip_atomic_fetch_add((LAS unsigned*)(smem + B_KV + 8448), 1u, __ATOMIC_RELAXED, __HIP_MEMORY_SCOPE_WORKGROUP);
#pragma unroll 1
  for (unsigned spins = 0; spins < 4 * SPIN_MAX; ++spins) {
    WAIT_L0(); NEXT();
    if (__builtin_amdgcn_readfirstlane(*(volatile LAS unsigned*)(smem + B_KV + 8448)) >= 2u) break;
  }
#pragma unroll
  for (int nt = 0; nt < 4; ++nt) {
    LAS char* kb = smem + B_KV + nt * 2048 + (nt < 3 ? r16 : r16 & 1) * 16;
    kvf[nt][0] = *(LAS bf16x8*)(kb + kq * 256); kvf[nt][1] = *(LAS bf16x8*)(kb + 1024);
  }
  phi2(qv[3][0]);
#pragma unroll
  for (int tt = 0; tt < 4; ++tt) {
    NEXT();
    if (tt == 3) { phi2(qv[3][1]); if (sw == 1) warm_code(X, smem); NEXT(); }
    const bf16x8 q0 = __builtin_bit_cast(bf16x8, ((u32x4){qv[tt][0].x, qv[tt][0].y, qv[tt][1].x, qv[tt][1].y}));
    const bf16x8 q1 = __builtin_bit_cast(bf16x8, ((u32x4){kq == tt ? qx.x : 0u, kq == tt ? qx.y : 0u, 0u, 0u}));
    const f32x4 z = {0, 0, 0, 0};
    const f32x4 den = MFMA(kvf[3][1], q1, MFMA(kvf[3][0], q0, z));
    f32x4 num[3];
#pragma unroll
    for (int nt = 0; nt < 3; ++nt) num[nt] = MFMA(kvf[nt][1], q1, MFMA(kvf[nt][0], q0, z));
    const float r0 = __builtin_amdgcn_rcpf(den[0] + 1e-6f), r1 = __builtin_amdgcn_rcpf(den[1] + 1e-6f), rm = kq == 0 ? r0 : r1;
    const unsigned so = WS_HX + (p * 8 + sw * 4 + tt) * (90 * 1024);
    ST16(ws, lane * 16, so + (72 + c) * 1024, ((u32x4){cvt_pk(num[0][0] * r0, num[0][1] * r0), cvt_pk(num[0][2] * r0, num[0][3] * r0),
                                                       cvt_pk(num[1][0] * rm, num[1][1] * rm), cvt_pk(num[1][2] * r1, num[1][3] * r1)}));
    ST8(ws, kq == 0 ? r16 * 8 : oob, so + 88 * 1024 + c * 128,
        ((u32x2){cvt_pk(num[2][0] * r1, num[2][1] * r1), cvt_pk(num[2][2] * r1, num[2][3] * r1)}));
  }
  NEXT(); NEXT(); NEXT();
  wait_v<0>();
  if (lane < 2) {
    if (lane == 0) flag_st(P.ws, WS_F3O + p * 128 + (c * 2 + sw) * 4, X.tag);
    else if (X.jj < 3) flag_st(P.ws, WS_F3W + X.pa * 128 + ((X.quad * 3 + X.jj) * 2 + sw) * 4, X.tag);
  }
  nb = 1 << 30;
  if (X.bid == 0 && sw == 0) {
#pragma unroll 1
    for (int i = 0; i < 4; ++i) {
      const unsigned off = WS_F3O + (i * 4 + kq) * 128 + r16 * 8;
      fl = ~X.tag; ready(off, 50);
    }
    if (lane == 0) flag_st(P.ws, WS_EPOCH, X.tag);
  }
  NEXT(); NEXT(); NEXT();
#undef NEXT
}

template <int DH>
DI void c_compute(const Params& P, const Ctx& X, LAS char* smem) {
  constexpr int ND = DH ? 2 : 3;
  int lane = lane_id();
  const int r16 = lane & 15, kq = lane >> 4, th = X.wid & 1;
  const int rg = 2 * X.p + (X.c >> 3), cg = X.c & 7;
  f32x4 acc[ND][2];
#pragma unroll
  for (int a = 0; a < ND; ++a) { acc[a][0] = (f32x4){0, 0, 0, 0}; acc[a][1] = (f32x4){0, 0, 0, 0}; }
  const int woff = DH * 2048 + lane * 16, woff4 = 4096 + kq * 128 + (r16 & 7) * 16;
  const int hoff = 18432 + th * 8192 + lane * 16;
  bf16x8 wa[2][ND], ha[2][2], wb[2][ND], hb[2][2];
#define C_RD(W, H, slot, kp) do { LAS char* b_ = smem + SLOT(slot); \
    _Pragma("unroll") for (int k = 0; k < 2; ++k) { \
      H[k][0] = *(LAS bf16x8*)(b_ + hoff + ((kp) * 2 + k) * 1024); H[k][1] = *(LAS bf16x8*)(b_ + hoff + 4096 + ((kp) * 2 + k) * 1024); \
      W[k][0] = *(LAS bf16x8*)(b_ + woff + ((kp) * 2 + k) * 4608); W[k][1] = *(LAS bf16x8*)(b_ + woff + 1024 + ((kp) * 2 + k) * 4608); \
      if (ND == 3) W[k][ND - 1] = *(LAS bf16x8*)(b_ + woff4 + ((kp) * 2 + k) * 4608); } } while (0)
#define C_MM(W, H) do { _Pragma("unroll") for (int k = 0; k < 2; ++k) _Pragma("unroll") for (int i = 0; i < ND; ++i) { \
    acc[i][0] = MFMA(W[k][i], H[k][0], acc[i][0]); acc[i][1] = MFMA(W[k][i], H[k][1], acc[i][1]); } } while (0)
#define C_ILV() do { _Pragma("unroll") for (int i_ = 0; i_ < ND + 2; ++i_) { SGB(0x100, 2); SGB(0x8, 1); } SGB(0x8, 3 * ND - 2); } while (0)
#define C_STAGE(n) do { WAIT_L0(); BAR(); const int slot = (n) & 3; \
    C_RD(wa, ha, slot, 0); C_MM(wb, hb); C_ILV(); SB0(); \
    C_RD(wb, hb, slot, 1); C_MM(wa, ha); C_ILV(); SB0(); } while (0)
  BAR();
  C_RD(wb, hb, 0, 0);
#pragma unroll
  for (int k = 0; k < 2; ++k)
#pragma unroll
    for (int t = 0; t < 2; ++t) {
      LAS char* a = smem + SLOT(0) + 18432 + th * 8192 + t * 4096 + k * 1024 + kq * 256 + r16 * 8;
      const u32x2 lo = *(LAS u32x2*)a, hi = *(LAS u32x2*)(a + 128);
      hb[k][t] = __builtin_bit_cast(bf16x8, ((u32x4){lo.x, lo.y, hi.x, hi.y}));
    }
  wait_v<0>();
  if (lane == 0) flag_st(P.ws, WS_F4 + (X.p * 2 + (X.wid >> 1)) * 128 + (X.c * 2 + (X.wid & 1)) * 4, X.tag);
  C_STAGE(33);
  u32x4 xr[2]; u32x2 xo[2]; size_t orow[2];
#pragma unroll
  for (int tt = 0; tt < 2; ++tt) {
    orow[tt] = (size_t)(rg * 64 + slot_row(th * 32 + tt * 16 + r16)) * 1152 + cg * 144;
    xr[tt] = *(const u32x4*)((const char*)P.x + orow[tt] + DH * 64 + kq * 16);
    if (DH == 0) xo[tt] = *(const u32x2*)((const char*)P.x + orow[tt] + 128 + (kq & 1) * 8);
  }
  C_STAGE(34);
#pragma unroll 1
  for (int n = 35; n < 55; n += 4) { C_STAGE(3); C_STAGE(0); C_STAGE(1); C_STAGE(2); }
  C_MM(wb, hb);
  SB0();
  const brs_t ro = __builtin_amdgcn_make_buffer_rsrc(P.out, 0, 2048 * 1152, 0x00020000);
#pragma unroll
  for (int tt = 0; tt < 2; ++tt) {
    const u32x4 x = xr[tt]; const f32x4 a0 = acc[0][tt], a1 = acc[1][tt];
    u32x4 v;
    v.x = cvt_pk(bf_lo(x.x) + a0[0], bf_hi(x.x) + a0[1]); v.y = cvt_pk(bf_lo(x.y) + a0[2], bf_hi(x.y) + a0[3]);
    v.z = cvt_pk(bf_lo(x.z) + a1[0], bf_hi(x.z) + a1[1]); v.w = cvt_pk(bf_lo(x.w) + a1[2], bf_hi(x.w) + a1[3]);
    __builtin_amdgcn_raw_buffer_store_b128(v, ro, (int)(orow[tt] + DH * 64 + kq * 16), 0, 0);
    if (DH == 0) {
      const f32x4 a2 = acc[ND - 1][tt];
      u32x2 w; w.x = cvt_pk(bf_lo(xo[tt].x) + a2[0], bf_hi(xo[tt].x) + a2[1]); w.y = cvt_pk(bf_lo(xo[tt].y) + a2[2], bf_hi(xo[tt].y) + a2[3]);
      __builtin_amdgcn_raw_buffer_store_b64(w, ro, kq < 2 ? (int)(orow[tt] + 128 + kq * 8) : 0x7fffff00, 0, 0);
    }
  }
}

DI void b_compute(const Params& P, const Ctx& X, LAS char* smem) {
  int lane = lane_id();
  const int wid = X.wid, p = X.p, c = X.c, r16 = lane & 15, kq = lane >> 4;
  const brs_t ws = __builtin_amdgcn_make_buffer_rsrc(P.ws, 0, WS_END, 0x00020000);
  f32x4 acc[9][2];
#pragma unroll
  for (int a = 0; a < 9; ++a) { acc[a][0] = (f32x4){0, 0, 0, 0}; acc[a][1] = (f32x4){0, 0, 0, 0}; }
  const int zoff = lane * 16;
  const int woff0 = 18432 + (wid * 32 + r16) * 128 + ((kq ^ (r16 >> 1)) * 16);
  const int woff1 = 18432 + (wid * 32 + r16) * 128 + (((kq + 4) ^ (r16 >> 1)) * 16);
  bf16x8 za[9], wa[2], zb[9], wb[2];
#define B_RD(Z, W, slot, kb) do { LAS char* b_ = smem + SLOT(slot); \
    W[0] = *(LAS bf16x8*)(b_ + ((kb) ? woff1 : woff0)); W[1] = *(LAS bf16x8*)(b_ + ((kb) ? woff1 : woff0) + 2048); \
    _Pragma("unroll") for (int i = 0; i < 9; ++i) Z[i] = *(LAS bf16x8*)(b_ + zoff + (i < 8 ? (kb) * 8192 + i * 1024 : 16384 + (kb) * 1024)); } while (0)
#define B_MM(Z, W) do { _Pragma("unroll") for (int i = 0; i < 9; ++i) { \
    acc[i][i & 1] = MFMA(Z[i], W[i & 1], acc[i][i & 1]); acc[i][(i & 1) ^ 1] = MFMA(Z[i], W[(i & 1) ^ 1], acc[i][(i & 1) ^ 1]); } } while (0)
#define B_ILV() do { _Pragma("unroll") for (int i_ = 0; i_ < 11; ++i_) { SGB(0x8, 1); SGB(0x100, 1); } SGB(0x8, 7); } while (0)
#define B_STAGE(n) do { WAIT_L0(); BAR(); const int slot = (n) & 3; \
    B_RD(za, wa, slot, 0); B_MM(zb, wb); B_ILV(); SB0(); \
    B_RD(zb, wb, slot, 1); B_MM(za, wa); B_ILV(); SB0(); } while (0)
  BAR();
  B_RD(za, wa, 0, 0); SB0();
  B_RD(zb, wb, 0, 1); B_MM(za, wa); B_ILV(); SB0();
  u32x4 z1[2][4]; u32x2 z1o[2]; unsigned bsv[2];
  const rsrc_t rs = MKRS(P.ws, WS_END), rb = MKRS(P.bsp, 4096);
#pragma unroll
  for (int tt = 0; tt < 2; ++tt)
#pragma unroll
    for (int m = 0; m < 4; ++m) z1[tt][m] = *(LAS u32x4*)(smem + B_Z1 + (m < 2 ? 16384 : 0) + ((wid * 2 + tt) * 2 + (m & 1)) * 1024 + zoff);
#pragma unroll
  for (int tt = 0; tt < 2; ++tt) ald2(bsv[tt], rb, (p * 128 + slot_row(wid * 32 + tt * 16 + r16)) * 2);
  B_STAGE(1); B_STAGE(2); B_STAGE(3);
  wait_v<0>();
  PIN(bsv[0]); PIN(bsv[1]);
  float bias[2] = {bf_lo(bsv[0]) * (1.0f / GC), bf_lo(bsv[1]) * (1.0f / GC)};
  PIN(bias[0]); PIN(bias[1]);
  const int nq = X.jj == 3 ? 20 : 64;
#pragma unroll 1
  for (int n = 4; n < 32; n += 4) {
    if (__builtin_expect(n >= 20, 0)) {
      const unsigned so = WS_Z1 + (p * 8 + wid * 2) * (72 * 1024);
      if (n == nq) {
#pragma unroll
        for (int m = 0; m < 4; ++m) ald16(z1[0][m], rs, zoff, so + c * 4096 + m * 1024);
      }
      asm volatile("" : "+s"(n));
      if (n == nq + 4) {
#pragma unroll
        for (int m = 0; m < 4; ++m) ald16(z1[1][m], rs, zoff, so + 72 * 1024 + c * 4096 + m * 1024);
      }
      asm volatile("" : "+s"(n));
      if (n == 20) {
#pragma unroll
        for (int a = 0; a < 3; ++a) { acc[a][0] += bias[0]; acc[a][1] += bias[1]; }
      }
      asm volatile("" : "+s"(n));
      if (n == 24) {
#pragma unroll
        for (int a = 3; a < 6; ++a) { acc[a][0] += bias[0]; acc[a][1] += bias[1]; }
      }
      asm volatile("" : "+s"(n));
      if (n == 28) {
#pragma unroll
        for (int a = 6; a < 9; ++a) { acc[a][0] += bias[0]; acc[a][1] += bias[1]; }
#pragma unroll
        for (int tt = 0; tt < 2; ++tt) {
          ald8(z1o[tt], rs, zoff, so + tt * (72 * 1024) + (64 + (c >> 1)) * 1024 + (c & 1) * 8);
        }
      }
    }
    B_STAGE(0); B_STAGE(1); B_STAGE(2); B_STAGE(3);
  }
  wait_v<0>();
#pragma unroll
  for (int tt = 0; tt < 2; ++tt) { PIN(z1[tt][0]); PIN(z1[tt][1]); PIN(z1[tt][2]); PIN(z1[tt][3]); PIN(z1o[tt]); }
  __builtin_amdgcn_s_setprio(3);
  const unsigned so = WS_HX + (p * 8 + wid * 2) * (90 * 1024);
  auto mm = [&](int i) { acc[i][0] = MFMA(zb[i], wb[0], acc[i][0]); acc[i][1] = MFMA(zb[i], wb[1], acc[i][1]); };
  mm(0); mm(1);
  sfor<0, 4>([&](auto M_) {
    constexpr int m = decltype(M_)::value;
    mm(2 * m + 2); if (m < 3) mm(2 * m + 3);
    SB0();
#pragma unroll
    for (int tt = 0; tt < 2; ++tt) {
      const u32x4 z = z1[tt][m]; const f32x4 g0 = acc[2 * m][tt], g1 = acc[2 * m + 1][tt];
      u32x4 v;
      v.x = cvt_pk(bf_lo(z.x) * g0[0], bf_hi(z.x) * g0[1]); v.y = cvt_pk(bf_lo(z.y) * g0[2], bf_hi(z.y) * g0[3]);
      v.z = cvt_pk(bf_lo(z.z) * g1[0], bf_hi(z.z) * g1[1]); v.w = cvt_pk(bf_lo(z.w) * g1[2], bf_hi(z.w) * g1[3]);
      AST16<m * 1024>(rs, zoff, so + tt * (90 * 1024) + c * 4096, v);
    }
    SB0();
  });
#pragma unroll
  for (int tt = 0; tt < 2; ++tt) {
    const u32x2 z = z1o[tt]; const f32x4 g0 = acc[8][tt];
    u32x2 v; v.x = cvt_pk(bf_lo(z.x) * g0[0], bf_hi(z.x) * g0[1]); v.y = cvt_pk(bf_lo(z.y) * g0[2], bf_hi(z.y) * g0[3]);
    ST8(ws, (kq >> 1) * 256 + r16 * 16 + (kq & 1) * 8, so + tt * (90 * 1024) + (64 + (c >> 1)) * 1024 + (c & 1) * 512, v);
  }
  __builtin_amdgcn_s_setprio(0);
}

template <int TY>
DI void a_prologue(const Params& P, Ctx& X, LAS char* smem, bf16x8 (&xf)[36], LAS char* (&xb)[4]) {
  const int wid = X.wid, lane = lane_id(), idx = X.bid >> 3;
  const int sl0 = ((wid + 2 * X.jj) & 7) * 16 + (lane >> 3);
  u32x4 xr[2][9];
#pragma unroll
  for (int hh = 0; hh < 2; ++hh) {
#pragma unroll
    for (int ln = 0; ln < 9; ++ln)
      xr[hh][ln] = *(const u32x4*)((const char*)P.x + (size_t)(X.pa * 128 + slot_row(sl0 + hh * 8)) * 1152 + (lane & 7) * 16 + ln * 128);
    SB0();
  }
  X.tag = *(const __attribute__((address_space(4))) unsigned*)(P.ws + WS_EPOCH);
  if (wid == 0) warm_code(X, smem);
  SB0();
  auto first = [&](int S) {
    if (TY == 0) {
      const rsrc_t rsw = MKRS(P.win, 576 * 9216);
      const unsigned voff = z_voff(wid, lane);
#pragma unroll
      for (int i = 0; i < 3; ++i)
        BLDS(rsw, voff, (2304 + (X.quad * 3 + X.jj) * 192) * 2 + (S * 3 + i) * 64 * 9216, smem + (S * 3 + i) * 8192 + wid * 1024);
    } else q_first<TY == 2>(P, X, smem, S);
  };
  unsigned rlo[2], rhi[2]; float sh_[2];
  LAS char* xw[2];
#pragma unroll
  for (int hh = 0; hh < 2; ++hh) {
    const bf16x2_t one = {(__bf16)1.0f, (__bf16)1.0f};
    float s[2] = {0.f, 0.f}, q[2] = {0.f, 0.f};
#pragma unroll
    for (int ln = 0; ln < 9; ++ln)
#pragma unroll
      for (int i = 0; i < 4; ++i) {
        const unsigned ua = xr[hh][ln][i];
        const bf16x2_t a = __builtin_bit_cast(bf16x2_t, ua);
        s[i & 1] = __builtin_amdgcn_fdot2_f32_bf16(a, one, s[i & 1], false); q[i & 1] = __builtin_amdgcn_fdot2_f32_bf16(a, a, q[i & 1], false);
      }
    PIN(s[0]); PIN(s[1]);
    if (hh == 1) first(0);
    const float m = oct_sum(s[0] + s[1]) * (1.0f / 576.0f);
    const float var = __builtin_fmaxf(oct_sum(q[0] + q[1]) * (1.0f / 576.0f) - m * m, 0.f);
    unsigned r = cvt_pk(__builtin_amdgcn_rsqf(var + 1e-5f) * (TY == 0 ? GC : 1.0f), 0.f);
    sh_[hh] = -m * bf_lo(r);
    rlo[hh] = r; rhi[hh] = r << 16;
    const int sl = sl0 + hh * 8;
    xw[hh] = smem + A_XN + sl * 128 + (((lane & 7) ^ ((sl >> 1) & 7)) * 16);
    SB0();
#pragma unroll
    for (int ln = 0; ln < 9; ++ln) {
      if (ln < 6) *(LAS u32x4*)(xw[hh] + XLN(ln)) = norm4(xr[hh][ln], rlo[hh], rhi[hh], sh_[hh]);
      else if (hh == 0) { xr[0][ln] = norm4(xr[0][ln], rlo[0], rhi[0], sh_[0]); PIN(xr[0][ln]); }
      SB0();
    }
  }
#pragma unroll
  for (int i = 0; i < 4; ++i) xb[i] = smem + A_XN + ((wid & 3) * 32 + (lane & 31)) * 128 + (((2 * i + (lane >> 5)) ^ ((lane >> 1) & 7)) * 16);
  WAIT_L0(); BAR();
#pragma unroll
  for (int ks = 0; ks < 24; ++ks) xf[ks] = XRD(ks);
  SB0();
#pragma unroll
  for (int ln = 6; ln < 9; ++ln) { xr[1][ln] = norm4(xr[1][ln], rlo[1], rhi[1], sh_[1]); PIN(xr[1][ln]); }
  first(1);
  wait_v<(TY == 2 ? 2 : 3)>();
  asm volatile("" : "+s"(X.tag));
  X.tag += 1u;
  WAIT_L0(); BAR();
#pragma unroll
  for (int ln = 6; ln < 9; ++ln)
#pragma unroll
    for (int hh = 0; hh < 2; ++hh) *(LAS u32x4*)(xw[hh] + XLN(ln)) = xr[hh][ln];
  SB0();
}

__global__ NOPK __launch_bounds__(512, 2) void kF(const Params P) {
  extern __shared__ __attribute__((aligned(16))) char smem_[];
  LAS char* smem = (LAS char*)smem_;
  Ctx X;
  X.wid = __builtin_amdgcn_readfirstlane(threadIdx.x >> 6); X.bid = blockIdx.x;
  const int xcd = X.bid & 7, idx = X.bid >> 3, wid = X.wid, lane = threadIdx.x & 63;
  X.pa = (xcd >> 2) * 8 + (idx >> 2); X.quad = xcd & 3; X.jj = idx & 3;
  X.p = X.pa; X.c = X.quad * 4 + X.jj;
  if (__builtin_expect(X.jj < 3, 1)) { bf16x8 xf[36]; LAS char* xb[4]; a_prologue<0>(P, X, smem, xf, xb); a_zblock(P, X, smem, xf, xb); }
  else if (wid < 4) { bf16x8 xf[36]; LAS char* xb[4]; a_prologue<1>(P, X, smem, xf, xb); a_qkvblock<0>(P, X, smem, xf, xb); }
  else { bf16x8 xf[36]; LAS char* xb[4]; a_prologue<2>(P, X, smem, xf, xb); a_qkvblock<1>(P, X, smem, xf, xb); }
  if (wid == 4) { bc_loader<0>(P, X, smem); __builtin_amdgcn_endpgm(); }
  if (wid == 5) { bc_loader<1>(P, X, smem); __builtin_amdgcn_endpgm(); }
  if (wid >= 6) { bc_side(P, X, smem); __builtin_amdgcn_endpgm(); }
  b_compute(P, X, smem);
  if (wid < 2) { c_compute<0>(P, X, smem); __builtin_amdgcn_endpgm(); }
  c_compute<1>(P, X, smem);
}

extern "C" __device__ __attribute__((used, noinline)) void kf_code_end() {}

extern "C" void kernel_launch(void* const* d_in, const int* in_sizes, int n_in, void* d_out, int out_size, void* d_ws,
                              size_t ws_size, hipStream_t stream) {
  Params P;
  P.x = (const u16*)d_in[0]; P.wqkv = (const u16*)d_in[1]; P.wo = (const u16*)d_in[2]; P.win = (const u16*)d_in[4];
  P.wsp = (const u16*)d_in[5]; P.bsp = (const u16*)d_in[6]; P.wout = (const u16*)d_in[7];
  P.out = (u16*)d_out; P.ws = (char*)d_ws;
  (void)hipFuncSetAttribute((const void*)kF, hipFuncAttributeMaxDynamicSharedMemorySize, LDS_BYTES);
  kF<<<256, 512, LDS_BYTES, stream>>>(P);
}
```
